```python
import torch

def _softmax(x, axis=-1):
    return torch.softmax(x, dim=axis)

def _gelu(x):
    return torch.nn.functional.gelu(x, approximate='tanh')

def _silu(x):
    return torch.nn.functional.silu(x)

def _sigmoid(x):
    return torch.sigmoid(x)

def _layernorm(x, axis=-1, eps=1e-05):
    m = x.mean(dim=axis, keepdim=True)
    v = x.var(dim=axis, keepdim=True, unbiased=False)
    return (x - m) / torch.sqrt(v + eps)

def _scatter_add(tgt, idx, src, axis=0):
    return tgt.clone().index_add_(axis, idx, src)

def _segment_sum(src, seg, n):
    out = torch.zeros((n,) + src.shape[1:], dtype=src.dtype)
    return out.index_add_(0, seg, src)

def _pad(x, pw):
    flat = tuple((v for pair in reversed(pw) for v in pair))
    return torch.nn.functional.pad(x, flat)
kernel_name = 'sequential_preLN__sliding_local_dilated__mixer_gmlp__prompt'

def setup_inputs():

    _w = lambda a: (lambda t: t.to(torch.bfloat16) if t.is_floating_point() else t)(torch.as_tensor(a))

    def _A_setup():
        B, S, D = (1, 2048, 1024)
        return {'x': _w((lambda *s: torch.randn(*s, dtype=torch.bfloat16) * 0.05)(B, S, D)), 'wqkv': _w((lambda *s: torch.randn(*s, dtype=torch.bfloat16) * 0.05)(D, 3 * D)), 'wo': _w((lambda *s: torch.randn(*s, dtype=torch.bfloat16) * 0.05)(D, D))}
    _A_in = _A_setup()

    def _B_setup():
        B, S, D, F = (1, 2048, 1024, 3584)
        Fh = F // 2
        return {'x': _w((lambda *s: torch.randn(*s, dtype=torch.bfloat16) * 0.05)(B, S, D)), 'w_in': _w((lambda *s: torch.randn(*s, dtype=torch.bfloat16) * 0.4)(D, 2 * Fh)), 'w_sp': _w(0.01 * (lambda *s: torch.randn(*s, dtype=torch.bfloat16) * 0.4)(S, S)), 'b_sp': _w(1.0 + 0.01 * (lambda *s: torch.randn(*s, dtype=torch.bfloat16) * 0.05)(S)), 'w_out': _w((lambda *s: torch.randn(*s, dtype=torch.bfloat16) * 0.05)(Fh, D))}
    _B_in = _B_setup()
    return {'A_x': _A_in['x'], 'A_wqkv': _A_in['wqkv'], 'A_wo': _A_in['wo'], 'B_x': _B_in['x'], 'B_w_in': _B_in['w_in'], 'B_w_sp': _B_in['w_sp'], 'B_b_sp': _B_in['b_sp'], 'B_w_out': _B_in['w_out']}

def reference(A_x, A_wqkv, A_wo, B_x, B_w_in, B_w_sp, B_b_sp, B_w_out):

    def _A_ref(x, wqkv, wo):
        B, S, D = x.shape
        H = 32
        Dh = D // H
        W, dil = (8, 2)
        qkv = (lambda x, *d: x.permute(*d))((x @ wqkv).reshape(B, S, 3, H, Dh), 2, 0, 3, 1, 4)
        q, k, v = (qkv[0], qkv[1], qkv[2])
        scale = Dh ** (-0.5)
        pos = torch.arange(S)
        off = torch.arange(W) * dil
        idx = pos[:, None] - off[None, :]
        mask = idx >= 0
        idx_c = torch.where(mask, idx, 0).reshape(-1)
        kg = k[:, :, idx_c, :].reshape(B, H, S, W, Dh)
        vg = v[:, :, idx_c, :].reshape(B, H, S, W, Dh)
        logits = torch.einsum('bhsd,bhswd->bhsw', q, kg) * scale
        logits = torch.where(mask[None, None, :, :], logits, -1000000000.0)
        attn = _softmax(logits, axis=-1)
        o = torch.einsum('bhsw,bhswd->bhsd', attn, vg)
        return (lambda x, *d: x.permute(*d))(o, 0, 2, 1, 3).reshape(B, S, D) @ wo

    def _B_ref(x, w_in, w_sp, b_sp, w_out):
        B, S, D = x.shape
        F = w_in.shape[-1]
        Fh = F // 2
        z = _gelu(torch.einsum('bsd,df->bsf', x, w_in))
        z1 = z[..., :Fh].clone()
        z2 = _layernorm(z[..., Fh:], axis=-1)
        g = torch.einsum('ts,bsf->btf', w_sp, z2) + b_sp[None, :, None]
        return torch.einsum('bsf,fd->bsd', z1 * g, w_out)
    x = A_x
    h = x + _A_ref(_layernorm(x, axis=-1), A_wqkv, A_wo)
    h2 = h + _B_ref(_layernorm(h, axis=-1), B_w_in, B_w_sp, B_b_sp, B_w_out)
    return h2
```

```cpp
#include <hip/hip_runtime.h>
#include <hip/hip_bf16.h>
#include <stdint.h>
#include <type_traits>

#define DI __device__ __forceinline__
#define LAS __attribute__((address_space(3)))
typedef __bf16 bf16x8 __attribute__((ext_vector_type(8)));
typedef __bf16 bf16x2 __attribute__((ext_vector_type(2)));
typedef short s16x4 __attribute__((ext_vector_type(4)));
typedef float f32x4 __attribute__((ext_vector_type(4)));
typedef float f32x2 __attribute__((ext_vector_type(2)));
typedef int i32x4 __attribute__((ext_vector_type(4)));
typedef unsigned u32x4 __attribute__((ext_vector_type(4)));
typedef unsigned u32x2 __attribute__((ext_vector_type(2)));
typedef unsigned long long u64;
typedef __attribute__((address_space(1))) u64 gu64;

constexpr int SEQ = 2048, DM = 1024, FF = 3584, FH = 1792;
constexpr float LN_EPS = 1e-5f;

constexpr size_t WS_FLAGS = 0;
constexpr size_t WS_XBUF = 8192;
constexpr size_t WS_O = 1u << 20;
constexpr size_t WS_H = WS_O + (4u << 20);
constexpr size_t WS_Z1 = WS_H + (4u << 20);
constexpr size_t WS_Z2 = WS_Z1 + (8u << 20);
constexpr size_t WS_Y = WS_Z2 + (8u << 20);

DI i32x4 make_srd(const void* p, unsigned bytes) {
  u64 a = (u64)p;
  i32x4 r;
  r[0] = __builtin_amdgcn_readfirstlane((int)(unsigned)a);
  r[1] = __builtin_amdgcn_readfirstlane((int)(unsigned)(a >> 32));
  r[2] = (int)bytes;
  r[3] = 0x00020000;
  return r;
}
DI void dma16(i32x4 srd, unsigned voff, unsigned soff, unsigned ldsaddr) {
  asm volatile("s_mov_b32 m0, %0\n\ts_nop 0\n\tbuffer_load_dwordx4 %1, %2, %3 offen lds"
               :: "s"(ldsaddr), "v"(voff), "s"(srd), "s"(soff) : "memory");
}
template <int N> DI void wait_vm() { asm volatile("s_waitcnt vmcnt(%0)" :: "i"(N) : "memory"); }
DI void wait_lgkm0() { asm volatile("s_waitcnt lgkmcnt(0)" ::: "memory"); }
DI void bar() { asm volatile("" ::: "memory"); __builtin_amdgcn_s_barrier(); asm volatile("" ::: "memory"); }
DI unsigned pk2(float lo, float hi) { bf16x2 v = {(__bf16)lo, (__bf16)hi}; return __builtin_bit_cast(unsigned, v); }
DI u32x2 pk4(f32x4 v) { return (u32x2){pk2(v[0], v[1]), pk2(v[2], v[3])}; }
DI float bflo(unsigned u) { return __uint_as_float(u << 16); }
DI float bfhi(unsigned u) { return __uint_as_float(u & 0xffff0000u); }
DI f32x4 unpk4(u32x2 u) { return (f32x4){bflo(u[0]), bfhi(u[0]), bflo(u[1]), bfhi(u[1])}; }
DI bf16x8 cat8(u32x2 a, u32x2 b) { u32x4 v = {a[0], a[1], b[0], b[1]}; return __builtin_bit_cast(bf16x8, v); }
DI f32x4 mfma(bf16x8 a, bf16x8 b, f32x4 c) { return __builtin_amdgcn_mfma_f32_16x16x32_bf16(a, b, c, 0, 0, 0); }
DI bf16x8 lds_a(LAS char* p) { return *(LAS bf16x8*)p; }
DI u32x2 lds_tr(LAS char* p) { return __builtin_bit_cast(u32x2, __builtin_amdgcn_ds_read_tr16_b64_v4i16((LAS s16x4*)p)); }
DI float dot2(unsigned a, unsigned b, float c) {
  return __builtin_amdgcn_fdot2_f32_bf16(__builtin_bit_cast(bf16x2, a), __builtin_bit_cast(bf16x2, b), c, false);
}
DI int swap23(int k) { return (k & ~12) | ((k & 4) << 1) | ((k & 8) >> 1); }
DI float gelu_t(float x) {
  float e = __builtin_amdgcn_exp2f(x * (-2.302208198f + -0.1029432397f * x * x));
  return x * __builtin_amdgcn_rcpf(1.0f + e);
}
constexpr unsigned ONES = 0x3f803f80u;

template <int XM> DI void tile_of(int bid, int& pm, int& pn) {
  const int xcd = bid & 7, idx = bid >> 3;
  constexpr int GM = 16 / XM, XN = 32 / XM;
  pm = (xcd % GM) * XM + idx % XM;
  pn = (xcd / GM) * XN + idx / XM;
}

DI int b_pos_lane(int lane) { const int g = lane >> 4, q = (lane & 15) >> 2; return (g >> 1) * 16 + (g & 1) * 4 + q; }
DI unsigned b128_base(int lane, int nb) { const int pos = b_pos_lane(lane); return pos * 128 + ((nb ^ ((pos >> 1) & 3)) * 32) + (lane & 3) * 8; }
DI unsigned b224_base(int lane) { return b_pos_lane(lane) * 224 + (lane & 3) * 8; }
DI unsigned a_base(int lane, int kk) { const int r = lane & 15, g = lane >> 4; return r * 128 + (((kk * 4 + g) ^ ((r >> 1) & 7)) * 16); }

template <int NK, int NS, int STAGE, class F, class L, class M>
DI void compute_loop(L load, M mma) {
  F f0, f1;
  bar();
  load(f0, 0u, 0);
  unsigned so = 0;
#pragma unroll 1
  for (int t = 0; t < NK - 1; ++t) {
    load(f1, so, 1);
    mma(f0);
    wait_lgkm0();
    bar();
    so += STAGE; if (so == NS * STAGE) so = 0;
    load(f0, so, 0);
    mma(f1);
  }
  load(f1, so, 1);
  mma(f0);
  wait_lgkm0();
  bar();
  mma(f1);
}
template <int R, int NS, int STAGE, int PW, class S>
DI void helper_tail(S& stat, unsigned so) {
  stat(so);
  wait_lgkm0();
  if constexpr (R >= 2) wait_vm<(R - 2) * PW>();
  bar();
  so += STAGE; if (so == NS * STAGE) so = 0;
  if constexpr (R >= 2) helper_tail<R - 1, NS, STAGE, PW>(stat, so);
}
template <int NK, int NS, int STAGE, int PW, class I, class S>
DI void helper_loop(I issue, S stat) {
#pragma unroll
  for (int s = 0; s < NS; ++s) issue(s, (unsigned)(s * STAGE));
  wait_vm<(NS - 1) * PW>();
  bar();
  unsigned so = 0;
#pragma unroll 1
  for (int t = 0; t < NK - NS; ++t) {
    stat(so);
    wait_lgkm0();
    wait_vm<(NS - 2) * PW>();
    bar();
    issue(t + NS, so);
    so += STAGE; if (so == NS * STAGE) so = 0;
  }
  helper_tail<NS, NS, STAGE, PW>(stat, so);
}

DI void rowstat128(LAS char* a, int ht, float& s, float& q) {
  const int row = ht >> 1;
#pragma unroll
  for (int j = 0; j < 4; ++j) {
    u32x4 v = *(LAS u32x4*)(a + row * 128 + ((((ht & 1) * 4 + j) ^ ((row >> 1) & 7)) * 16));
#pragma unroll
    for (int e = 0; e < 4; ++e) { s = dot2(v[e], ONES, s); q = dot2(v[e], v[e], q); }
  }
}
template <int RS> DI void colsum16(LAS char* p, float& c) {
#pragma unroll
  for (int i = 0; i < 4; ++i) {
    u32x2 v = lds_tr(p + ((i >> 1) * 32 + (i & 1) * 8) * RS);
    c = dot2(v[0], ONES, c); c = dot2(v[1], ONES, c);
  }
}

namespace g1 {
constexpr int NK = 16, NS = 3, ABYTES = 144 * 128, BBYTES = 3 * 8192, STAGE = ABYTES + BBYTES;
constexpr int L_MU = NS * STAGE, L_RS = L_MU + 144 * 4, L_CS = L_RS + 144 * 4, L_X = L_CS + 192 * 4, LDS = L_X + 6 * 1024;
struct Fr { bf16x8 a[5]; u32x2 b[6][2]; };
}
__global__ __launch_bounds__(512, 2) void k_g1(const __bf16* __restrict__ x, const __bf16* __restrict__ w, char* __restrict__ ws) {
  using namespace g1;
  extern __shared__ __attribute__((aligned(16))) char smem_[];
  LAS char* lds = (LAS char*)smem_;
  const int tid = threadIdx.x, lane = tid & 63, wid = __builtin_amdgcn_readfirstlane(tid >> 6);
  int pm, pn; tile_of<8>(blockIdx.x, pm, pn);
  if (blockIdx.x == 0) ((u64*)(ws + WS_FLAGS))[tid] = 0;
  if (wid >= 4) {
    const int hw = wid - 4, ht = tid - 256;
    const i32x4 srdA = make_srd(x, SEQ * DM * 2), srdB = make_srd(w, DM * 3 * DM * 2);
    unsigned va[5], vb[6];
#pragma unroll
    for (int i = 0; i < 5; ++i) {
      const int p = i < 4 ? hw * 4 + i : 16 + (hw & 1);
      const int r = p * 8 + (lane >> 3);
      int grow = r < 128 ? pm * 128 + r : pm * 128 - 16 + (r - 128);
      grow = grow < 0 ? 0 : grow;
      va[i] = grow * (DM * 2) + (((lane & 7) ^ ((r >> 1) & 7)) * 16);
    }
#pragma unroll
    for (int i = 0; i < 6; ++i) {
      const int p = hw * 6 + i, seg = p >> 3, pos = (p & 7) * 8 + (lane >> 3), k = swap23(pos);
      const int pc = (lane & 7) >> 1, nb = pc ^ ((pos >> 1) & 3);
      vb[i] = k * (3 * DM * 2) + (seg * DM + pn * 64 + nb * 16 + (lane & 1) * 8) * 2;
    }
    float rs_ = 0.f, rq_ = 0.f, hs_ = 0.f, hq_ = 0.f, cs_[3] = {0.f, 0.f, 0.f};
    unsigned cb[3];
#pragma unroll
    for (int i = 0; i < 3; ++i) { const int n = hw * 3 + i; cb[i] = ABYTES + (n >> 2) * 8192 + b128_base(lane, n & 3); }
    auto issue = [&](int t, unsigned so) {
#pragma unroll
      for (int i = 0; i < 4; ++i) dma16(srdA, va[i], t * 128u, so + (hw * 4 + i) * 1024u);
      if (hw < 2) dma16(srdA, va[4], t * 128u, so + (16 + hw) * 1024u);
#pragma unroll
      for (int i = 0; i < 6; ++i) dma16(srdB, vb[i], t * (64u * 3 * DM * 2), so + ABYTES + (hw * 6 + i) * 1024u);
    };
    auto stat = [&](unsigned so) {
      rowstat128(lds + so, ht, rs_, rq_);
      if (hw < 2) {
        u32x4 v = *(LAS u32x4*)(lds + so + 128 * 128 + ht * 16);
#pragma unroll
        for (int e = 0; e < 4; ++e) { hs_ = dot2(v[e], ONES, hs_); hq_ = dot2(v[e], v[e], hq_); }
      }
#pragma unroll
      for (int i = 0; i < 3; ++i) colsum16<128>(lds + so + cb[i], cs_[i]);
    };
    helper_loop<NK, NS, STAGE, 10>(issue, stat);
    rs_ += __shfl_xor(rs_, 1); rq_ += __shfl_xor(rq_, 1);
    if ((ht & 1) == 0) {
      const float mu = rs_ * (1.f / DM), var = fmaxf(rq_ * (1.f / DM) - mu * mu, 0.f);
      *(LAS float*)(lds + L_MU + (ht >> 1) * 4) = mu;
      *(LAS float*)(lds + L_RS + (ht >> 1) * 4) = 1.0f / sqrtf(var + LN_EPS);
    }
    if (hw < 2) {
#pragma unroll
      for (int o = 1; o < 8; o <<= 1) { hs_ += __shfl_xor(hs_, o); hq_ += __shfl_xor(hq_, o); }
      if ((ht & 7) == 0) {
        const float mu = hs_ * (1.f / DM), var = fmaxf(hq_ * (1.f / DM) - mu * mu, 0.f);
        *(LAS float*)(lds + L_MU + (128 + (ht >> 3)) * 4) = mu;
        *(LAS float*)(lds + L_RS + (128 + (ht >> 3)) * 4) = 1.0f / sqrtf(var + LN_EPS);
      }
    }
#pragma unroll
    for (int i = 0; i < 3; ++i) {
      float c = cs_[i]; c += __shfl_xor(c, 16); c += __shfl_xor(c, 32);
      if (lane < 16) *(LAS float*)(lds + L_CS + ((hw * 3 + i) * 16 + lane) * 4) = c;
    }
    wait_lgkm0(); bar();
    bar();
    return;
  }
  const int hd = wid & 1, rh = wid >> 1;
  unsigned ab[2], hb[2], bb[2];
#pragma unroll
  for (int kk = 0; kk < 2; ++kk) { ab[kk] = a_base(lane, kk) + rh * 64 * 128; hb[kk] = a_base(lane, kk) + 128 * 128; bb[kk] = ABYTES + b128_base(lane, hd * 2 + kk); }
  f32x4 aq[4][2], ak[4][2], av[4][2], ah[2];
#pragma unroll
  for (int m = 0; m < 4; ++m)
#pragma unroll
    for (int j = 0; j < 2; ++j) { aq[m][j] = ak[m][j] = av[m][j] = (f32x4){0.f, 0.f, 0.f, 0.f}; }
  ah[0] = ah[1] = (f32x4){0.f, 0.f, 0.f, 0.f};
  auto load = [&](Fr& f, unsigned so, int kk) {
#pragma unroll
    for (int m = 0; m < 4; ++m) f.a[m] = lds_a(lds + so + ab[kk] + m * 2048);
    f.a[4] = lds_a(lds + so + hb[kk]);
#pragma unroll
    for (int s = 0; s < 3; ++s)
#pragma unroll
      for (int j = 0; j < 2; ++j)
#pragma unroll
        for (int hh = 0; hh < 2; ++hh) f.b[s * 2 + j][hh] = lds_tr(lds + so + bb[j] + s * 8192 + (kk * 32 + hh * 8) * 128);
  };
  auto mma = [&](Fr& f) {
    bf16x8 b[6];
#pragma unroll
    for (int i = 0; i < 6; ++i) b[i] = cat8(f.b[i][0], f.b[i][1]);
#pragma unroll
    for (int m = 0; m < 4; ++m)
#pragma unroll
      for (int j = 0; j < 2; ++j) {
        aq[m][j] = mfma(b[j], f.a[m], aq[m][j]);
        ak[m][j] = mfma(b[2 + j], f.a[m], ak[m][j]);
        av[m][j] = mfma(f.a[m], b[4 + j], av[m][j]);
      }
#pragma unroll
    for (int j = 0; j < 2; ++j) {
      const bf16x8 p = rh == 0 ? b[2 + j] : f.a[4], q = rh == 0 ? f.a[4] : b[4 + j];
      ah[j] = mfma(p, q, ah[j]);
    }
  };
  compute_loop<NK, NS, STAGE, Fr>(load, mma);
  bar();
  const int i16 = lane & 15, g = lane >> 4;
  f32x4 csq[2], csk[2]; float csv[2];
#pragma unroll
  for (int j = 0; j < 2; ++j) {
    csq[j] = *(LAS f32x4*)(lds + L_CS + (hd * 32 + j * 16 + 4 * g) * 4);
    csk[j] = *(LAS f32x4*)(lds + L_CS + (64 + hd * 32 + j * 16 + 4 * g) * 4);
    csv[j] = *(LAS float*)(lds + L_CS + (128 + hd * 32 + j * 16 + i16) * 4);
  }
  bf16x8 Q[4], K[5]; u32x2 V[5][2];
#pragma unroll
  for (int m = 0; m < 4; ++m) {
    const int r = rh * 64 + m * 16;
    const float mu = *(LAS float*)(lds + L_MU + (r + i16) * 4), rs = *(LAS float*)(lds + L_RS + (r + i16) * 4);
    const f32x4 mu4 = *(LAS f32x4*)(lds + L_MU + (r + 4 * g) * 4), rs4 = *(LAS f32x4*)(lds + L_RS + (r + 4 * g) * 4);
    Q[m] = cat8(pk4((aq[m][0] - mu * csq[0]) * rs), pk4((aq[m][1] - mu * csq[1]) * rs));
    K[m + 1] = cat8(pk4((ak[m][0] - mu * csk[0]) * rs), pk4((ak[m][1] - mu * csk[1]) * rs));
    V[m + 1][0] = pk4((av[m][0] - mu4 * csv[0]) * rs4);
    V[m + 1][1] = pk4((av[m][1] - mu4 * csv[1]) * rs4);
  }
  LAS char* xch = lds + L_X + hd * 3072 + lane * 16;
  if (rh == 0) {
    const float mu = *(LAS float*)(lds + L_MU + (128 + i16) * 4), rs = *(LAS float*)(lds + L_RS + (128 + i16) * 4);
    K[0] = cat8(pk4((ah[0] - mu * csk[0]) * rs), pk4((ah[1] - mu * csk[1]) * rs));
    *(LAS bf16x8*)(xch) = K[4];
    *(LAS bf16x8*)(xch + 1024) = cat8(V[4][0], V[4][1]);
  } else {
    const f32x4 mu4 = *(LAS f32x4*)(lds + L_MU + (128 + 4 * g) * 4), rs4 = *(LAS f32x4*)(lds + L_RS + (128 + 4 * g) * 4);
    *(LAS bf16x8*)(xch + 2048) = cat8(pk4((ah[0] - mu4 * csv[0]) * rs4), pk4((ah[1] - mu4 * csv[1]) * rs4));
  }
  wait_lgkm0(); bar();
  {
    u32x4 v = *(LAS u32x4*)(xch + (rh == 0 ? 2048 : 1024));
    V[0][0] = (u32x2){v[0], v[1]}; V[0][1] = (u32x2){v[2], v[3]};
    if (rh == 1) K[0] = *(LAS bf16x8*)(xch);
  }
  char* oblk = ws + WS_O + (size_t)(pm * 16 + pn) * 16384;
  constexpr float SC = 0.17677669529663687f * 1.4426950408889634f;
#pragma unroll
  for (int m = 0; m < 4; ++m) {
    const f32x4 z = {0.f, 0.f, 0.f, 0.f};
    f32x4 sp = mfma(K[m], Q[m], z), sc = mfma(K[m + 1], Q[m], z);
    const bool first = (pm == 0) && (rh == 0) && (m == 0);
    float s[8], mx = -INFINITY;
#pragma unroll
    for (int r = 0; r < 4; ++r) {
      const int dp = i16 + 16 - (4 * g + r), dc = i16 - (4 * g + r);
      s[r] = ((dp & 1) == 0 && dp <= 14 && !first) ? sp[r] * SC : -INFINITY;
      s[4 + r] = ((dc & 1) == 0 && dc >= 0) ? sc[r] * SC : -INFINITY;
      mx = fmaxf(mx, fmaxf(s[r], s[4 + r]));
    }
    mx = fmaxf(mx, __shfl_xor(mx, 16)); mx = fmaxf(mx, __shfl_xor(mx, 32));
    float sum = 0.f;
#pragma unroll
    for (int r = 0; r < 8; ++r) { s[r] = __builtin_amdgcn_exp2f(s[r] - mx); sum += s[r]; }
    sum += __shfl_xor(sum, 16); sum += __shfl_xor(sum, 32);
    const float inv = 1.0f / sum;
    const bf16x8 P = cat8(pk4((f32x4){s[0], s[1], s[2], s[3]} * inv), pk4((f32x4){s[4], s[5], s[6], s[7]} * inv));
    const int row = rh * 64 + m * 16 + i16;
#pragma unroll
    for (int db = 0; db < 2; ++db) {
      const f32x4 o = mfma(cat8(V[m][db], V[m + 1][db]), P, z);
      const int chunk = hd * 4 + db * 2 + (g >> 1);
      *(u32x2*)(oblk + row * 128 + ((chunk ^ ((row >> 1) & 7)) * 16) + (g & 1) * 8) = pk4(o);
    }
  }
}

namespace gs {
constexpr int NS = 4, ABYTES = 16384, BBYTES = 8192, STAGE = ABYTES + BBYTES, LDS = NS * STAGE;
struct Fr { bf16x8 a[4]; u32x2 b[2][2]; };
}
template <int NK, bool LAST>
__global__ __launch_bounds__(512, 2) void k_gs(const char* __restrict__ aimg, const __bf16* __restrict__ w, const char* __restrict__ resid, char* __restrict__ out) {
  using namespace gs;
  extern __shared__ __attribute__((aligned(16))) char smem_[];
  LAS char* lds = (LAS char*)smem_;
  const int tid = threadIdx.x, lane = tid & 63, wid = __builtin_amdgcn_readfirstlane(tid >> 6);
  int pm, pn; tile_of<4>(blockIdx.x, pm, pn);
  if (wid >= 4) {
    const int hw = wid - 4;
    const i32x4 srdA = make_srd(aimg + (size_t)pm * NK * 16384, NK * 16384), srdB = make_srd(w, NK * 64 * DM * 2);
    const unsigned va = hw * 4096 + lane * 16;
    unsigned vb[2];
#pragma unroll
    for (int i = 0; i < 2; ++i) {
      const int pos = (hw * 2 + i) * 8 + (lane >> 3), k = swap23(pos), nb = ((lane & 7) >> 1) ^ ((pos >> 1) & 3);
      vb[i] = k * (DM * 2) + (pn * 64 + nb * 16 + (lane & 1) * 8) * 2;
    }
    auto issue = [&](int t, unsigned so) {
#pragma unroll
      for (int i = 0; i < 4; ++i) dma16(srdA, va, t * 16384u + i * 1024u, so + (hw * 4 + i) * 1024u);
#pragma unroll
      for (int i = 0; i < 2; ++i) dma16(srdB, vb[i], t * (64u * DM * 2), so + ABYTES + (hw * 2 + i) * 1024u);
    };
    helper_loop<NK, NS, STAGE, 6>(issue, [](unsigned) {});
    return;
  }
  const int wc = wid & 1, wr = wid >> 1, i16 = lane & 15, g = lane >> 4;
  unsigned ab[2], bb[2];
#pragma unroll
  for (int kk = 0; kk < 2; ++kk) { ab[kk] = a_base(lane, kk) + wr * 64 * 128; bb[kk] = ABYTES + b128_base(lane, wc * 2 + kk); }
  u32x2 res[4][2];
#pragma unroll
  for (int m = 0; m < 4; ++m)
#pragma unroll
    for (int j = 0; j < 2; ++j) {
      const int row = wr * 64 + m * 16 + i16, chunk = wc * 4 + j * 2 + (g >> 1);
      if (LAST) res[m][j] = *(const u32x2*)(resid + (size_t)(pm * 16 + pn) * 16384 + row * 128 + ((chunk ^ ((row >> 1) & 7)) * 16) + (g & 1) * 8);
      else res[m][j] = *(const u32x2*)(resid + (size_t)(pm * 128 + row) * (DM * 2) + pn * 128 + chunk * 16 + (g & 1) * 8);
    }
  f32x4 acc[4][2];
#pragma unroll
  for (int m = 0; m < 4; ++m) acc[m][0] = acc[m][1] = (f32x4){0.f, 0.f, 0.f, 0.f};
  auto load = [&](Fr& f, unsigned so, int kk) {
#pragma unroll
    for (int m = 0; m < 4; ++m) f.a[m] = lds_a(lds + so + ab[kk] + m * 2048);
#pragma unroll
    for (int j = 0; j < 2; ++j)
#pragma unroll
      for (int hh = 0; hh < 2; ++hh) f.b[j][hh] = lds_tr(lds + so + bb[j] + (kk * 32 + hh * 8) * 128);
  };
  auto mma = [&](Fr& f) {
#pragma unroll
    for (int m = 0; m < 4; ++m)
#pragma unroll
      for (int j = 0; j < 2; ++j) acc[m][j] = mfma(cat8(f.b[j][0], f.b[j][1]), f.a[m], acc[m][j]);
  };
  compute_loop<NK, NS, STAGE, Fr>(load, mma);
#pragma unroll
  for (int m = 0; m < 4; ++m)
#pragma unroll
    for (int j = 0; j < 2; ++j) {
      const int row = wr * 64 + m * 16 + i16, chunk = wc * 4 + j * 2 + (g >> 1);
      const u32x2 v = pk4(acc[m][j] + unpk4(res[m][j]));
      if (LAST) *(u32x2*)(out + (size_t)(pm * 128 + row) * (DM * 2) + pn * 128 + chunk * 16 + (g & 1) * 8) = v;
      else *(u32x2*)(out + (size_t)(pm * 16 + pn) * 16384 + row * 128 + ((chunk ^ ((row >> 1) & 7)) * 16) + (g & 1) * 8) = v;
    }
}

namespace g3 {
constexpr int NK = 16, NS = 3, ABYTES = 16384, SEG = 14336, STAGE = ABYTES + 2 * SEG;
constexpr int L_MU = NS * STAGE, L_RS = L_MU + 512, L_CS = L_RS + 512, L_P = L_CS + 224 * 4, L_S3 = L_P + 2048, LDS = L_S3 + 1024;
struct Fr { bf16x8 a[4]; u32x2 b[7][2]; };
}
__global__ __launch_bounds__(512, 2) void k_g3(const __bf16* __restrict__ w, char* __restrict__ ws) {
  using namespace g3;
  extern __shared__ __attribute__((aligned(16))) char smem_[];
  LAS char* lds = (LAS char*)smem_;
  const int tid = threadIdx.x, lane = tid & 63, wid = __builtin_amdgcn_readfirstlane(tid >> 6);
  int pm, pn; tile_of<8>(blockIdx.x, pm, pn);
  if (wid >= 4) {
    const int hw = wid - 4, ht = tid - 256;
    const i32x4 srdA = make_srd(ws + WS_H + (size_t)pm * NK * 16384, NK * 16384), srdB = make_srd(w, DM * FF * 2);
    const unsigned va = hw * 4096 + lane * 16;
    unsigned vb[7];
#pragma unroll
    for (int i = 0; i < 7; ++i) {
      const int p = hw * 7 + i, seg = p / 14, q = (p % 14) * 64 + lane, pos = q / 14, gi = q % 14;
      vb[i] = swap23(pos) * (FF * 2) + (seg * FH + pn * 112 + gi * 8) * 2;
    }
    float rs_ = 0.f, rq_ = 0.f, cs_[4] = {0.f, 0.f, 0.f, 0.f};
    unsigned cb[4];
#pragma unroll
    for (int i = 0; i < 4; ++i) { int n = hw * 4 + i; n = n > 13 ? 13 : n; cb[i] = ABYTES + (n / 7) * SEG + (n % 7) * 32 + b224_base(lane); }
    auto issue = [&](int t, unsigned so) {
#pragma unroll
      for (int i = 0; i < 4; ++i) dma16(srdA, va, t * 16384u + i * 1024u, so + (hw * 4 + i) * 1024u);
#pragma unroll
      for (int i = 0; i < 7; ++i) dma16(srdB, vb[i], t * (64u * FF * 2), so + ABYTES + (hw * 7 + i) * 1024u);
    };
    auto stat = [&](unsigned so) {
      rowstat128(lds + so, ht, rs_, rq_);
#pragma unroll
      for (int i = 0; i < 4; ++i) colsum16<224>(lds + so + cb[i], cs_[i]);
    };
    helper_loop<NK, NS, STAGE, 11>(issue, stat);
    rs_ += __shfl_xor(rs_, 1); rq_ += __shfl_xor(rq_, 1);
    if ((ht & 1) == 0) {
      const float mu = rs_ * (1.f / DM), var = fmaxf(rq_ * (1.f / DM) - mu * mu, 0.f);
      *(LAS float*)(lds + L_MU + (ht >> 1) * 4) = mu;
      *(LAS float*)(lds + L_RS + (ht >> 1) * 4) = 1.0f / sqrtf(var + LN_EPS);
    }
#pragma unroll
    for (int i = 0; i < 4; ++i) {
      float c = cs_[i]; c += __shfl_xor(c, 16); c += __shfl_xor(c, 32);
      if (lane < 16 && hw * 4 + i < 14) *(LAS float*)(lds + L_CS + ((hw * 4 + i) * 16 + lane) * 4) = c;
    }
    wait_lgkm0(); bar();
    bar();
    bar();
    return;
  }
  const int wc = wid & 1, wr = wid >> 1, i16 = lane & 15, g = lane >> 4;
  unsigned ab[2];
#pragma unroll
  for (int kk = 0; kk < 2; ++kk) ab[kk] = a_base(lane, kk) + wr * 64 * 128;
  const unsigned bP = ABYTES + b224_base(lane) + (wc == 0 ? 0 : SEG + 3 * 32), bQ = ABYTES + b224_base(lane) + (wc == 0 ? SEG : 4 * 32);
  f32x4 acc[4][7];
#pragma unroll
  for (int m = 0; m < 4; ++m)
#pragma unroll
    for (int j = 0; j < 7; ++j) acc[m][j] = (f32x4){0.f, 0.f, 0.f, 0.f};
  auto load = [&](Fr& f, unsigned so, int kk) {
#pragma unroll
    for (int m = 0; m < 4; ++m) f.a[m] = lds_a(lds + so + ab[kk] + m * 2048);
#pragma unroll
    for (int j = 0; j < 7; ++j)
#pragma unroll
      for (int hh = 0; hh < 2; ++hh) f.b[j][hh] = lds_tr(lds + so + (j < 4 ? bP + j * 32 : bQ + (j - 4) * 32) + (kk * 32 + hh * 8) * 224);
  };
  auto mma = [&](Fr& f) {
#pragma unroll
    for (int m = 0; m < 4; ++m)
#pragma unroll
      for (int j = 0; j < 7; ++j) acc[m][j] = mfma(cat8(f.b[j][0], f.b[j][1]), f.a[m], acc[m][j]);
  };
  compute_loop<NK, NS, STAGE, Fr>(load, mma);
  bar();
  float mu[4], rs[4];
#pragma unroll
  for (int m = 0; m < 4; ++m) { mu[m] = *(LAS float*)(lds + L_MU + (wr * 64 + m * 16 + i16) * 4); rs[m] = *(LAS float*)(lds + L_RS + (wr * 64 + m * 16 + i16) * 4); }
  auto act = [&](int j, int blk) {
    const f32x4 cs = *(LAS f32x4*)(lds + L_CS + (blk * 16 + 4 * g) * 4);
#pragma unroll
    for (int m = 0; m < 4; ++m) {
      f32x4 v = (acc[m][j] - mu[m] * cs) * rs[m];
#pragma unroll
      for (int e = 0; e < 4; ++e) v[e] = gelu_t(v[e]);
      acc[m][j] = v;
    }
  };
  auto tail = [&](auto WC) {
    constexpr int W = decltype(WC)::value;
    constexpr int Z2J0 = W == 0 ? 4 : 0, Z2N = W == 0 ? 3 : 4, Z2B0 = W == 0 ? 0 : 3;
    constexpr int Z1J0 = W == 0 ? 0 : 4, Z1N = W == 0 ? 4 : 3, Z1B0 = W == 0 ? 0 : 4;
#pragma unroll
    for (int j = 0; j < Z2N; ++j) act(Z2J0 + j, 7 + Z2B0 + j);
#pragma unroll
    for (int m = 0; m < 4; ++m) {
      float s = 0.f, q = 0.f;
#pragma unroll
      for (int j = 0; j < Z2N; ++j)
#pragma unroll
        for (int e = 0; e < 4; ++e) { const float v = acc[m][Z2J0 + j][e]; s += v; q += v * v; }
      s += __shfl_xor(s, 16); q += __shfl_xor(q, 16); s += __shfl_xor(s, 32); q += __shfl_xor(q, 32);
      if (g == 0) *(LAS f32x2*)(lds + L_P + (W * 128 + wr * 64 + m * 16 + i16) * 8) = (f32x2){s, q};
    }
    wait_lgkm0(); bar();
    gu64* flags = (gu64*)(ws + WS_FLAGS);
    gu64* xbuf = (gu64*)(ws + WS_XBUF);
    if (W == 0) {
      const int row = wr * 64 + lane;
      const f32x2 p = *(LAS f32x2*)(lds + L_P + row * 8) + *(LAS f32x2*)(lds + L_P + (128 + row) * 8);
      __hip_atomic_store(xbuf + (size_t)(pm * 16 + pn) * 128 + row, __builtin_bit_cast(u64, p), __ATOMIC_RELAXED, __HIP_MEMORY_SCOPE_AGENT);
      wait_vm<0>();
      if (lane == 0) __hip_atomic_store(flags + (pm * 2 + wr) * 16 + pn, 1ull, __ATOMIC_RELAXED, __HIP_MEMORY_SCOPE_AGENT);
    }
    char* z1t = ws + WS_Z1 + (size_t)(pm * 16 + pn) * (128 * 224);
#pragma unroll
    for (int j = 0; j < Z1N; ++j) {
      act(Z1J0 + j, Z1B0 + j);
#pragma unroll
      for (int m = 0; m < 4; ++m) *(u32x2*)(z1t + (wr * 64 + m * 16 + i16) * 224 + ((Z1B0 + j) * 16 + 4 * g) * 2) = pk4(acc[m][Z1J0 + j]);
    }
    if (W == 0) {
      for (unsigned spin = 0; spin < (1u << 22); ++spin) {
        const u64 f = __hip_atomic_load(flags + (pm * 2 + wr) * 16 + (lane & 15), __ATOMIC_RELAXED, __HIP_MEMORY_SCOPE_AGENT);
        if (__all(f == 1ull)) break;
        __builtin_amdgcn_s_sleep(1);
      }
      asm volatile("" ::: "memory");
      const int row = wr * 64 + lane;
      f32x2 t = {0.f, 0.f};
#pragma unroll
      for (int i = 0; i < 16; ++i)
        t += __builtin_bit_cast(f32x2, __hip_atomic_load(xbuf + (size_t)(pm * 16 + i) * 128 + row, __ATOMIC_RELAXED, __HIP_MEMORY_SCOPE_AGENT));
      const float m3 = t[0] * (1.f / FH), var = fmaxf(t[1] * (1.f / FH) - m3 * m3, 0.f);
      *(LAS f32x2*)(lds + L_S3 + row * 8) = (f32x2){m3, 1.0f / sqrtf(var + LN_EPS)};
    }
    wait_lgkm0(); bar();
    char* z2b = ws + WS_Z2 + (size_t)(pn * 32 + pm * 2 + wr) * SEG;
#pragma unroll
    for (int m = 0; m < 4; ++m) {
      const f32x2 st = *(LAS f32x2*)(lds + L_S3 + (wr * 64 + m * 16 + i16) * 8);
      const int pos = swap23(m * 16 + i16);
#pragma unroll
      for (int j = 0; j < Z2N; ++j) *(u32x2*)(z2b + pos * 224 + ((Z2B0 + j) * 16 + 4 * g) * 2) = pk4((acc[m][Z2J0 + j] - st[0]) * st[1]);
    }
  };
  if (wc == 0) tail(std::integral_constant<int, 0>{}); else tail(std::integral_constant<int, 1>{});
}

namespace g4 {
constexpr int NK = 32, NS = 4, ABYTES = 16384, SEG = 14336, STAGE = ABYTES + SEG, LDS = NS * STAGE;
struct Fr { bf16x8 a[2]; u32x2 b[7][2]; };
}
__global__ __launch_bounds__(512, 2) void k_g4(const __bf16* __restrict__ wsp, const __bf16* __restrict__ bsp, char* __restrict__ ws) {
  using namespace g4;
  extern __shared__ __attribute__((aligned(16))) char smem_[];
  LAS char* lds = (LAS char*)smem_;
  const int tid = threadIdx.x, lane = tid & 63, wid = __builtin_amdgcn_readfirstlane(tid >> 6);
  int pm, pn; tile_of<8>(blockIdx.x, pm, pn);
  if (wid >= 4) {
    const int hw = wid - 4;
    const i32x4 srdA = make_srd(wsp, SEQ * SEQ * 2), srdB = make_srd(ws + WS_Z2 + (size_t)pn * NK * SEG, NK * SEG);
    unsigned va[4];
#pragma unroll
    for (int i = 0; i < 4; ++i) { const int r = (hw * 4 + i) * 8 + (lane >> 3); va[i] = (pm * 128 + r) * (SEQ * 2) + (((lane & 7) ^ ((r >> 1) & 7)) * 16); }
    const unsigned vb = lane * 16;
    auto issue = [&](int t, unsigned so) {
#pragma unroll
      for (int i = 0; i < 4; ++i) dma16(srdA, va[i], t * 128u, so + (hw * 4 + i) * 1024u);
#pragma unroll
      for (int i = 0; i < 3; ++i) dma16(srdB, vb, t * (unsigned)SEG + (hw * 3 + i) * 1024u, so + ABYTES + (hw * 3 + i) * 1024u);
      if (hw < 2) dma16(srdB, vb, t * (unsigned)SEG + (12 + hw) * 1024u, so + ABYTES + (12 + hw) * 1024u);
    };
    helper_loop<NK, NS, STAGE, 7>(issue, [](unsigned) {});
    return;
  }
  const int i16 = lane & 15, g = lane >> 4;
  unsigned ab[2];
#pragma unroll
  for (int kk = 0; kk < 2; ++kk) ab[kk] = a_base(lane, kk) + wid * 32 * 128;
  const unsigned bb = ABYTES + b224_base(lane);
  u32x2 z1[2][7]; float bias[2];
  const char* z1t = ws + WS_Z1 + (size_t)(pm * 16 + pn) * (128 * 224);
#pragma unroll
  for (int m = 0; m < 2; ++m) {
    const int row = wid * 32 + m * 16 + i16;
    bias[m] = (float)bsp[pm * 128 + row];
#pragma unroll
    for (int j = 0; j < 7; ++j) z1[m][j] = *(const u32x2*)(z1t + row * 224 + (j * 16 + 4 * g) * 2);
  }
  f32x4 acc[2][7];
#pragma unroll
  for (int m = 0; m < 2; ++m)
#pragma unroll
    for (int j = 0; j < 7; ++j) acc[m][j] = (f32x4){0.f, 0.f, 0.f, 0.f};
  auto load = [&](Fr& f, unsigned so, int kk) {
#pragma unroll
    for (int m = 0; m < 2; ++m) f.a[m] = lds_a(lds + so + ab[kk] + m * 2048);
#pragma unroll
    for (int j = 0; j < 7; ++j)
#pragma unroll
      for (int hh = 0; hh < 2; ++hh) f.b[j][hh] = lds_tr(lds + so + bb + j * 32 + (kk * 32 + hh * 8) * 224);
  };
  auto mma = [&](Fr& f) {
#pragma unroll
    for (int m = 0; m < 2; ++m)
#pragma unroll
      for (int j = 0; j < 7; ++j) acc[m][j] = mfma(cat8(f.b[j][0], f.b[j][1]), f.a[m], acc[m][j]);
  };
  compute_loop<NK, NS, STAGE, Fr>(load, mma);
  char* yp = ws + WS_Y + (size_t)pm * 28 * 16384;
#pragma unroll
  for (int m = 0; m < 2; ++m) {
    const int row = wid * 32 + m * 16 + i16;
#pragma unroll
    for (int j = 0; j < 7; ++j) {
      const int c = pn * 112 + j * 16 + 4 * g, kt = c >> 6, chunk = (c & 63) >> 3;
      *(u32x2*)(yp + (kt * 128 + row) * 128 + ((chunk ^ ((row >> 1) & 7)) * 16) + (c & 7) * 2) = pk4(unpk4(z1[m][j]) * (acc[m][j] + bias[m]));
    }
  }
}

extern "C" void kernel_launch(void* const* d_in, const int* in_sizes, int n_in, void* d_out, int out_size, void* d_ws, size_t ws_size,
                              hipStream_t stream) {
  const __bf16* x = (const __bf16*)d_in[0];
  const __bf16* wqkv = (const __bf16*)d_in[1];
  const __bf16* wo = (const __bf16*)d_in[2];
  const __bf16* win = (const __bf16*)d_in[4];
  const __bf16* wsp = (const __bf16*)d_in[5];
  const __bf16* bsp = (const __bf16*)d_in[6];
  const __bf16* wout = (const __bf16*)d_in[7];
  char* ws = (char*)d_ws;
  (void)hipFuncSetAttribute((const void*)k_g1, hipFuncAttributeMaxDynamicSharedMemorySize, g1::LDS);
  (void)hipFuncSetAttribute((const void*)k_gs<16, false>, hipFuncAttributeMaxDynamicSharedMemorySize, gs::LDS);
  (void)hipFuncSetAttribute((const void*)k_g3, hipFuncAttributeMaxDynamicSharedMemorySize, g3::LDS);
  (void)hipFuncSetAttribute((const void*)k_g4, hipFuncAttributeMaxDynamicSharedMemorySize, g4::LDS);
  (void)hipFuncSetAttribute((const void*)k_gs<28, true>, hipFuncAttributeMaxDynamicSharedMemorySize, gs::LDS);
  k_g1<<<256, 512, g1::LDS, stream>>>(x, wqkv, ws);
  k_gs<16, false><<<256, 512, gs::LDS, stream>>>(ws + WS_O, wo, (const char*)x, ws + WS_H);
  k_g3<<<256, 512, g3::LDS, stream>>>(win, ws);
  k_g4<<<256, 512, g4::LDS, stream>>>(wsp, bsp, ws);
  k_gs<28, true><<<256, 512, gs::LDS, stream>>>(ws + WS_Y, wout, ws + WS_H, (char*)d_out);
}
```

```cpp
#include <hip/hip_runtime.h>
#include <hip/hip_bf16.h>
#include <stdint.h>
#include <type_traits>

#define DI __device__ __forceinline__
#define LAS __attribute__((address_space(3)))
typedef __bf16 bf16x8 __attribute__((ext_vector_type(8)));
typedef __bf16 bf16x2 __attribute__((ext_vector_type(2)));
typedef short s16x4 __attribute__((ext_vector_type(4)));
typedef float f32x4 __attribute__((ext_vector_type(4)));
typedef float f32x2 __attribute__((ext_vector_type(2)));
typedef int i32x4 __attribute__((ext_vector_type(4)));
typedef unsigned u32x4 __attribute__((ext_vector_type(4)));
typedef unsigned u32x2 __attribute__((ext_vector_type(2)));
typedef unsigned long long u64;
typedef __attribute__((address_space(1))) u64 gu64;
typedef __attribute__((address_space(1))) unsigned gu32;
template <int V> using IC = std::integral_constant<int, V>;

#define POLL_SLEEP 1
#define G1ORDER 1
#define ROT2 1
#define ROT3 1
#define ROT4 1
#define ROT5 1

constexpr int SEQ = 2048, DM = 1024, FF = 3584, FH = 1792;
constexpr float LN_EPS = 1e-5f;

constexpr unsigned WS_EPOCH = 0;
constexpr unsigned F_S1 = 32768, F_O = 2 * 32768, F_H = 3 * 32768, F_W3 = 4 * 32768, F_S3 = 5 * 32768, F_Z2 = 6 * 32768, F_Y = 7 * 32768;
constexpr unsigned WS_S1R = 1u << 18;
constexpr unsigned WS_S1C = WS_S1R + (1u << 18);
constexpr unsigned WS_S2R = WS_S1C + (1u << 18);
constexpr unsigned WS_S2C = WS_S2R + (1u << 18);
constexpr unsigned WS_S3 = WS_S2C + (1u << 18);
constexpr unsigned WS_O = 2u << 20;
constexpr unsigned WS_H = WS_O + (4u << 20);
constexpr unsigned WS_Z2 = WS_H + (4u << 20);
constexpr unsigned WS_Y = WS_Z2 + (8u << 20);
constexpr unsigned WS_W3 = WS_Y + (8u << 20);
constexpr unsigned WS_END = WS_W3 + (8u << 20);
constexpr size_t WS_TS = 64u << 20;
#define TS(i) do {} while (0)

constexpr unsigned L_MISC = 135168;
constexpr unsigned L_MU = L_MISC, L_RS = L_MU + 576, L_CS = L_RS + 576, L_P = L_CS + 896, L_M3 = L_P + 2048, L_R3 = L_M3 + 512, L_TAG = L_R3 + 512,
                   L_C2 = L_TAG + 16, L_X = 129024;
constexpr unsigned L_H = 147456;
constexpr unsigned L_ZS = 98304;
constexpr unsigned L_Z1 = 49152;
constexpr unsigned LDS_BYTES = 163840;
static_assert(L_C2 + 2 * 896 <= L_H && L_X + 6144 <= L_MISC, "lds");

DI i32x4 make_srd(const void* p, unsigned bytes) {
  u64 a = (u64)p;
  i32x4 r;
  r[0] = __builtin_amdgcn_readfirstlane((int)(unsigned)a);
  r[1] = __builtin_amdgcn_readfirstlane((int)(unsigned)(a >> 32));
  r[2] = (int)bytes;
  r[3] = 0x00020000;
  return r;
}
template <bool DEV> DI void dma16(i32x4 srd, unsigned voff, unsigned soff, unsigned ldsaddr) {
  if constexpr (DEV)
    asm volatile("s_mov_b32 m0, %0\n\ts_nop 0\n\tbuffer_load_dwordx4 %1, %2, %3 offen sc1 lds" :: "s"(ldsaddr), "v"(voff), "s"(srd), "s"(soff) : "memory");
  else
    asm volatile("s_mov_b32 m0, %0\n\ts_nop 0\n\tbuffer_load_dwordx4 %1, %2, %3 offen lds" :: "s"(ldsaddr), "v"(voff), "s"(srd), "s"(soff) : "memory");
}
DI void ld16_async(u32x4& d, i32x4 srd, unsigned voff) { asm volatile("buffer_load_dwordx4 %0, %1, %2, 0 offen sc1" : "=v"(d) : "v"(voff), "s"(srd) : "memory"); }
DI void ld8_async(u32x2& d, i32x4 srd, unsigned voff) { asm volatile("buffer_load_dwordx2 %0, %1, %2, 0 offen sc1" : "=v"(d) : "v"(voff), "s"(srd) : "memory"); }
template <class T> DI void pin(T& v) { asm volatile("" : "+v"(v)); }
DI void st16_dev(i32x4 srd, unsigned voff, u32x4 v) { asm volatile("buffer_store_dwordx4 %0, %1, %2, 0 offen sc1\n\ts_nop 1" :: "v"(v), "v"(voff), "s"(srd) : "memory"); }
DI void st8_dev(i32x4 srd, unsigned voff, u32x2 v) { asm volatile("buffer_store_dwordx2 %0, %1, %2, 0 offen sc1" :: "v"(v), "v"(voff), "s"(srd) : "memory"); }
DI void st4_dev(i32x4 srd, unsigned voff, unsigned v) { asm volatile("buffer_store_dword %0, %1, %2, 0 offen sc1" :: "v"(v), "v"(voff), "s"(srd) : "memory"); }
template <int N> DI void wait_vm() { asm volatile("s_waitcnt vmcnt(%0)" :: "i"(N) : "memory"); }
DI void wait_lgkm0() { __builtin_amdgcn_s_waitcnt(0xC07F); asm volatile("" ::: "memory"); }
DI void bar() { asm volatile("" ::: "memory"); __builtin_amdgcn_s_barrier(); asm volatile("" ::: "memory"); }
DI unsigned pk2(float lo, float hi) { bf16x2 v = {(__bf16)lo, (__bf16)hi}; return __builtin_bit_cast(unsigned, v); }
DI u32x2 pk4(f32x4 v) { return (u32x2){pk2(v[0], v[1]), pk2(v[2], v[3])}; }
DI f32x2 unpk2(unsigned u) { return (f32x2){__uint_as_float(u << 16), __uint_as_float(u & 0xffff0000u)}; }
DI f32x4 unpk4(u32x2 u) { const f32x2 a = unpk2(u[0]), b = unpk2(u[1]); return (f32x4){a[0], a[1], b[0], b[1]}; }
DI bf16x8 cat8(u32x2 a, u32x2 b) { u32x4 v = {a[0], a[1], b[0], b[1]}; return __builtin_bit_cast(bf16x8, v); }
DI f32x4 mfma(bf16x8 a, bf16x8 b, f32x4 c) { return __builtin_amdgcn_mfma_f32_16x16x32_bf16(a, b, c, 0, 0, 0); }
DI bf16x8 lds_a(LAS char* p) { return *(LAS bf16x8*)p; }
DI u32x2 lds_tr(LAS char* p) { return __builtin_bit_cast(u32x2, __builtin_amdgcn_ds_read_tr16_b64_v4i16((LAS s16x4*)p)); }
#undef lds_tr
DI int swap23(int k) { return (k & ~12) | ((k & 4) << 1) | ((k & 8) >> 1); }
DI float sum4(float x) {
  auto r = __builtin_amdgcn_permlane16_swap(__float_as_uint(x), __float_as_uint(x), false, false);
  x = __uint_as_float(r[0]) + __uint_as_float(r[1]);
  auto s = __builtin_amdgcn_permlane32_swap(__float_as_uint(x), __float_as_uint(x), false, false);
  return __uint_as_float(s[0]) + __uint_as_float(s[1]);
}
DI float max4(float x) {
  auto r = __builtin_amdgcn_permlane16_swap(__float_as_uint(x), __float_as_uint(x), false, false);
  x = fmaxf(__uint_as_float(r[0]), __uint_as_float(r[1]));
  auto s = __builtin_amdgcn_permlane32_swap(__float_as_uint(x), __float_as_uint(x), false, false);
  return fmaxf(__uint_as_float(s[0]), __uint_as_float(s[1]));
}
DI float dpp_xor1(float x) { return __int_as_float(__builtin_amdgcn_update_dpp(0, __float_as_int(x), 0xB1, 0xF, 0xF, true)); }
DI float dpp_xor2(float x) { return __int_as_float(__builtin_amdgcn_update_dpp(0, __float_as_int(x), 0x4E, 0xF, 0xF, true)); }
DI f32x4 gelu4(f32x4 x) {
  const f32x4 a = x * (x * x * -0.1029432397f + -2.302208198f);
  f32x4 d;
#pragma unroll
  for (int e = 0; e < 4; ++e) d[e] = __builtin_amdgcn_exp2f(a[e]);
  d = d + 1.0f;
#pragma unroll
  for (int e = 0; e < 4; ++e) d[e] = __builtin_amdgcn_rcpf(d[e]);
  return x * d;
}
DI f32x2 mean_rstd(f32x2 t, float inv_n) {
  const float mu = t[0] * inv_n, var = fmaxf(t[1] * inv_n - mu * mu, 0.f);
  return (f32x2){mu, __builtin_amdgcn_rsqf(var + LN_EPS)};
}
DI u64 uniform64(u64 v) {
  return ((u64)(unsigned)__builtin_amdgcn_readfirstlane((int)(unsigned)(v >> 32)) << 32) | (unsigned)__builtin_amdgcn_readfirstlane((int)(unsigned)v);
}
DI int fresh_tid() { int t = threadIdx.x; asm volatile("" : "+v"(t)); return t; }
DI void set_flag(char* ws, unsigned flag_off, u64 tag) { __hip_atomic_store((gu64*)(ws + flag_off), tag, __ATOMIC_RELAXED, __HIP_MEMORY_SCOPE_AGENT); }
DI void await(char* ws, unsigned flag_off, u64 tag) {
#pragma unroll 1
  for (unsigned spin = 0; spin < (1u << 17); ++spin) {
    const u64 f = __hip_atomic_load((gu64*)(ws + flag_off), __ATOMIC_RELAXED, __HIP_MEMORY_SCOPE_AGENT);
    if (__all(f == tag)) break;
    __builtin_amdgcn_s_sleep(POLL_SLEEP);
  }
  asm volatile("" ::: "memory");
}
DI unsigned row_flags(unsigned f, int pm, int lane) { return f + (pm * 16 + (lane & 15)) * 128; }
DI unsigned col_flags(unsigned f, int pn, int lane) { return f + ((lane & 15) * 16 + pn) * 128; }
DI void gather16(u32x4 (&d)[4], i32x4 srd, unsigned base, int slot, int lane) {
#pragma unroll
  for (int j = 0; j < 4; ++j) ld16_async(d[j], srd, base + ((lane >> 4) * 4 + j) * 1024 + slot * 16);
}
DI f32x4 reduce16(u32x4 (&d)[4]) {
  f32x4 t = {0.f, 0.f, 0.f, 0.f};
#pragma unroll
  for (int j = 0; j < 4; ++j) { pin(d[j]); t += __builtin_bit_cast(f32x4, d[j]); }
  return (f32x4){sum4(t[0]), sum4(t[1]), sum4(t[2]), sum4(t[3])};
}
DI void put_rows(LAS char* lds, unsigned l_mu, unsigned l_rs, int row2, f32x4 t, float inv_n, bool on) {
  const f32x2 a = mean_rstd((f32x2){t[0], t[1]}, inv_n), b = mean_rstd((f32x2){t[2], t[3]}, inv_n);
  if (on) { *(LAS f32x2*)(lds + l_mu + row2 * 8) = (f32x2){a[0], b[0]}; *(LAS f32x2*)(lds + l_rs + row2 * 8) = (f32x2){a[1], b[1]}; }
}

DI int b_pos_lane(int lane) { const int g = lane >> 4, q = (lane & 15) >> 2; return (g >> 1) * 16 + (g & 1) * 4 + q; }
DI unsigned b128_base(int lane, int nb) { const int pos = b_pos_lane(lane); return pos * 128 + ((nb ^ ((pos >> 1) & 3)) * 32) + (lane & 3) * 8; }
DI unsigned b224_base(int lane) { return b_pos_lane(lane) * 224 + (lane & 3) * 8; }
DI unsigned a_base(int lane, int kk) { const int r = lane & 15, g = lane >> 4; return r * 128 + (((kk * 4 + g) ^ ((r >> 1) & 7)) * 16); }
DI unsigned img_off(int row, int chunk) { return row * 128 + ((chunk ^ ((row >> 1) & 7)) * 16); }

#define SGB(mask, n) __builtin_amdgcn_sched_group_barrier(mask, n, 0)
template <int NR, int NM> DI void interleave() {
  constexpr int RP = NR > NM - 2 ? 2 : 1;
  SGB(0x8, 1);
  SGB(0x2, 8);
#pragma unroll
  for (int i = 0; i < (NR + RP - 1) / RP; ++i) { SGB(0x100, RP); SGB(0x8, 1); }
  SGB(0x8, NM);
}
DI void cbar() {
  __builtin_amdgcn_sched_barrier(0);
  __builtin_amdgcn_s_waitcnt(0xC07F);
  bar();
  __builtin_amdgcn_sched_barrier(0);
}
template <int NK, int NS, int SA, int SB, int NR, int NM, class F, class L, class M>
DI void compute_loop(L load, M mma) {
  F f0, f1;
  bar();
  load(f0, 0u, 0u, 0);
  unsigned sa = 0, sb = 0;
  __builtin_amdgcn_sched_barrier(0);
#pragma unroll 1
  for (int t = 0; t < NK - 1; ++t) {
    load(f1, sa, sb, 1);
    mma(f0);
    interleave<NR, NM>();
    cbar();
    sa += SA; sb += SB; if (sa == NS * SA) { sa = 0; sb = 0; }
    load(f0, sa, sb, 0);
    mma(f1);
    interleave<NR, NM>();
    __builtin_amdgcn_sched_barrier(0);
  }
  load(f1, sa, sb, 1);
  mma(f0);
  interleave<NR, NM>();
  cbar();
  mma(f1);
}

template <int NK, int NS, int SA, int SB, int NR, int NM, class F, class L, class M>
DI void compute_deep(L load, M mma) {
  F f0, f1;
  bar();
  load(f0, 0u, 0u);
  unsigned sa = 0, sb = 0;
#pragma unroll 1
  for (int t = 0; t < NK - 2; t += 2) {
    cbar();
    sa += SA; sb += SB; if (sa == NS * SA) { sa = 0; sb = 0; }
    load(f1, sa, sb);
    mma(f0);
    interleave<NR, NM>();
    cbar();
    sa += SA; sb += SB; if (sa == NS * SA) { sa = 0; sb = 0; }
    load(f0, sa, sb);
    mma(f1);
    interleave<NR, NM>();
  }
  cbar();
  sa += SA; sb += SB; if (sa == NS * SA) { sa = 0; sb = 0; }
  load(f1, sa, sb);
  mma(f0);
  interleave<NR, NM>();
  cbar();
  mma(f1);
}

template <int NK_, int NS_, int XP_, int WP_> struct Pipe {
  static constexpr int NK = NK_, NS = NS_, XP = XP_, PW = XP_ + WP_;
  static constexpr int allowed(int t) {
    int n = 0;
    for (int s = t + 2; s <= t + NS - 1 && s < NK; ++s) n += s < NS ? XP : PW;
    return n;
  }
};
template <class P, int T, int EXTRA = 0, class IX, class IW> DI void hstep(IX& ix, IW& iw, int& slot) {
  wait_vm<P::allowed(T) + EXTRA>();
  bar();
  if constexpr (T + P::NS < P::NK) { ix(T + P::NS, slot); iw(T + P::NS, slot); }
  slot = slot + 1 == P::NS ? 0 : slot + 1;
}
template <class P, int T0, int T1, class IX, class IW> DI void hsteps(IX& ix, IW& iw, int& slot) {
  if constexpr (T0 < T1) {
    if constexpr (T0 >= P::NS - 2 && T1 <= P::NK - P::NS && T1 - T0 > 1) {
#pragma unroll 1
      for (int t = T0; t < T1; ++t) {
        wait_vm<(P::NS - 2) * P::PW>();
        bar();
        ix(t + P::NS, slot); iw(t + P::NS, slot);
        slot = slot + 1 == P::NS ? 0 : slot + 1;
      }
    } else if constexpr (T0 >= P::NS - 2 && T0 + 1 < P::NK - P::NS && T1 > P::NK - P::NS) {
      hsteps<P, T0, P::NK - P::NS>(ix, iw, slot);
      hsteps<P, P::NK - P::NS, T1>(ix, iw, slot);
    } else {
      hstep<P, T0>(ix, iw, slot);
      hsteps<P, T0 + 1, T1>(ix, iw, slot);
    }
  }
}
template <class P, class IX> DI void hstart(IX& ix) {
  ix(0, 0); ix(1, 1);
  wait_vm<P::XP>();
  bar();
#pragma unroll
  for (int s = 2; s < P::NS; ++s) ix(s, s);
}
DI int rot(int t, int r0, int nk) { const int k = t + r0; return k >= nk ? k - nk : k; }

namespace g1 { constexpr int NK = 16, NS = 3, AB = 144 * 128, ST = AB + 3 * 8192; struct Fr { bf16x8 a[5]; u32x2 b[6][2]; }; }
namespace gs { constexpr int NS = 4, AB = 16384, ST = AB + 8192; struct Fr { bf16x8 a[2][4]; u32x2 b[2][2][2]; }; }
namespace g3 { constexpr int NK = 16, NS = 3, AB = 16384, SEG = 14336, ST = AB + 2 * SEG; struct Fr { bf16x8 a[4]; u32x2 b[7][2]; }; }
namespace g4 { constexpr int NK = 32, NS = 3, AB = 16384, SEG = 14336, B0 = NS * AB; struct Fr { bf16x8 a[2][2]; u32x2 b[2][7][2]; }; }

__global__ __launch_bounds__(512, 2) void k_mega(const __bf16* __restrict__ x, const __bf16* __restrict__ wqkv, const __bf16* __restrict__ wo,
                                                 const __bf16* __restrict__ win, const __bf16* __restrict__ wsp, const __bf16* __restrict__ bsp,
                                                 const __bf16* __restrict__ wout, char* __restrict__ out, char* __restrict__ ws) {
  extern __shared__ __attribute__((aligned(16))) char smem_[];
  LAS char* lds = (LAS char*)smem_;
  const int tid = threadIdx.x, lane = tid & 63, wid = __builtin_amdgcn_readfirstlane(tid >> 6);
  const int xcd = blockIdx.x & 7, idx = blockIdx.x >> 3;
  const int pm1 = (idx & 7) * 2 + (xcd & 1), pn1 = (idx >> 3) * 4 + (xcd >> 1);
  const int pm2 = (idx & 3) * 4 + (xcd & 3), pn2 = (idx >> 2) * 2 + (xcd >> 2);
  const i32x4 srdW = make_srd(ws, WS_END);
  TS(0);

  if (wid >= 4) {
    const int hw = wid - 4, ht = tid - 256;
    int slot;
    u64 tag;
    auto nop2 = [](int, int) {};
    {
      using namespace g1;
      using P = Pipe<NK, NS, 10, 0>;
      const i32x4 srdA = make_srd(x, SEQ * DM * 2), srdB = make_srd(wqkv, DM * 3 * DM * 2);
      const int r0 = (pm1 + pn1) & 15;
      unsigned va[5], vb[6];
#pragma unroll
      for (int i = 0; i < 5; ++i) {
        const int p = i < 4 ? hw * 4 + i : 16 + (hw & 1);
        const int r = p * 8 + (lane >> 3);
        int grow = r < 128 ? pm1 * 128 + r : pm1 * 128 - 16 + (r - 128);
        grow = grow < 0 ? 0 : grow;
        va[i] = grow * (DM * 2) + (((lane & 7) ^ ((r >> 1) & 7)) * 16);
      }
#pragma unroll
      for (int i = 0; i < 6; ++i) {
        const int p = hw * 6 + i, seg = p >> 3, pos = (p & 7) * 8 + (lane >> 3), nb = ((lane & 7) >> 1) ^ ((pos >> 1) & 3);
        vb[i] = swap23(pos) * (3 * DM * 2) + (seg * DM + pn1 * 64 + nb * 16 + (lane & 1) * 8) * 2;
      }
      auto ix = [&](int t, int s) {
        const unsigned kt = __builtin_amdgcn_readfirstlane(t == 0 ? r0 : (t - 1 < r0 ? t - 1 : t)), so = s * ST;
#pragma unroll
        for (int i = 0; i < 4; ++i) dma16<false>(srdA, va[i], kt * 128u, so + (hw * 4 + i) * 1024u);
        if (hw < 2) dma16<false>(srdA, va[4], kt * 128u, so + (16 + hw) * 1024u);
#pragma unroll
        for (int i = 0; i < 6; ++i) dma16<false>(srdB, vb[i], kt * (64u * 3 * DM * 2), so + AB + (hw * 6 + i) * 1024u);
      };
      hstart<P>(ix);
      slot = 0;
      tag = uniform64(*(LAS u64*)(lds + L_TAG));
      u32x4 ra[4]; u32x2 rb[3][4];
      {
        const int row = ht >> 1;
#pragma unroll
        for (int j = 0; j < 4; ++j) ra[j] = *(LAS u32x4*)(lds + img_off(row, (ht & 1) * 4 + j));
#pragma unroll
        for (int i = 0; i < 3; ++i) {
          const int n = hw * 3 + i;
          const unsigned cb = AB + (n >> 2) * 8192 + b128_base(lane, n & 3);
#pragma unroll
          for (int j = 0; j < 4; ++j) rb[i][j] = lds_tr(lds + cb + ((j >> 1) * 32 + (j & 1) * 8) * 128);
        }
      }
      wait_lgkm0();
      hstep<P, 0>(ix, nop2, slot);
      {
        f32x2 s = {0.f, 0.f}, q = {0.f, 0.f};
#pragma unroll
        for (int j = 0; j < 4; ++j)
#pragma unroll
          for (int e = 0; e < 4; ++e) { const f32x2 v = unpk2(ra[j][e]); s += v; q += v * v; }
        float s1 = s[0] + s[1], q1 = q[0] + q[1];
        s1 += dpp_xor1(s1); q1 += dpp_xor1(q1);
        st8_dev(srdW, (ht & 1) == 0 ? WS_S1R + (pm1 * 16 + r0) * 1024 + (ht >> 1) * 8 : 0xfffffff0u, (u32x2){__float_as_uint(s1), __float_as_uint(q1)});
        float c1[3];
#pragma unroll
        for (int i = 0; i < 3; ++i) {
          f32x2 c = {0.f, 0.f};
#pragma unroll
          for (int j = 0; j < 4; ++j) c += unpk2(rb[i][j][0]) + unpk2(rb[i][j][1]);
          c1[i] = sum4(c[0] + c[1]);
        }
        st4_dev(srdW, WS_S1C + (pn1 * 16 + r0) * 1024 + ht * 4, __float_as_uint(lane < 16 ? c1[0] : lane < 32 ? c1[1] : c1[2]));
      }
      hstep<P, 1, 2>(ix, nop2, slot);
      hstep<P, 2>(ix, nop2, slot);
      st8_dev(srdW, ht == 0 ? F_S1 + (pm1 * 16 + pn1) * 128 : 0xfffffff0u, __builtin_bit_cast(u32x2, tag));
      hstep<P, 3, 1>(ix, nop2, slot);
      hsteps<P, 4, 8>(ix, nop2, slot);
      const int pmh = pm1 > 0 ? pm1 - 1 : 0;
      const unsigned fo = lane < 16 ? row_flags(F_S1, pm1, lane) : lane < 32 ? row_flags(F_S1, pmh, lane) : col_flags(F_S1, pn1, lane);
      u32x2 f0;
      ld8_async(f0, srdW, fo);
      hstep<P, 8, 1>(ix, nop2, slot);
      hstep<P, 9>(ix, nop2, slot);
      pin(f0);
      if (!__all(__builtin_bit_cast(u64, f0) == tag)) await(ws, fo, tag);
      u32x4 gr[4], gh[4], gc[4];
      gather16(gr, srdW, WS_S1R + pm1 * 16384, hw * 16 + (lane & 15), lane);
      gather16(gh, srdW, WS_S1R + pmh * 16384, 56 + (lane & 7), lane);
      gather16(gc, srdW, WS_S1C + pn1 * 16384, hw * 16 + (lane & 15), lane);
      hstep<P, 10, 12>(ix, nop2, slot);
      hstep<P, 11, 12>(ix, nop2, slot);
      hstep<P, 12>(ix, nop2, slot);
      put_rows(lds, L_MU, L_RS, hw * 16 + (lane & 15), reduce16(gr), 1.f / DM, lane < 16);
      put_rows(lds, L_MU, L_RS, 64 + (lane & 7), reduce16(gh), 1.f / DM, ht < 8);
      {
        const f32x4 c = reduce16(gc);
        if (lane < 12) *(LAS f32x4*)(lds + L_CS + (hw * 48 + lane * 4) * 4) = c;
      }
      wait_lgkm0();
      hsteps<P, 13, 16>(ix, nop2, slot);
    }
    TS(1);
    unsigned vbs[2];
#pragma unroll
    for (int i = 0; i < 2; ++i) {
      const int pos = (hw * 2 + i) * 8 + (lane >> 3), nb = ((lane & 7) >> 1) ^ ((pos >> 1) & 3);
      vbs[i] = swap23(pos) * (DM * 2) + (pn2 * 64 + nb * 16 + (lane & 1) * 8) * 2;
    }
    const unsigned vlin = hw * 4096 + lane * 16;
    const i32x4 srdWin = make_srd(win, DM * FF * 2);
    unsigned vb3[7];
#pragma unroll
    for (int i = 0; i < 7; ++i) {
      const int p = hw * 7 + i, seg = p / 14, q = (p % 14) * 64 + lane, pos = q / 14, gi = q % 14;
      vb3[i] = swap23(pos) * (FF * 2) + (seg * FH + pn1 * 112 + gi * 8) * 2;
    }
    {
      using namespace gs;
      constexpr int NK = 16;
      using P = Pipe<NK, NS, 4, 2>;
      const i32x4 srdB = make_srd(wo, DM * DM * 2), srdX = make_srd(x, SEQ * DM * 2);
      const int r0 = (((idx & 3) + (idx >> 2)) % ROT2) * (NK / ROT2);
      auto ix = [&](int t, int s) {
        const unsigned kt = (t + r0) & 15;
#pragma unroll
        for (int i = 0; i < 4; ++i) dma16<true>(srdW, vlin, WS_O + (pm2 * 16 + kt) * 16384u + i * 1024u, s * ST + (hw * 4 + i) * 1024u);
      };
      auto iw = [&](int t, int s) {
        const unsigned kt = (t + r0) & 15;
#pragma unroll
        for (int i = 0; i < 2; ++i) dma16<false>(srdB, vbs[i], kt * (64u * DM * 2), s * ST + AB + (hw * 2 + i) * 1024u);
      };
#pragma unroll
      for (int s = 0; s < NS; ++s) iw(s, s);
#pragma unroll
      for (int i = 0; i < 7; ++i) dma16<false>(srdWin, vb3[i], pm1 * (64u * FF * 2), L_ZS + (hw * 7 + i) * 1024u);
      bar();
      bar();
      wait_vm<0>();
      {
        const unsigned cb = L_ZS + hw * 7168 + b224_base(lane);
#pragma unroll
        for (int n = 0; n < 7; ++n) {
          const u32x2 a = lds_tr(lds + cb + n * 32), b = lds_tr(lds + cb + n * 32 + 8 * 224);
          const f32x2 c = (unpk2(a[0]) + unpk2(a[1])) + (unpk2(b[0]) + unpk2(b[1]));
          *(LAS float*)(lds + L_C2 + ((hw & 1) * 224 + (hw >> 1) * 112 + n * 16 + (lane & 15)) * 4) = sum4(c[0] + c[1]);
        }
      }
      u32x4 wt[7];
#pragma unroll
      for (int i = 0; i < 7; ++i) wt[i] = *(LAS u32x4*)(lds + L_ZS + (hw * 7 + i) * 1024 + lane * 16);
      wait_lgkm0();
      bar();
#pragma unroll
      for (int i = 0; i < 7; ++i) st16_dev(srdW, WS_W3 + (pn1 * 16 + pm1) * 28672 + (hw * 7 + i) * 1024 + lane * 16, wt[i]);
      TS(2);
      if (hw == 0) await(ws, row_flags(F_O, pm2, lane), tag);
      bar();
      TS(3);
      hstart<P>(ix);
      slot = 0;
#pragma unroll
      for (int i = 0; i < 4; ++i) {
        const int r = (hw * 4 + i) * 8 + (lane >> 3);
        dma16<false>(srdX, (pm2 * 128 + r) * (DM * 2) + pn2 * 128 + (((lane & 7) ^ ((r >> 1) & 7)) * 16), 0u, L_H + (hw * 4 + i) * 1024u);
      }
      {
        const int c = ht < 224 ? ht : 0;
        const float v = *(LAS float*)(lds + L_C2 + c * 4) + *(LAS float*)(lds + L_C2 + (224 + c) * 4);
        st4_dev(srdW, WS_S2C + (pn1 * 16 + pm1) * 1024 + ht * 4, __float_as_uint(v));
      }
      hstep<P, 0, 5>(ix, iw, slot);
      hstep<P, 1, 5>(ix, iw, slot);
      hstep<P, 2, 5>(ix, iw, slot);
      hstep<P, 3>(ix, iw, slot);
      st8_dev(srdW, ht == 0 ? F_W3 + (pm1 * 16 + pn1) * 128 : 0xfffffff0u, __builtin_bit_cast(u32x2, tag));
      hstep<P, 4, 1>(ix, iw, slot);
      hstep<P, 5, 1>(ix, iw, slot);
      hsteps<P, 6, 10>(ix, iw, slot);
      u32x2 f0;
      const unsigned fo = col_flags(F_W3, pn1, lane);
      ld8_async(f0, srdW, fo);
      hstep<P, 10, 1>(ix, iw, slot);
      hstep<P, 11, 1>(ix, iw, slot);
      hsteps<P, 12, NK>(ix, iw, slot);
      pin(f0);
      if (!__all(__builtin_bit_cast(u64, f0) == tag)) await(ws, fo, tag);
    }
    TS(4);
    unsigned va4[4];
#pragma unroll
    for (int i = 0; i < 4; ++i) { const int r = (hw * 4 + i) * 8 + (lane >> 3); va4[i] = (pm1 * 128 + r) * (SEQ * 2) + (((lane & 7) ^ ((r >> 1) & 7)) * 16); }
    {
      using namespace g3;
      using P = Pipe<NK, NS, 4, 7>;
      const int r0 = (((idx & 7) + (idx >> 3)) % ROT3) * (NK / ROT3);
      auto ix = [&](int t, int s) {
        const unsigned kt = (t + r0) & 15;
#pragma unroll
        for (int i = 0; i < 4; ++i) dma16<true>(srdW, vlin, WS_H + (pm1 * 16 + kt) * 16384u + i * 1024u, s * ST + (hw * 4 + i) * 1024u);
      };
      auto iw = [&](int t, int s) {
        const unsigned kt = (t + r0) & 15;
#pragma unroll
        for (int i = 0; i < 7; ++i) dma16<true>(srdW, lane * 16, WS_W3 + (pn1 * 16 + kt) * 28672u + (hw * 7 + i) * 1024u, s * ST + AB + (hw * 7 + i) * 1024u);
      };
#pragma unroll
      for (int s = 0; s < NS; ++s) iw(s, s);
      bar();
      bar();
      TS(5);
      if (hw == 0) await(ws, row_flags(F_H, pm1, lane), tag);
      bar();
      TS(6);
      hstart<P>(ix);
      slot = 0;
      u32x4 gr[4], gc[4];
      gather16(gr, srdW, WS_S2R + pm1 * 16384, hw * 16 + (lane & 15), lane);
      gather16(gc, srdW, WS_S2C + pn1 * 16384, hw * 16 + (lane & 15), lane);
      hstep<P, 0, 8>(ix, iw, slot);
      hstep<P, 1, 8>(ix, iw, slot);
      hstep<P, 2>(ix, iw, slot);
      put_rows(lds, L_MU, L_RS, hw * 16 + (lane & 15), reduce16(gr), 1.f / DM, lane < 16);
      {
        const f32x4 c = reduce16(gc);
        if (lane < 16 && hw * 16 + lane < 56) *(LAS f32x4*)(lds + L_CS + (hw * 16 + lane) * 16) = c;
      }
      wait_lgkm0();
      hsteps<P, 3, NK>(ix, iw, slot);
    }
    TS(7);
    {
      using namespace g4;
      using P = Pipe<NK, NS, 3, 4>;
      const i32x4 srdA = make_srd(wsp, SEQ * SEQ * 2);
      const int r0 = (((idx & 7) + (idx >> 3)) % ROT4) * (NK / ROT4);
      const unsigned vb = lane * 16;
      auto ix = [&](int t, int s) {
        const unsigned src = WS_Z2 + (pn1 * 32 + ((t + r0) & 31)) * (unsigned)SEG, dst = B0 + s * SEG;
#pragma unroll
        for (int i = 0; i < 3; ++i) dma16<true>(srdW, vb, src + (hw * 3 + i) * 1024u, dst + (hw * 3 + i) * 1024u);
        if (hw < 2) dma16<true>(srdW, vb, src + (12 + hw) * 1024u, dst + (12 + hw) * 1024u);
      };
      auto iw = [&](int t, int s) {
        const unsigned kt = (t + r0) & 31;
#pragma unroll
        for (int i = 0; i < 4; ++i) dma16<false>(srdA, va4[i], kt * 128u, s * AB + (hw * 4 + i) * 1024u);
      };
#pragma unroll
      for (int s = 0; s < NS; ++s) iw(s, s);
      bar();
      if (hw == 0) {
        const f32x4 p = *(LAS f32x4*)(lds + L_P + lane * 16) + *(LAS f32x4*)(lds + L_P + 1024 + lane * 16);
        st16_dev(srdW, WS_S3 + (pm1 * 16 + pn1) * 1024 + lane * 16, __builtin_bit_cast(u32x4, p));
        wait_vm<0>();
        if (lane == 0) set_flag(ws, F_S3 + (pm1 * 16 + pn1) * 128, tag);
      }
      await(ws, row_flags(F_S3, pm1, lane), tag);
      {
        u32x4 gr[4];
        gather16(gr, srdW, WS_S3 + pm1 * 16384, hw * 16 + (lane & 15), lane);
        wait_vm<0>();
        put_rows(lds, L_M3, L_R3, hw * 16 + (lane & 15), reduce16(gr), 1.f / FH, lane < 16);
        wait_lgkm0();
      }
      bar();
      bar();
      bar();
      TS(8);
      if (hw == 0) await(ws, col_flags(F_Z2, pn1, lane), tag);
      bar();
      TS(9);
      hstart<P>(ix);
      slot = 0;
      hsteps<P, 0, NK>(ix, iw, slot);
    }
    TS(10);
    {
      using namespace gs;
      constexpr int NK = 28;
      using P = Pipe<NK, NS, 4, 2>;
      const i32x4 srdB = make_srd(wout, FH * DM * 2);
      const int r0 = (((idx & 3) + (idx >> 2)) % ROT5) * (NK / ROT5);
      auto ix = [&](int t, int s) {
        const unsigned kt = rot(t, r0, NK);
#pragma unroll
        for (int i = 0; i < 4; ++i) dma16<true>(srdW, vlin, WS_Y + (pm2 * 28 + kt) * 16384u + i * 1024u, s * ST + (hw * 4 + i) * 1024u);
      };
      auto iw = [&](int t, int s) {
        const unsigned kt = rot(t, r0, NK);
#pragma unroll
        for (int i = 0; i < 2; ++i) dma16<false>(srdB, vbs[i], kt * (64u * DM * 2), s * ST + AB + (hw * 2 + i) * 1024u);
      };
#pragma unroll
      for (int s = 0; s < NS; ++s) iw(s, s);
      bar();
      bar();
      TS(11);
      if (hw == 0) await(ws, row_flags(F_Y, pm2, lane), tag);
      bar();
      TS(12);
      hstart<P>(ix);
      slot = 0;
      hsteps<P, 0, NK>(ix, iw, slot);
      bar();
    }
    TS(13);
    return;
  }

  const f32x4 zero4 = {0.f, 0.f, 0.f, 0.f};
  const u64 tag = uniform64(__hip_atomic_load((gu64*)(ws + WS_EPOCH), __ATOMIC_RELAXED, __HIP_MEMORY_SCOPE_AGENT)) + 1;
  if (tid == 0) *(LAS u64*)(lds + L_TAG) = tag;
  wait_lgkm0();
#define LANES const int tid = fresh_tid(), lane = tid & 63, i16 = lane & 15, g = lane >> 4; (void)i16; (void)g
  auto publish = [&](unsigned flag) {
    wait_vm<0>();
    bar();
    if (threadIdx.x == 0) set_flag(ws, flag, tag);
    bar();
  };
  auto copy16k = [&](unsigned dst) {
    LANES;
#pragma unroll
    for (int i = 0; i < 4; ++i) st16_dev(srdW, dst + (i * 256 + tid) * 16, *(LAS u32x4*)(lds + L_H + (i * 256 + tid) * 16));
  };
  {
    using namespace g1;
    LANES;
    const int hd = wid & 1, rh = wid >> 1;
    unsigned ab[2], hb[2], bb[2];
#pragma unroll
    for (int kk = 0; kk < 2; ++kk) { ab[kk] = a_base(lane, kk) + rh * 64 * 128; hb[kk] = a_base(lane, kk) + 128 * 128; bb[kk] = AB + b128_base(lane, hd * 2 + kk); }
    f32x4 aq[4][2], ak[4][2], av[4][2], ah[2];
#pragma unroll
    for (int m = 0; m < 4; ++m)
#pragma unroll
      for (int j = 0; j < 2; ++j) aq[m][j] = ak[m][j] = av[m][j] = zero4;
    ah[0] = ah[1] = zero4;
    auto load = [&](Fr& f, unsigned so, unsigned, int kk) {
#pragma unroll
      for (int m = 0; m < 4; ++m) f.a[m] = lds_a(lds + so + ab[kk] + m * 2048);
      f.a[4] = lds_a(lds + so + hb[kk]);
#pragma unroll
      for (int s = 0; s < 3; ++s)
#pragma unroll
        for (int j = 0; j < 2; ++j)
#pragma unroll
          for (int hh = 0; hh < 2; ++hh) f.b[s * 2 + j][hh] = lds_tr(lds + so + bb[j] + s * 8192 + (kk * 32 + hh * 8) * 128);
    };
    auto mma2 = [&](Fr& f, auto RH) {
      bf16x8 b[6];
#pragma unroll
      for (int i = 0; i < 6; ++i) b[i] = cat8(f.b[i][0], f.b[i][1]);
#pragma unroll
      for (int m = 0; m < 4; ++m)
#pragma unroll
        for (int j = 0; j < 2; ++j) {
          aq[m][j] = mfma(b[j], f.a[m], aq[m][j]);
          ak[m][j] = mfma(b[2 + j], f.a[m], ak[m][j]);
          av[m][j] = mfma(f.a[m], b[4 + j], av[m][j]);
        }
#pragma unroll
      for (int j = 0; j < 2; ++j) {
        if constexpr (decltype(RH)::value == 0) ah[j] = mfma(b[2 + j], f.a[4], ah[j]);
        else ah[j] = mfma(f.a[4], b[4 + j], ah[j]);
      }
    };
    if (rh == 0) compute_loop<NK, NS, ST, ST, 17, 26, Fr>(load, [&](Fr& f) { mma2(f, IC<0>{}); });
    else compute_loop<NK, NS, ST, ST, 17, 26, Fr>(load, [&](Fr& f) { mma2(f, IC<1>{}); });
    TS(1);
    f32x4 csq[2], csk[2]; float csv[2];
#pragma unroll
    for (int j = 0; j < 2; ++j) {
      csq[j] = *(LAS f32x4*)(lds + L_CS + (hd * 32 + j * 16 + 4 * g) * 4);
      csk[j] = *(LAS f32x4*)(lds + L_CS + (64 + hd * 32 + j * 16 + 4 * g) * 4);
      csv[j] = *(LAS float*)(lds + L_CS + (128 + hd * 32 + j * 16 + i16) * 4);
    }
    bf16x8 Q[4], K[5]; u32x2 V[5][2];
#pragma unroll
    for (int m = 0; m < 4; ++m) {
      const int r = rh * 64 + m * 16;
      const float mu = *(LAS float*)(lds + L_MU + (r + i16) * 4), rs = *(LAS float*)(lds + L_RS + (r + i16) * 4);
      const f32x4 mu4 = *(LAS f32x4*)(lds + L_MU + (r + 4 * g) * 4), rs4 = *(LAS f32x4*)(lds + L_RS + (r + 4 * g) * 4);
      Q[m] = cat8(pk4((aq[m][0] - mu * csq[0]) * rs), pk4((aq[m][1] - mu * csq[1]) * rs));
      K[m + 1] = cat8(pk4((ak[m][0] - mu * csk[0]) * rs), pk4((ak[m][1] - mu * csk[1]) * rs));
      V[m + 1][0] = pk4((av[m][0] - mu4 * csv[0]) * rs4);
      V[m + 1][1] = pk4((av[m][1] - mu4 * csv[1]) * rs4);
    }
    LAS char* xch = lds + L_X + hd * 3072 + lane * 16;
    if (rh == 0) {
      const float mu = *(LAS float*)(lds + L_MU + (128 + i16) * 4), rs = *(LAS float*)(lds + L_RS + (128 + i16) * 4);
      K[0] = cat8(pk4((ah[0] - mu * csk[0]) * rs), pk4((ah[1] - mu * csk[1]) * rs));
      *(LAS bf16x8*)(xch) = K[4];
      *(LAS bf16x8*)(xch + 1024) = cat8(V[4][0], V[4][1]);
    } else {
      const f32x4 mu4 = *(LAS f32x4*)(lds + L_MU + (128 + 4 * g) * 4), rs4 = *(LAS f32x4*)(lds + L_RS + (128 + 4 * g) * 4);
      *(LAS bf16x8*)(xch + 2048) = cat8(pk4((ah[0] - mu4 * csv[0]) * rs4), pk4((ah[1] - mu4 * csv[1]) * rs4));
      K[0] = K[4];
    }
    wait_lgkm0(); bar();
    {
      u32x4 v = *(LAS u32x4*)(xch + (rh == 0 ? 2048 : 1024));
      V[0][0] = (u32x2){v[0], v[1]}; V[0][1] = (u32x2){v[2], v[3]};
      if (rh == 1) K[0] = *(LAS bf16x8*)(xch);
    }
    constexpr float SC = 0.17677669529663687f * 1.4426950408889634f;
    f32x4 sp[4], sc[4];
#pragma unroll
    for (int m = 0; m < 4; ++m) { sp[m] = mfma(K[m], Q[m], zero4); sc[m] = mfma(K[m + 1], Q[m], zero4); }
    f32x4 mp, mc;
#pragma unroll
    for (int r = 0; r < 4; ++r) {
      const int dc = i16 - (4 * g + r);
      mp[r] = ((dc & 1) == 0 && dc <= -2) ? 0.f : -INFINITY;
      mc[r] = ((dc & 1) == 0 && dc >= 0) ? 0.f : -INFINITY;
    }
    const float nohalo = (pm1 == 0 && rh == 0) ? -INFINITY : 0.f;
    float mx[4], sum[4];
#pragma unroll
    for (int m = 0; m < 4; ++m) {
      sp[m] = sp[m] * SC + (m == 0 ? mp + nohalo : mp);
      sc[m] = sc[m] * SC + mc;
      mx[m] = fmaxf(fmaxf(fmaxf(sp[m][0], sp[m][1]), fmaxf(sp[m][2], sp[m][3])), fmaxf(fmaxf(sc[m][0], sc[m][1]), fmaxf(sc[m][2], sc[m][3])));
    }
#pragma unroll
    for (int m = 0; m < 4; ++m) mx[m] = max4(mx[m]);
#pragma unroll
    for (int m = 0; m < 4; ++m) {
      sp[m] -= mx[m]; sc[m] -= mx[m];
#pragma unroll
      for (int r = 0; r < 4; ++r) { sp[m][r] = __builtin_amdgcn_exp2f(sp[m][r]); sc[m][r] = __builtin_amdgcn_exp2f(sc[m][r]); }
      const f32x4 t = sp[m] + sc[m];
      sum[m] = (t[0] + t[1]) + (t[2] + t[3]);
    }
#pragma unroll
    for (int m = 0; m < 4; ++m) sum[m] = __builtin_amdgcn_rcpf(sum4(sum[m]));
    f32x4 o[4][2];
#pragma unroll
    for (int m = 0; m < 4; ++m) {
      const bf16x8 P = cat8(pk4(sp[m] * sum[m]), pk4(sc[m] * sum[m]));
#pragma unroll
      for (int db = 0; db < 2; ++db) o[m][db] = mfma(cat8(V[m][db], V[m + 1][db]), P, zero4);
    }
#pragma unroll
    for (int m = 0; m < 4; ++m)
#pragma unroll
      for (int db = 0; db < 2; ++db)
        *(LAS u32x2*)(lds + L_H + img_off(rh * 64 + m * 16 + i16, hd * 4 + db * 2 + (g >> 1)) + (g & 1) * 8) = pk4(o[m][db]);
    wait_lgkm0(); bar();
    TS(2);
    copy16k(WS_O + (pm1 * 16 + pn1) * 16384);
    TS(3);
    publish(F_O + (pm1 * 16 + pn1) * 128);
  }
  const int wc = wid & 1, wr = wid >> 1;
  auto small_gemm = [&](auto NKC, auto STATS) {
    using namespace gs;
    LANES;
    unsigned ab[2], bb[2];
#pragma unroll
    for (int kk = 0; kk < 2; ++kk) { ab[kk] = a_base(lane, kk) + wr * 64 * 128; bb[kk] = AB + b128_base(lane, wc * 2 + kk); }
    f32x4 acc[4][2];
#pragma unroll
    for (int m = 0; m < 4; ++m) acc[m][0] = acc[m][1] = zero4;
    auto load = [&](Fr& f, unsigned so, unsigned) {
#pragma unroll
      for (int kk = 0; kk < 2; ++kk) {
#pragma unroll
        for (int j = 0; j < 2; ++j)
#pragma unroll
          for (int hh = 0; hh < 2; ++hh) f.b[kk][j][hh] = lds_tr(lds + so + bb[j] + (kk * 32 + hh * 8) * 128);
#pragma unroll
        for (int m = 0; m < 4; ++m) f.a[kk][m] = lds_a(lds + so + ab[kk] + m * 2048);
      }
    };
    auto mma = [&](Fr& f) {
#pragma unroll
      for (int kk = 0; kk < 2; ++kk)
#pragma unroll
        for (int m = 0; m < 4; ++m)
#pragma unroll
          for (int j = 0; j < 2; ++j) acc[m][j] = mfma(cat8(f.b[kk][j][0], f.b[kk][j][1]), f.a[kk][m], acc[m][j]);
    };
    compute_deep<decltype(NKC)::value, NS, ST, ST, 16, 16, Fr>(load, mma);
    u32x2 res[4][2];
#pragma unroll
    for (int m = 0; m < 4; ++m)
#pragma unroll
      for (int j = 0; j < 2; ++j) res[m][j] = *(LAS u32x2*)(lds + L_H + img_off(wr * 64 + m * 16 + i16, wc * 4 + j * 2 + (g >> 1)) + (g & 1) * 8);
#pragma unroll
    for (int m = 0; m < 4; ++m)
#pragma unroll
      for (int j = 0; j < 2; ++j) {
        acc[m][j] += unpk4(res[m][j]);
        *(LAS u32x2*)(lds + L_H + img_off(wr * 64 + m * 16 + i16, wc * 4 + j * 2 + (g >> 1)) + (g & 1) * 8) = pk4(acc[m][j]);
      }
    if constexpr (decltype(STATS)::value) {
      float s1[4], q1[4];
#pragma unroll
      for (int m = 0; m < 4; ++m) {
        const f32x4 sv = acc[m][0] + acc[m][1], qv = acc[m][0] * acc[m][0] + acc[m][1] * acc[m][1];
        s1[m] = (sv[0] + sv[1]) + (sv[2] + sv[3]); q1[m] = (qv[0] + qv[1]) + (qv[2] + qv[3]);
      }
#pragma unroll
      for (int m = 0; m < 4; ++m) { s1[m] = sum4(s1[m]); q1[m] = sum4(q1[m]); }
#pragma unroll
      for (int m = 0; m < 4; ++m) *(LAS f32x2*)(lds + L_P + (wc * 128 + wr * 64 + m * 16 + i16) * 8) = (f32x2){s1[m], q1[m]};
    }
    wait_lgkm0(); bar();
  };
  small_gemm(IC<16>{}, IC<1>{});
  TS(5);
  copy16k(WS_H + (pm2 * 16 + pn2) * 16384);
  if (wid == 0) {
    const f32x4 p = *(LAS f32x4*)(lds + L_P + lane * 16) + *(LAS f32x4*)(lds + L_P + 1024 + lane * 16);
    st16_dev(srdW, WS_S2R + (pm2 * 16 + pn2) * 1024 + lane * 16, __builtin_bit_cast(u32x4, p));
  }
  TS(6);
  publish(F_H + (pm2 * 16 + pn2) * 128);
  u32x2 z1[2][7];
  {
    using namespace g3;
    LANES;
    unsigned ab[2];
#pragma unroll
    for (int kk = 0; kk < 2; ++kk) ab[kk] = a_base(lane, kk) + wr * 64 * 128;
    const unsigned bP = AB + b224_base(lane) + SEG + wc * 96, bM = AB + b224_base(lane) + (wc == 0 ? 96 : SEG + 192), bQ = AB + b224_base(lane) + wc * 128;
    f32x4 acc[4][7];
#pragma unroll
    for (int m = 0; m < 4; ++m)
#pragma unroll
      for (int j = 0; j < 7; ++j) acc[m][j] = zero4;
    auto load = [&](Fr& f, unsigned so, unsigned, int kk) {
#pragma unroll
      for (int m = 0; m < 4; ++m) f.a[m] = lds_a(lds + so + ab[kk] + m * 2048);
#pragma unroll
      for (int j = 0; j < 7; ++j)
#pragma unroll
        for (int hh = 0; hh < 2; ++hh) f.b[j][hh] = lds_tr(lds + so + (j < 3 ? bP + j * 32 : j == 3 ? bM : bQ + (j - 4) * 32) + (kk * 32 + hh * 8) * 224);
    };
    auto mma = [&](Fr& f) {
#pragma unroll
      for (int m = 0; m < 4; ++m)
#pragma unroll
        for (int j = 0; j < 7; ++j) acc[m][j] = mfma(cat8(f.b[j][0], f.b[j][1]), f.a[m], acc[m][j]);
    };
    compute_loop<NK, NS, ST, ST, 18, 28, Fr>(load, mma);
    __builtin_amdgcn_sched_barrier(0);
    TS(7);
    float k1 = -2.302208198f, k2 = -0.1029432397f;
    pin(k1); pin(k2);
    float mu[4], rs[4];
#pragma unroll
    for (int m = 0; m < 4; ++m) { mu[m] = *(LAS float*)(lds + L_MU + (wr * 64 + m * 16 + i16) * 4); rs[m] = *(LAS float*)(lds + L_RS + (wr * 64 + m * 16 + i16) * 4); }
    f32x4 cs[7];
#pragma unroll
    for (int j = 0; j < 7; ++j) {
      const int blk = j < 3 ? 7 + wc * 3 + j : j == 3 ? (wc == 0 ? 3 : 13) : wc * 4 + j - 4;
      cs[j] = *(LAS f32x4*)(lds + L_CS + (blk * 16 + 4 * g) * 4);
    }
    auto act = [&](int j) {
      f32x4 xv[4], e[4];
#pragma unroll
      for (int m = 0; m < 4; ++m) xv[m] = (acc[m][j] - mu[m] * cs[j]) * rs[m];
#pragma unroll
      for (int m = 0; m < 4; ++m) e[m] = xv[m] * xv[m];
#pragma unroll
      for (int m = 0; m < 4; ++m) e[m] = e[m] * k2 + k1;
#pragma unroll
      for (int m = 0; m < 4; ++m) e[m] = e[m] * xv[m];
#pragma unroll
      for (int m = 0; m < 4; ++m)
#pragma unroll
        for (int i = 0; i < 4; ++i) e[m][i] = __builtin_amdgcn_exp2f(e[m][i]);
#pragma unroll
      for (int m = 0; m < 4; ++m) e[m] = e[m] + 1.0f;
#pragma unroll
      for (int m = 0; m < 4; ++m)
#pragma unroll
        for (int i = 0; i < 4; ++i) e[m][i] = __builtin_amdgcn_rcpf(e[m][i]);
#pragma unroll
      for (int m = 0; m < 4; ++m) { acc[m][j] = xv[m] * e[m]; pin(acc[m][j]); }
      __builtin_amdgcn_sched_barrier(0);
    };
#pragma unroll
    for (int j = 0; j < 4; ++j) act(j);
    {
      const float w3 = wc == 0 ? 0.f : 1.f;
      float s1[4], q1[4];
#pragma unroll
      for (int m = 0; m < 4; ++m) {
        const f32x4 v3 = acc[m][3] * w3;
        const f32x4 sv = (acc[m][0] + acc[m][1]) + (acc[m][2] + v3);
        const f32x4 qv = acc[m][0] * acc[m][0] + acc[m][1] * acc[m][1] + acc[m][2] * acc[m][2] + v3 * v3;
        s1[m] = (sv[0] + sv[1]) + (sv[2] + sv[3]); q1[m] = (qv[0] + qv[1]) + (qv[2] + qv[3]);
      }
#pragma unroll
      for (int m = 0; m < 4; ++m) { s1[m] = sum4(s1[m]); q1[m] = sum4(q1[m]); }
#pragma unroll
      for (int m = 0; m < 4; ++m) *(LAS f32x2*)(lds + L_P + (wc * 128 + wr * 64 + m * 16 + i16) * 8) = (f32x2){s1[m], q1[m]};
    }
    wait_lgkm0(); bar();
    TS(8);
    LAS char* z1p = lds + L_Z1 + (wr * 64 + i16) * 232 + 8 * g;
    if (wc == 0) {
#pragma unroll
      for (int m = 0; m < 4; ++m) *(LAS u32x2*)(z1p + m * 16 * 232 + 96) = pk4(acc[m][3]);
    }
#pragma unroll
    for (int j = 4; j < 7; ++j) {
      act(j);
#pragma unroll
      for (int m = 0; m < 4; ++m) *(LAS u32x2*)(z1p + m * 16 * 232 + wc * 128 + (j - 4) * 32) = pk4(acc[m][j]);
    }
    TS(9);
    wait_lgkm0(); bar();
    TS(10);
    {
      float m3[4], r3[4];
#pragma unroll
      for (int m = 0; m < 4; ++m) { m3[m] = *(LAS float*)(lds + L_M3 + (wr * 64 + m * 16 + i16) * 4); r3[m] = *(LAS float*)(lds + L_R3 + (wr * 64 + m * 16 + i16) * 4); }
      LAS char* z2p = lds + L_ZS + (wr * 64 + swap23(i16)) * 240 + 8 * g + wc * 96;
#pragma unroll
      for (int m = 0; m < 4; ++m)
#pragma unroll
        for (int j = 0; j < 3; ++j) *(LAS u32x2*)(z2p + m * 16 * 240 + j * 32) = pk4((acc[m][j] - m3[m]) * r3[m]);
      if (wc == 1) {
#pragma unroll
        for (int m = 0; m < 4; ++m) *(LAS u32x2*)(z2p + m * 16 * 240 + 96) = pk4((acc[m][3] - m3[m]) * r3[m]);
      }
    }
#pragma unroll
    for (int m = 0; m < 2; ++m)
#pragma unroll
      for (int j = 0; j < 7; ++j) z1[m][j] = *(LAS u32x2*)(lds + L_Z1 + (wid * 32 + m * 16 + i16) * 232 + (j * 16 + 4 * g) * 2);
    wait_lgkm0(); bar();
    TS(11);
#pragma unroll
    for (int i = 0; i < 7; ++i) {
      const int c = i * 256 + tid, r = c / 14, ch = c - r * 14;
      st16_dev(srdW, WS_Z2 + (pn1 * 32 + pm1 * 2) * SEG + c * 16, *(LAS u32x4*)(lds + L_ZS + r * 240 + ch * 16));
    }
    TS(12);
    publish(F_Z2 + (pm1 * 16 + pn1) * 128);
  }
  {
    using namespace g4;
    LANES;
    unsigned ab[2];
#pragma unroll
    for (int kk = 0; kk < 2; ++kk) ab[kk] = a_base(lane, kk) + wid * 32 * 128;
    const unsigned bb = B0 + b224_base(lane);
    float bias[2];
#pragma unroll
    for (int m = 0; m < 2; ++m) bias[m] = (float)bsp[pm1 * 128 + wid * 32 + m * 16 + i16];
    f32x4 acc[2][7];
#pragma unroll
    for (int m = 0; m < 2; ++m)
#pragma unroll
      for (int j = 0; j < 7; ++j) acc[m][j] = zero4;
    auto load = [&](Fr& f, unsigned sa, unsigned sb) {
#pragma unroll
      for (int kk = 0; kk < 2; ++kk) {
#pragma unroll
        for (int m = 0; m < 2; ++m) f.a[kk][m] = lds_a(lds + sa + ab[kk] + m * 2048);
#pragma unroll
        for (int j = 0; j < 7; ++j)
#pragma unroll
          for (int hh = 0; hh < 2; ++hh) f.b[kk][j][hh] = lds_tr(lds + sb + bb + j * 32 + (kk * 32 + hh * 8) * 224);
      }
    };
    auto mma = [&](Fr& f) {
#pragma unroll
      for (int kk = 0; kk < 2; ++kk)
#pragma unroll
        for (int m = 0; m < 2; ++m)
#pragma unroll
          for (int j = 0; j < 7; ++j) acc[m][j] = mfma(cat8(f.b[kk][j][0], f.b[kk][j][1]), f.a[kk][m], acc[m][j]);
    };
    compute_deep<NK, NS, AB, SEG, 32, 28, Fr>(load, mma);
    TS(13);
#pragma unroll
    for (int m = 0; m < 2; ++m)
#pragma unroll
      for (int j = 0; j < 7; ++j)
        *(LAS u32x2*)(lds + L_ZS + (wid * 32 + m * 16 + i16) * 240 + (j * 16 + 4 * g) * 2) = pk4(unpk4(z1[m][j]) * (acc[m][j] + bias[m]));
    wait_lgkm0(); bar();
    TS(14);
#pragma unroll
    for (int i = 0; i < 7; ++i) {
      const int c = i * 256 + tid, r = c / 14, ch = c - r * 14, gc = pn1 * 14 + ch;
      st16_dev(srdW, WS_Y + (pm1 * 28 + (gc >> 3)) * 16384 + img_off(r, gc & 7), *(LAS u32x4*)(lds + L_ZS + r * 240 + ch * 16));
    }
    TS(15);
    publish(F_Y + (pm1 * 16 + pn1) * 128);
  }
  small_gemm(IC<28>{}, IC<0>{});
  TS(16);
#pragma unroll
  for (int i = 0; i < 4; ++i) {
    const int c = i * 256 + tid, r = c >> 3, ch = (c & 7) ^ ((r >> 1) & 7);
    *(u32x4*)(out + (size_t)(pm2 * 128 + r) * (DM * 2) + pn2 * 128 + ch * 16) = *(LAS u32x4*)(lds + L_H + c * 16);
  }
  if (blockIdx.x == 0 && tid == 0) __hip_atomic_store((gu64*)(ws + WS_EPOCH), tag, __ATOMIC_RELAXED, __HIP_MEMORY_SCOPE_AGENT);
  TS(17);
}

extern "C" void kernel_launch(void* const* d_in, const int* in_sizes, int n_in, void* d_out, int out_size, void* d_ws, size_t ws_size,
                              hipStream_t stream) {
  (void)hipFuncSetAttribute((const void*)k_mega, hipFuncAttributeMaxDynamicSharedMemorySize, LDS_BYTES);
  k_mega<<<256, 512, LDS_BYTES, stream>>>((const __bf16*)d_in[0], (const __bf16*)d_in[1], (const __bf16*)d_in[2], (const __bf16*)d_in[4],
                                          (const __bf16*)d_in[5], (const __bf16*)d_in[6], (const __bf16*)d_in[7], (char*)d_out, (char*)d_ws);
}
```

```cpp
#include <hip/hip_runtime.h>
#include <hip/hip_bf16.h>
#include <stdint.h>
#include <type_traits>

#define DI __device__ __forceinline__
#define LAS __attribute__((address_space(3)))
#define AI __attribute__((always_inline))
typedef __bf16 bf16x8 __attribute__((ext_vector_type(8)));
typedef __bf16 bf16x2 __attribute__((ext_vector_type(2)));
typedef short s16x4 __attribute__((ext_vector_type(4)));
typedef float f32x4 __attribute__((ext_vector_type(4)));
typedef float f32x2 __attribute__((ext_vector_type(2)));
typedef int i32x4 __attribute__((ext_vector_type(4)));
typedef unsigned u32x4 __attribute__((ext_vector_type(4)));
typedef unsigned u32x2 __attribute__((ext_vector_type(2)));
typedef unsigned long long u64;
typedef __attribute__((address_space(1))) u64 gu64;
typedef __attribute__((address_space(1))) unsigned gu32;
template <int V> using IC = std::integral_constant<int, V>;

#define POLL_SLEEP 1
#define G1NT 0
#define CODEPF 1
#define XROT 1
#define WSBASE 0

constexpr int SEQ = 2048, DM = 1024, FF = 3584, FH = 1792;
constexpr float LN_EPS = 1e-5f;

constexpr unsigned WS_EPOCH = 0;
constexpr unsigned F_S1 = 32768, F_O = 2 * 32768, F_H = 3 * 32768, F_W3 = 4 * 32768, F_S3 = 5 * 32768, F_Z2 = 6 * 32768, F_Y = 7 * 32768;
constexpr unsigned WS_S1R = 1u << 18;
constexpr unsigned WS_S1C = WS_S1R + (1u << 18);
constexpr unsigned WS_S2R = WS_S1C + (1u << 18);
constexpr unsigned WS_S2C = WS_S2R + (1u << 18);
constexpr unsigned WS_S3 = WS_S2C + (1u << 18);
constexpr unsigned WS_O = 2u << 20;
constexpr unsigned WS_H = WS_O + (4u << 20);
constexpr unsigned WS_Z2 = WS_H + (4u << 20);
constexpr unsigned WS_Y = WS_Z2 + (8u << 20);
constexpr unsigned WS_W3 = WS_Y + (8u << 20);
constexpr unsigned WS_END = WS_W3 + (8u << 20);
constexpr size_t WS_TS = 64u << 20;
#define TS(i) do {} while (0)

constexpr unsigned L_WT = 98304;
constexpr unsigned L_ZS = 122880;
constexpr unsigned L_X = 129024;
constexpr unsigned L_H = 135168;
constexpr unsigned L_MISC = 153600;
constexpr unsigned L_MU = L_MISC, L_RS = L_MU + 576, L_CS = L_RS + 576, L_P = L_CS + 896, L_M3 = L_P + 2048, L_R3 = L_M3 + 512, L_TAG = L_R3 + 512,
                   L_C2 = L_TAG + 16;
constexpr unsigned LDS_BYTES = 163840;
static_assert(L_C2 + 2 * 896 <= LDS_BYTES, "lds");

DI i32x4 make_srd(const void* p, unsigned bytes) {
  u64 a = (u64)p;
  i32x4 r;
  r[0] = __builtin_amdgcn_readfirstlane((int)(unsigned)a);
  r[1] = __builtin_amdgcn_readfirstlane((int)(unsigned)(a >> 32));
  r[2] = (int)bytes;
  r[3] = 0x00020000;
  return r;
}
template <int POL> DI void dma16(i32x4 srd, unsigned voff, unsigned soff, unsigned ldsaddr) {
  if constexpr (POL == 2)
    asm volatile("s_mov_b32 m0, %0\n\ts_nop 0\n\tbuffer_load_dwordx4 %1, %2, %3 offen nt lds" :: "s"(ldsaddr), "v"(voff), "s"(srd), "s"(soff) : "memory");
  else if constexpr (POL == 1)
    asm volatile("s_mov_b32 m0, %0\n\ts_nop 0\n\tbuffer_load_dwordx4 %1, %2, %3 offen sc1 lds" :: "s"(ldsaddr), "v"(voff), "s"(srd), "s"(soff) : "memory");
  else
    asm volatile("s_mov_b32 m0, %0\n\ts_nop 0\n\tbuffer_load_dwordx4 %1, %2, %3 offen lds" :: "s"(ldsaddr), "v"(voff), "s"(srd), "s"(soff) : "memory");
}
template <int N> DI void dmaL(i32x4 srd, unsigned v, unsigned so, unsigned l) {
  if constexpr (N == 1) asm volatile("s_mov_b32 m0, %0\n\ts_nop 0\n\tbuffer_load_dwordx4 %1, %2, %3 offen sc1 lds" :: "s"(l), "v"(v), "s"(srd), "s"(so) : "memory");
  if constexpr (N == 2) asm volatile("s_mov_b32 m0, %0\n\ts_nop 0\n\tbuffer_load_dwordx4 %1, %2, %3 offen sc1 lds\n\tbuffer_load_dwordx4 %1, %2, %3 offen offset:1024 sc1 lds" :: "s"(l), "v"(v), "s"(srd), "s"(so) : "memory");
  if constexpr (N == 4) asm volatile("s_mov_b32 m0, %0\n\ts_nop 0\n\tbuffer_load_dwordx4 %1, %2, %3 offen sc1 lds\n\tbuffer_load_dwordx4 %1, %2, %3 offen offset:1024 sc1 lds\n\t"
                                     "buffer_load_dwordx4 %1, %2, %3 offen offset:2048 sc1 lds\n\tbuffer_load_dwordx4 %1, %2, %3 offen offset:3072 sc1 lds" :: "s"(l), "v"(v), "s"(srd), "s"(so) : "memory");
}
template <int N> DI void dmaS(i32x4 srd, const unsigned* v, unsigned so, unsigned l) {
  if constexpr (N == 1) asm volatile("s_mov_b32 m0, %0\n\ts_nop 0\n\tbuffer_load_dwordx4 %1, %2, %3 offen lds" :: "s"(l), "v"(v[0]), "s"(srd), "s"(so) : "memory");
  if constexpr (N == 2) asm volatile("s_mov_b32 m0, %0\n\ts_nop 0\n\tbuffer_load_dwordx4 %1, %2, %3 offen lds\n\tbuffer_load_dwordx4 %4, %2, %3 offen offset:1024 lds" :: "s"(l), "v"(v[0]), "s"(srd), "s"(so), "v"(v[1]) : "memory");
  if constexpr (N == 4) asm volatile("s_mov_b32 m0, %0\n\ts_nop 0\n\tbuffer_load_dwordx4 %1, %2, %3 offen lds\n\tbuffer_load_dwordx4 %4, %2, %3 offen offset:1024 lds\n\t"
                                     "buffer_load_dwordx4 %5, %2, %3 offen offset:2048 lds\n\tbuffer_load_dwordx4 %6, %2, %3 offen offset:3072 lds"
                                     :: "s"(l), "v"(v[0]), "s"(srd), "s"(so), "v"(v[1]), "v"(v[2]), "v"(v[3]) : "memory");
}
DI void ld16_async(u32x4& d, i32x4 srd, unsigned voff) { asm volatile("buffer_load_dwordx4 %0, %1, %2, 0 offen sc1" : "=v"(d) : "v"(voff), "s"(srd) : "memory"); }
DI void ld8_async(u32x2& d, i32x4 srd, unsigned voff) { asm volatile("buffer_load_dwordx2 %0, %1, %2, 0 offen sc1" : "=v"(d) : "v"(voff), "s"(srd) : "memory"); }
DI void ld16_plain(u32x4& d, i32x4 srd, unsigned voff, unsigned soff) { asm volatile("buffer_load_dwordx4 %0, %1, %2, %3 offen" : "=v"(d) : "v"(voff), "s"(srd), "s"(soff) : "memory"); }
template <class T> DI void pin(T& v) { asm volatile("" : "+v"(v)); }
DI void st16_dev(i32x4 srd, unsigned voff, u32x4 v) { asm volatile("buffer_store_dwordx4 %0, %1, %2, 0 offen sc1\n\ts_nop 1" :: "v"(v), "v"(voff), "s"(srd) : "memory"); }
DI void st8_dev(i32x4 srd, unsigned voff, u32x2 v) { asm volatile("buffer_store_dwordx2 %0, %1, %2, 0 offen sc1" :: "v"(v), "v"(voff), "s"(srd) : "memory"); }
DI void st4_dev(i32x4 srd, unsigned voff, unsigned v) { asm volatile("buffer_store_dword %0, %1, %2, 0 offen sc1" :: "v"(v), "v"(voff), "s"(srd) : "memory"); }
template <int N> DI void wait_vm() { asm volatile("s_waitcnt vmcnt(%0)" :: "i"(N) : "memory"); }
DI void wait_lgkm0() { __builtin_amdgcn_s_waitcnt(0xC07F); asm volatile("" ::: "memory"); }
DI void bar() { asm volatile("" ::: "memory"); __builtin_amdgcn_s_barrier(); asm volatile("" ::: "memory"); }
#define HBAR() bar()
#define STPARG
#define STPPASS
DI unsigned pk2(float lo, float hi) { bf16x2 v = {(__bf16)lo, (__bf16)hi}; return __builtin_bit_cast(unsigned, v); }
DI u32x2 pk4(f32x4 v) { return (u32x2){pk2(v[0], v[1]), pk2(v[2], v[3])}; }
DI f32x2 unpk2(unsigned u) { return (f32x2){__uint_as_float(u << 16), __uint_as_float(u & 0xffff0000u)}; }
DI f32x4 unpk4(u32x2 u) { const f32x2 a = unpk2(u[0]), b = unpk2(u[1]); return (f32x4){a[0], a[1], b[0], b[1]}; }
DI bf16x8 cat8(u32x2 a, u32x2 b) { u32x4 v = {a[0], a[1], b[0], b[1]}; return __builtin_bit_cast(bf16x8, v); }
DI f32x4 mfma(bf16x8 a, bf16x8 b, f32x4 c) { return __builtin_amdgcn_mfma_f32_16x16x32_bf16(a, b, c, 0, 0, 0); }
DI bf16x8 lds_a(LAS char* p) { return *(LAS bf16x8*)p; }
DI u32x2 lds_tr(LAS char* p) { return __builtin_bit_cast(u32x2, __builtin_amdgcn_ds_read_tr16_b64_v4i16((LAS s16x4*)p)); }
#undef lds_tr
DI int swap23(int k) { return (k & ~12) | ((k & 4) << 1) | ((k & 8) >> 1); }
DI float sum4(float x) {
  auto r = __builtin_amdgcn_permlane16_swap(__float_as_uint(x), __float_as_uint(x), false, false);
  x = __uint_as_float(r[0]) + __uint_as_float(r[1]);
  auto s = __builtin_amdgcn_permlane32_swap(__float_as_uint(x), __float_as_uint(x), false, false);
  return __uint_as_float(s[0]) + __uint_as_float(s[1]);
}
DI float max4(float x) {
  auto r = __builtin_amdgcn_permlane16_swap(__float_as_uint(x), __float_as_uint(x), false, false);
  x = fmaxf(__uint_as_float(r[0]), __uint_as_float(r[1]));
  auto s = __builtin_amdgcn_permlane32_swap(__float_as_uint(x), __float_as_uint(x), false, false);
  return fmaxf(__uint_as_float(s[0]), __uint_as_float(s[1]));
}
DI f32x2 mean_rstd(f32x2 t, float inv_n) {
  const float mu = t[0] * inv_n, var = fmaxf(t[1] * inv_n - mu * mu, 0.f);
  return (f32x2){mu, __builtin_amdgcn_rsqf(var + LN_EPS)};
}
DI u64 uniform64(u64 v) {
  return ((u64)(unsigned)__builtin_amdgcn_readfirstlane((int)(unsigned)(v >> 32)) << 32) | (unsigned)__builtin_amdgcn_readfirstlane((int)(unsigned)v);
}
DI int fresh_tid() { int t = threadIdx.x; asm volatile("" : "+v"(t)); return t; }
DI void set_flag(char* ws, unsigned flag_off, u64 tag) { __hip_atomic_store((gu64*)(ws + flag_off), tag, __ATOMIC_RELAXED, __HIP_MEMORY_SCOPE_AGENT); }
DI void await(char* ws, unsigned flag_off, u64 tag) {
#pragma unroll 1
  for (unsigned spin = 0; spin < (1u << 17); ++spin) {
    const u64 f = __hip_atomic_load((gu64*)(ws + flag_off), __ATOMIC_RELAXED, __HIP_MEMORY_SCOPE_AGENT);
    if (__all(f == tag)) break;
    __builtin_amdgcn_s_sleep(POLL_SLEEP);
  }
  asm volatile("" ::: "memory");
}
DI unsigned row_flags(unsigned f, int pm, int lane) { return f + (pm * 16 + (lane & 15)) * 128; }
DI unsigned col_flags(unsigned f, int pn, int lane) { return f + ((lane & 15) * 16 + pn) * 128; }
DI void gather16(u32x4 (&d)[16], i32x4 srd, unsigned off) {
#pragma unroll
  for (int j = 0; j < 16; ++j) ld16_async(d[j], srd, off + j * 1024);
}
DI f32x4 reduce16(u32x4 (&d)[16]) {
  f32x4 t[2] = {{0.f, 0.f, 0.f, 0.f}, {0.f, 0.f, 0.f, 0.f}};
#pragma unroll
  for (int j = 0; j < 16; ++j) { pin(d[j]); t[j & 1] += __builtin_bit_cast(f32x4, d[j]); }
  return t[0] + t[1];
}
DI void put_rows(LAS char* lds, unsigned l_mu, unsigned l_rs, int row2, f32x4 t, float inv_n) {
  const f32x2 a = mean_rstd((f32x2){t[0], t[1]}, inv_n), b = mean_rstd((f32x2){t[2], t[3]}, inv_n);
  *(LAS f32x2*)(lds + l_mu + row2 * 8) = (f32x2){a[0], b[0]};
  *(LAS f32x2*)(lds + l_rs + row2 * 8) = (f32x2){a[1], b[1]};
}
template <int N> DI void bars() {
#pragma unroll 1
  for (int i = 0; i < N; ++i) bar();
}

DI int b_pos_lane(int lane) { const int g = lane >> 4, q = (lane & 15) >> 2; return (g >> 1) * 16 + (g & 1) * 4 + q; }
DI unsigned b128_base(int lane, int nb) { const int pos = b_pos_lane(lane); return pos * 128 + ((nb ^ ((pos >> 1) & 3)) * 32) + (lane & 3) * 8; }
DI unsigned b224_base(int lane) { return b_pos_lane(lane) * 224 + (lane & 3) * 8; }
DI unsigned a_base(int lane, int kk) { const int r = lane & 15, g = lane >> 4; return r * 128 + (((kk * 4 + g) ^ ((r >> 1) & 7)) * 16); }
DI unsigned img_off(int row, int chunk) { return row * 128 + ((chunk ^ ((row >> 1) & 7)) * 16); }

#define SGB(mask, n) __builtin_amdgcn_sched_group_barrier(mask, n, 0)
template <int NR, int NM> DI void interleave() {
  constexpr int RP = NR > NM - 2 ? 2 : 1;
  SGB(0x8, 1);
  SGB(0x2, 8);
#pragma unroll
  for (int i = 0; i < (NR + RP - 1) / RP; ++i) { SGB(0x100, RP); SGB(0x8, 1); }
  SGB(0x8, NM);
}
DI void cbar() {
  __builtin_amdgcn_sched_barrier(0);
  __builtin_amdgcn_s_waitcnt(0xC07F);
  bar();
  __builtin_amdgcn_sched_barrier(0);
}
template <int NK, int NS, int SA, int SB, int NR, int NM, class F, class L, class M>
DI void compute_loop(L load, M mma) {
  F f0, f1;
  bar();
  load(f0, 0u, 0u, 0);
  unsigned sa = 0, sb = 0;
  __builtin_amdgcn_sched_barrier(0);
#pragma unroll 1
  for (int t = 0; t < NK - 1; ++t) {
    load(f1, sa, sb, 1);
    mma(f0);
    interleave<NR, NM>();
    cbar();
    sa += SA; sb += SB; if (sa == NS * SA) { sa = 0; sb = 0; }
    load(f0, sa, sb, 0);
    mma(f1);
    interleave<NR, NM>();
    __builtin_amdgcn_sched_barrier(0);
  }
  load(f1, sa, sb, 1);
  mma(f0);
  interleave<NR, NM>();
  cbar();
  mma(f1);
}

template <int N, int NS, int SA, int SB, int NR, int NM, class F, class L, class M>
DI void deep_segment(F& f0, unsigned& sa, unsigned& sb, L load, M mma) {
  F f1;
#pragma unroll 1
  for (int t = 0; t < N; t += 2) {
    cbar();
    sa += SA; sb += SB; if (sa == NS * SA) { sa = 0; sb = 0; }
    load(f1, sa, sb);
    mma(f0);
    interleave<NR, NM>();
    cbar();
    sa += SA; sb += SB; if (sa == NS * SA) { sa = 0; sb = 0; }
    load(f0, sa, sb);
    mma(f1);
    interleave<NR, NM>();
  }
  __builtin_amdgcn_sched_barrier(0);
}

template <int NK_, int NS_, int XP_, int WP_> struct Pipe {
  static constexpr int NK = NK_, NS = NS_, XP = XP_, PW = XP_ + WP_;
  static constexpr int allowed(int t) {
    int n = 0;
    for (int s = t + 2; s <= t + NS - 1 && s < NK; ++s) n += s < NS ? XP : PW;
    return n;
  }
};
template <class P, int T, int EXTRA = 0, class IX, class IW> DI void hstep(IX& ix, IW& iw, int& slot STPARG) {
  wait_vm<P::allowed(T) + EXTRA>();
  HBAR();
  if constexpr (T + P::NS < P::NK) { ix(T + P::NS, slot); iw(T + P::NS, slot); }
  slot = slot + 1 == P::NS ? 0 : slot + 1;
}
template <class P, int T0, int T1, class IX, class IW> DI void hsteps(IX& ix, IW& iw, int& slot STPARG) {
  if constexpr (T0 < T1) {
    if constexpr (T0 >= P::NS - 2 && T1 <= P::NK - P::NS && T1 - T0 > 1) {
#pragma unroll 1
      for (int t = T0; t < T1; ++t) {
        wait_vm<(P::NS - 2) * P::PW>();
        HBAR();
        ix(t + P::NS, slot); iw(t + P::NS, slot);
        slot = slot + 1 == P::NS ? 0 : slot + 1;
      }
    } else if constexpr (T0 >= P::NS - 2 && T0 + 1 < P::NK - P::NS && T1 > P::NK - P::NS) {
      hsteps<P, T0, P::NK - P::NS>(ix, iw, slot STPPASS);
      hsteps<P, P::NK - P::NS, T1>(ix, iw, slot STPPASS);
    } else {
      hstep<P, T0>(ix, iw, slot STPPASS);
      hsteps<P, T0 + 1, T1>(ix, iw, slot STPPASS);
    }
  }
}
template <class P, class IX> DI void hstart(IX& ix STPARG) {
  ix(0, 0); ix(1, 1);
  wait_vm<P::XP>();
  HBAR();
#pragma unroll
  for (int s = 2; s < P::NS; ++s) ix(s, s);
}
DI int rot(int t, int r0, int nk) { const int k = t + r0; return k >= nk ? k - nk : k; }

namespace g1 { constexpr int NK = 16, NS = 3, AB = 144 * 128, ST = AB + 3 * 8192; struct Fr { bf16x8 a[5]; u32x2 b[6][2]; }; }
namespace gs { constexpr int NS = 4, AB = 16384, ST = AB + 8192; struct Fr { bf16x8 a[2][4]; u32x2 b[2][2][2]; }; }
namespace gf { constexpr int NK = 64, NS = 4, AB = 16384, SEG = 14336, B0 = NS * AB; struct Fr { bf16x8 a[2][2]; u32x2 b[2][7][2]; }; }

__global__ __launch_bounds__(512, 2) void k_mega(const __bf16* __restrict__ x, const __bf16* __restrict__ wqkv, const __bf16* __restrict__ wo,
                                                 const __bf16* __restrict__ win, const __bf16* __restrict__ wsp, const __bf16* __restrict__ bsp,
                                                 const __bf16* __restrict__ wout, char* __restrict__ out, char* __restrict__ ws) {
  extern __shared__ __attribute__((aligned(16))) char smem_[];
  LAS char* lds = (LAS char*)smem_;
  const int tid = threadIdx.x, lane = tid & 63, wid = __builtin_amdgcn_readfirstlane(tid >> 6);
  const int xcd = blockIdx.x & 7, idx = blockIdx.x >> 3;
  const int pm1 = (idx & 7) * 2 + (xcd & 1), pn1 = (idx >> 3) * 4 + (xcd >> 1);
  const int pm2 = (idx & 3) * 4 + (xcd & 3), pn2 = (idx >> 2) * 2 + (xcd >> 2);
  const i32x4 srdW = make_srd(ws, WS_END);
  TS(0);

  const bf16x8 ones = __builtin_bit_cast(bf16x8, (u32x4){0x3f803f80u, 0x3f803f80u, 0x3f803f80u, 0x3f803f80u});
  const f32x4 zero4 = {0.f, 0.f, 0.f, 0.f};
  const int r1 = (((xcd >> 1) + (xcd & 1) * 2 + (idx & 1)) & 3) * 4;
  const int srow = idx >> 3, scol = (idx & 7) >> 1;
  auto g1_first_steps = [&](int hw, auto between) AI {
    using namespace g1;
#pragma unroll 1
    for (int t = 0; t < 4; ++t) {
      bf16x8 ra[2][2]; u32x2 rb[3][4];
      const unsigned so = (t == 3 ? 0 : t) * ST;
      if (t == srow) {
#pragma unroll
        for (int m = 0; m < 2; ++m)
#pragma unroll
          for (int kk = 0; kk < 2; ++kk) ra[m][kk] = lds_a(lds + so + a_base(lane, kk) + (hw * 2 + m) * 2048);
      }
      if (t == scol) {
#pragma unroll
        for (int i = 0; i < 3; ++i) {
          const int n = hw * 3 + i;
          const unsigned cb = so + AB + (n >> 2) * 8192 + b128_base(lane, n & 3);
#pragma unroll
          for (int j = 0; j < 4; ++j) rb[i][j] = lds_tr(lds + cb + ((j >> 1) * 32 + (j & 1) * 8) * 128);
        }
      }
      wait_lgkm0();
      between(t);
      if (t == srow) {
#pragma unroll
        for (int m = 0; m < 2; ++m) {
          const f32x4 sm = mfma(ones, ra[m][1], mfma(ones, ra[m][0], zero4)), sq = mfma(ra[m][1], ra[m][1], mfma(ra[m][0], ra[m][0], zero4));
          const float d = (lane & 2) ? ((lane & 1) ? sq[3] : sq[2]) : ((lane & 1) ? sq[1] : sq[0]);
          if (((lane & 15) >> 2) == (lane >> 4)) *(LAS f32x2*)(lds + L_P + (hw * 32 + m * 16 + (lane & 15)) * 8) = (f32x2){sm[0], d};
        }
      }
      if (t == scol) {
#pragma unroll
        for (int i = 0; i < 3; ++i)
          *(LAS float*)(lds + L_P + 1024 + ((hw * 3 + i) * 16 + (lane & 15)) * 4) =
              mfma(ones, cat8(rb[i][2], rb[i][3]), mfma(ones, cat8(rb[i][0], rb[i][1]), zero4))[0];
      }
    }
    wait_lgkm0();
  };

  if (wid == 7) {
    const unsigned l16 = lane * 16;
    bar();
    const u64 tag = uniform64(*(LAS u64*)(lds + L_TAG));
    const u32x2 tag2 = __builtin_bit_cast(u32x2, tag);
    const unsigned myflag = lane < 16 ? (pm1 * 16 + pn1) * 128 + lane * 8 : 0xff000000u;
    g1_first_steps(3, [&](int) { bar(); });
    bar();
    st16_dev(srdW, WS_S1R + (pm1 * 16 + ((srow + r1) & 15)) * 1024 + l16, *(LAS u32x4*)(lds + L_P + l16));
    st16_dev(srdW, WS_S1C + (pn1 * 16 + ((scol + r1) & 15)) * 1024 + l16, *(LAS u32x4*)(lds + L_P + 1024 + l16));
    st16_dev(srdW, WS_S3 + (pm1 * 16 + pn1) * 1024 + l16, (u32x4){~0u, ~0u, ~0u, ~0u});
    bars<2>();
    wait_vm<0>();
    st8_dev(srdW, F_S1 + myflag, tag2);
    bars<2>();
    u32x4 wt[28], xt[16];
    {
      const int pmh = pm1 > 0 ? pm1 - 1 : 0;
      const unsigned fo = lane < 16 ? row_flags(F_S1, pm1, lane) : lane < 32 ? row_flags(F_S1, pmh, lane) : col_flags(F_S1, pn1, lane);
      u32x2 f0;
      ld8_async(f0, srdW, fo);
      bar();
      wait_vm<0>();
      pin(f0);
      if (!__all(__builtin_bit_cast(u64, f0) == tag)) await(ws, fo, tag);
      u32x4 gr[16], gc[16], gh[2];
      gather16(gr, srdW, WS_S1R + pm1 * 16384 + l16);
      gather16(gc, srdW, WS_S1C + pn1 * 16384 + l16);
#pragma unroll
      for (int j = 0; j < 2; ++j) ld16_async(gh[j], srdW, WS_S1R + (pmh * 16 + (lane >> 3) * 2 + j) * 1024 + (56 + (lane & 7)) * 16);
      bars<2>();
      wait_vm<0>();
      put_rows(lds, L_MU, L_RS, lane, reduce16(gr), 1.f / DM);
      const f32x4 c = reduce16(gc);
      if (lane < 48) *(LAS f32x4*)(lds + L_CS + l16) = c;
      bar();
      pin(gh[0]); pin(gh[1]);
      f32x4 hv = __builtin_bit_cast(f32x4, gh[0]) + __builtin_bit_cast(f32x4, gh[1]);
#pragma unroll
      for (int e = 0; e < 4; ++e) { hv[e] += __shfl_xor(hv[e], 8); hv[e] = sum4(hv[e]); }
      put_rows(lds, L_MU, L_RS, 64 + (lane & 7), hv, 1.f / DM);
      wait_lgkm0();
      bar();
    }
    {
      const i32x4 srdWin = make_srd(win, DM * FF * 2), srdX = make_srd(x, SEQ * DM * 2);
      int pos = lane / 14, gi = lane - pos * 14;
#pragma unroll
      for (int i = 0; i < 14; ++i) {
        const unsigned v = swap23(pos) * (FF * 2) + (pn1 * 112 + gi * 8) * 2;
        ld16_plain(wt[i], srdWin, v, pm1 * (64u * FF * 2));
        ld16_plain(wt[14 + i], srdWin, v, pm1 * (64u * FF * 2) + FH * 2);
        gi += 8; pos += 4; if (gi >= 14) { gi -= 14; pos += 1; }
      }
      bars<2>();
#pragma unroll
      for (int i = 0; i < 16; ++i) {
        const int r = i * 8 + (lane >> 3);
        ld16_plain(xt[i], srdX, (lane >> 3) * (DM * 2) + (((lane & 7) ^ ((r >> 1) & 7)) * 16), (pm2 * 128 + i * 8) * (DM * 2) + pn2 * 128);
      }
    }
    bars<3>();
    wait_vm<0>();
#pragma unroll
    for (int i = 0; i < 28; ++i) { pin(wt[i]); st16_dev(srdW, WS_W3 + (pn1 * 16 + pm1) * 28672 + i * 1024 + l16, wt[i]); }
#pragma unroll
    for (int i = 0; i < 16; ++i) { pin(xt[i]); *(LAS u32x4*)(lds + L_H + i * 1024 + l16) = xt[i]; }
    await(ws, row_flags(F_O, pm2, lane), tag);
    bar();
    bar();
#pragma unroll
    for (int i = 0; i < 14; ++i) *(LAS u32x4*)(lds + L_WT + i * 1024 + l16) = wt[i];
    bar();
#pragma unroll
    for (int i = 14; i < 28; ++i) *(LAS u32x4*)(lds + L_WT + i * 1024 + l16) = wt[i];
    wait_lgkm0();
    {
      const unsigned cb = L_WT + b224_base(lane);
#pragma unroll
      for (int q = 0; q < 4; ++q) {
        bar();
        float c[4];
#pragma unroll
        for (int u = 0; u < 4; ++u) {
          const int n = q * 4 + u < 14 ? q * 4 + u : 13;
          LAS char* p = lds + cb + (n / 7) * 14336 + (n % 7) * 32;
          c[u] = mfma(ones, cat8(lds_tr(p + 32 * 224), lds_tr(p + 40 * 224)), mfma(ones, cat8(lds_tr(p), lds_tr(p + 8 * 224)), zero4))[0];
        }
        st4_dev(srdW, WS_S2C + (pn1 * 16 + pm1) * 1024 + q * 256 + lane * 4, __float_as_uint(lane < 32 ? (lane < 16 ? c[0] : c[1]) : (lane < 48 ? c[2] : c[3])));
      }
    }
    bars<2>();
    wait_vm<0>();
    st8_dev(srdW, F_W3 + myflag, tag2);
    bars<6>();
    {
      u32x2 f0;
      const unsigned fo = col_flags(F_W3, pn1, lane);
      ld8_async(f0, srdW, fo);
      bars<2>();
      wait_vm<0>();
      pin(f0);
      if (!__all(__builtin_bit_cast(u64, f0) == tag)) await(ws, fo, tag);
    }
    bar();
    bar();
    u32x4 hpark[16];
#pragma unroll
    for (int i = 0; i < 16; ++i) hpark[i] = *(LAS u32x4*)(lds + L_H + i * 1024 + l16);
    wait_lgkm0();
    bar();
    await(ws, row_flags(F_H, pm1, lane), tag);
    bar();
    bar();
    {
      u32x4 gr[16], gc[16];
      gather16(gr, srdW, WS_S2R + pm1 * 16384 + l16);
      gather16(gc, srdW, WS_S2C + pn1 * 16384 + l16);
      dma16<0>(make_srd(bsp, SEQ * 2), lane < 16 ? pm1 * 256 + l16 : 0xfffffff0u, 0u, L_C2);
      bars<4>();
      wait_vm<0>();
      put_rows(lds, L_MU, L_RS, lane, reduce16(gr), 1.f / DM);
      bar();
      const f32x4 c = reduce16(gc);
      if (lane < 56) *(LAS f32x4*)(lds + L_CS + l16) = c;
      wait_lgkm0();
    }
    bars<14>();
    {
      u32x4 gv[16];
      gather16(gv, srdW, WS_S3 + pm1 * 16384 + l16);
      bar();
#pragma unroll 1
      for (unsigned spin = 0; spin < (1u << 17); ++spin) {
        wait_vm<0>();
        unsigned mx = 0;
#pragma unroll
        for (int j = 0; j < 16; ++j) { pin(gv[j]); mx = max(max(mx, max(gv[j][0], gv[j][1])), max(gv[j][2], gv[j][3])); }
        if (__all(mx != ~0u)) break;
        gather16(gv, srdW, WS_S3 + pm1 * 16384 + l16);
      }
      wait_vm<0>();
      put_rows(lds, L_M3, L_R3, lane, reduce16(gv), 1.f / FH);
      wait_lgkm0();
    }
    bars<3>();
    {
      int r = lane / 14, ch = lane - r * 14;
#pragma unroll
      for (int half = 0; half < 2; ++half) {
        u32x4 v[14];
#pragma unroll
        for (int i = 0; i < 14; ++i) {
          v[i] = *(LAS u32x4*)(lds + L_ZS + r * 240 + ch * 16);
          ch += 8; r += 4; if (ch >= 14) { ch -= 14; r += 1; }
        }
#pragma unroll
        for (int i = 0; i < 14; ++i) st16_dev(srdW, WS_Z2 + (pn1 * 32 + pm1 * 2 + half) * 14336 + i * 1024 + l16, v[i]);
        bar();
      }
    }
    wait_vm<0>();
    st8_dev(srdW, F_Z2 + myflag, tag2);
    bars<2>();
    {
      u32x2 f0;
      const unsigned fo = col_flags(F_Z2, pn1, lane);
      ld8_async(f0, srdW, fo);
      bar();
      wait_vm<0>();
      pin(f0);
      if (!__all(__builtin_bit_cast(u64, f0) == tag)) await(ws, fo, tag);
    }
    bars<38>();
#pragma unroll
    for (int i = 0; i < 16; ++i) *(LAS u32x4*)(lds + L_H + i * 1024 + l16) = hpark[i];
    await(ws, row_flags(F_Y, pm2, lane), tag);
    wait_lgkm0();
    bars<31>();
    return;
  }

  if (wid >= 4) {
    const int dw = wid - 4;
    int slot;
    const unsigned l16 = lane * 16;
    {
      using namespace g1;
      using P = Pipe<NK, NS, 14, 0>;
      const i32x4 srdA = make_srd(x, SEQ * DM * 2), srdB = make_srd(wqkv, DM * 3 * DM * 2);
      unsigned va[6], vb[8];
#pragma unroll
      for (int i = 0; i < 8; ++i) {
        const int p = dw * 8 + i, pos = (p & 7) * 8 + (lane >> 3), nb = ((lane & 7) >> 1) ^ ((pos >> 1) & 3);
        vb[i] = swap23(pos) * (3 * DM * 2) + (dw * DM + pn1 * 64 + nb * 16 + (lane & 1) * 8) * 2 - (i & 3) * 1024;
      }
#pragma unroll
      for (int i = 0; i < 6; ++i) {
        const int r = (dw * 6 + i) * 8 + (lane >> 3);
        int grow = r < 128 ? pm1 * 128 + r : pm1 * 128 - 16 + (r - 128);
        grow = grow < 0 ? 8 : grow;
        va[i] = grow * (DM * 2) + (((lane & 7) ^ ((r >> 1) & 7)) * 16) - (i & 3) * 1024;
      }
      auto ia = [&](int t, int s) {
        const unsigned kt = (t + r1) & 15;
        dmaS<4>(srdA, va, kt * 128u, s * ST + dw * 6144u);
        dmaS<2>(srdA, va + 4, kt * 128u, s * ST + dw * 6144u + 4096u);
      };
      auto ib = [&](int t, int s) {
        const unsigned kt = (t + r1) & 15;
        dmaS<4>(srdB, vb, kt * (64u * 3 * DM * 2), s * ST + AB + dw * 8192u);
        dmaS<4>(srdB, vb + 4, kt * (64u * 3 * DM * 2), s * ST + AB + dw * 8192u + 4096u);
      };
      ib(0, 0); ia(0, 0); ib(1, 1);
      wait_vm<8>();
      HBAR();
      ia(1, 1); ia(2, 2); ib(2, 2);
      TS(14);
      slot = 0;
      g1_first_steps(dw, [&](int t) {
        wait_vm<P::PW>();
        HBAR();
        ia(t + NS, slot); ib(t + NS, slot);
        slot = slot + 1 == NS ? 0 : slot + 1;
      });
      TS(15);
      hsteps<P, 4, NK>(ia, ib, slot STPPASS);
    }
    TS(1);
    auto lin16 = [&](unsigned src, unsigned dst) AI {
      dmaL<4>(srdW, l16, src + dw * 4096u, dst + dw * 4096u);
      if (dw == 2) dmaL<2>(srdW, l16, src + 14 * 1024u, dst + 14 * 1024u);
      else dmaL<1>(srdW, l16, src + (12 + dw) * 1024u, dst + (12 + dw) * 1024u);
    };
    unsigned vbs[3];
#pragma unroll
    for (int i = 0; i < 3; ++i) {
      const int pos = (i < 2 ? dw * 2 + i : (6 + dw) & 7) * 8 + (lane >> 3), nb = ((lane & 7) >> 1) ^ ((pos >> 1) & 3);
      vbs[i] = swap23(pos) * (DM * 2) + (pn2 * 64 + nb * 16 + (lane & 1) * 8) * 2 - (i == 1 ? 1024 : 0);
    }
    auto w8 = [&](i32x4 srd, unsigned kt, int s) AI {
      dmaS<2>(srd, vbs, kt * (64u * DM * 2), s * gs::ST + gs::AB + dw * 2048u);
      if (dw != 2) dmaS<1>(srd, vbs + 2, kt * (64u * DM * 2), s * gs::ST + gs::AB + (6 + dw) * 1024u);
    };
    {
      using namespace gs;
      constexpr int NK = 16;
      using P = Pipe<NK, NS, 5, 3>;
      const i32x4 srdB = make_srd(wo, DM * DM * 2);
      const int r0 = XROT * (xcd & 3) * 4;
      auto ix = [&](int t, int s) { lin16(WS_O + (pm2 * 16 + ((t + r0) & 15)) * 16384u, s * ST); };
      auto iw = [&](int t, int s) { w8(srdB, (t + r0) & 15, s); };
      iw(0, 0); iw(1, 1);
      HBAR();
      iw(2, 2); iw(3, 3);
      HBAR();
      HBAR();
      TS(2);
      HBAR();
      TS(3);
      hstart<P>(ix STPPASS);
      slot = 0;
      hsteps<P, 0, NK>(ix, iw, slot STPPASS);
    }
    TS(4);
    {
      using namespace gf;
      using P = Pipe<NK, NS, 5, 5>;
      const i32x4 srdSp = make_srd(wsp, SEQ * SEQ * 2);
      auto lin14 = [&](unsigned src, int s) AI {
        const unsigned dst = B0 + s * SEG;
        dmaL<4>(srdW, l16, src + dw * 4096u, dst + dw * 4096u);
        if (dw != 2) dmaL<1>(srdW, l16, src + (12 + dw) * 1024u, dst + (12 + dw) * 1024u);
      };
      auto ia3 = [&](int t, int s) { lin16(WS_H + (pm1 * 16 + (t & 15)) * 16384u, s * AB); };
      auto ib3 = [&](int t, int s) { lin14(WS_W3 + (pn1 * 16 + (t & 15)) * 28672u + (t < 16 ? 14336u : 0u), s); };
      unsigned va4[6];
#pragma unroll
      for (int i = 0; i < 6; ++i) { const int r = (i < 4 ? dw * 4 + i : 8 + dw + i) * 8 + (lane >> 3); va4[i] = (pm1 * 128 + r) * (SEQ * 2) + (((lane & 7) ^ ((r >> 1) & 7)) * 16) - (i & 3) * 1024; }
      const int r4 = XROT * (xcd >> 1) * 8;
      auto ia4 = [&](int t, int s) {
        const unsigned kt = (t + r4) & 31;
        dmaS<4>(srdSp, va4, kt * 128u, s * AB + dw * 4096u);
        if (dw == 2) dmaS<2>(srdSp, va4 + 4, kt * 128u, s * AB + 14 * 1024u);
        else dmaS<1>(srdSp, va4 + 4, kt * 128u, s * AB + (12 + dw) * 1024u);
      };
      auto ib4 = [&](int t, int s) { lin14(WS_Z2 + (pn1 * 32 + ((t + r4) & 31)) * (unsigned)SEG, s); };
      ib3(0, 0); ib3(1, 1);
      HBAR();
      HBAR();
      ib3(2, 2); ib3(3, 3);
      TS(5);
      HBAR();
      TS(6);
      hstart<P>(ia3 STPPASS);
      slot = 0;
      hsteps<P, 0, 28>(ia3, ib3, slot STPPASS);
      TS(9);
      hsteps<P, 28, NK>(ia4, ib4, slot STPPASS);
    }
    TS(10);
    {
      using namespace gs;
      constexpr int NK = 28;
      using P = Pipe<NK, NS, 5, 3>;
      const i32x4 srdB = make_srd(wout, FH * DM * 2);
      const int r0 = XROT * (xcd & 3) * 7;
      auto ix = [&](int t, int s) { lin16(WS_Y + (pm2 * 28 + rot(t, r0, NK)) * 16384u, s * ST); };
      auto iw = [&](int t, int s) { w8(srdB, rot(t, r0, NK), s); };
#pragma unroll
      for (int s = 0; s < NS; ++s) iw(s, s);
      HBAR();
      HBAR();
      TS(11);
      HBAR();
      TS(12);
      hstart<P>(ix STPPASS);
      slot = 0;
      hsteps<P, 0, NK>(ix, iw, slot STPPASS);
      HBAR();
    }
    TS(13);
    return;
  }

  const u64 tag = uniform64(__hip_atomic_load((gu64*)(ws + WS_EPOCH), __ATOMIC_RELAXED, __HIP_MEMORY_SCOPE_AGENT)) + 1;
  if (wid == 1) {
    u64 pc;
    asm volatile("s_getpc_b64 %0" : "=s"(pc));
    const unsigned v = *(const gu32*)((pc & ~127ull) + (idx * 14 + (lane < 14 ? lane : 0)) * 128);
    asm volatile("" :: "v"(v));
  }
  if (tid == 0) *(LAS u64*)(lds + L_TAG) = tag;
  wait_lgkm0();
#define LANES const int tid = fresh_tid(), lane = tid & 63, i16 = lane & 15, g = lane >> 4; (void)i16; (void)g
  auto publish = [&](unsigned flag) {
    wait_vm<0>();
    bar();
    if (threadIdx.x < 16) set_flag(ws, flag + threadIdx.x * 8, tag);
    bar();
  };
  auto copy16k = [&](unsigned dst) {
    LANES;
#pragma unroll
    for (int i = 0; i < 4; ++i) st16_dev(srdW, dst + (i * 256 + tid) * 16, *(LAS u32x4*)(lds + L_H + (i * 256 + tid) * 16));
  };
  {
    using namespace g1;
    LANES;
    const int hd = wid & 1, rh = wid >> 1;
    unsigned ab[2], hb[2], bb[2];
#pragma unroll
    for (int kk = 0; kk < 2; ++kk) { ab[kk] = a_base(lane, kk) + rh * 64 * 128; hb[kk] = a_base(lane, kk) + 128 * 128; bb[kk] = AB + b128_base(lane, hd * 2 + kk); }
    f32x4 aq[4][2], ak[4][2], av[4][2], ah[2];
#pragma unroll
    for (int m = 0; m < 4; ++m)
#pragma unroll
      for (int j = 0; j < 2; ++j) aq[m][j] = ak[m][j] = av[m][j] = zero4;
    ah[0] = ah[1] = zero4;
    auto load = [&](Fr& f, unsigned so, unsigned, int kk) {
#pragma unroll
      for (int m = 0; m < 4; ++m) f.a[m] = lds_a(lds + so + ab[kk] + m * 2048);
      f.a[4] = lds_a(lds + so + hb[kk]);
#pragma unroll
      for (int s = 0; s < 3; ++s)
#pragma unroll
        for (int j = 0; j < 2; ++j)
#pragma unroll
          for (int hh = 0; hh < 2; ++hh) f.b[s * 2 + j][hh] = lds_tr(lds + so + bb[j] + s * 8192 + (kk * 32 + hh * 8) * 128);
    };
    auto mma2 = [&](Fr& f, auto RH) {
      bf16x8 b[6];
#pragma unroll
      for (int i = 0; i < 6; ++i) b[i] = cat8(f.b[i][0], f.b[i][1]);
#pragma unroll
      for (int m = 0; m < 4; ++m)
#pragma unroll
        for (int j = 0; j < 2; ++j) {
          aq[m][j] = mfma(b[j], f.a[m], aq[m][j]);
          ak[m][j] = mfma(b[2 + j], f.a[m], ak[m][j]);
          av[m][j] = mfma(f.a[m], b[4 + j], av[m][j]);
        }
#pragma unroll
      for (int j = 0; j < 2; ++j) {
        if constexpr (decltype(RH)::value == 0) ah[j] = mfma(b[2 + j], f.a[4], ah[j]);
        else ah[j] = mfma(f.a[4], b[4 + j], ah[j]);
      }
    };
    if (rh == 0) compute_loop<NK, NS, ST, ST, 17, 26, Fr>(load, [&](Fr& f) { mma2(f, IC<0>{}); });
    else compute_loop<NK, NS, ST, ST, 17, 26, Fr>(load, [&](Fr& f) { mma2(f, IC<1>{}); });
    TS(1);
    f32x4 csq[2], csk[2]; float csv[2];
#pragma unroll
    for (int j = 0; j < 2; ++j) {
      csq[j] = *(LAS f32x4*)(lds + L_CS + (hd * 32 + j * 16 + 4 * g) * 4);
      csk[j] = *(LAS f32x4*)(lds + L_CS + (64 + hd * 32 + j * 16 + 4 * g) * 4);
      csv[j] = *(LAS float*)(lds + L_CS + (128 + hd * 32 + j * 16 + i16) * 4);
    }
    bf16x8 Q[4], K[5]; u32x2 V[5][2];
#pragma unroll
    for (int m = 0; m < 4; ++m) {
      const int r = rh * 64 + m * 16;
      const float mu = *(LAS float*)(lds + L_MU + (r + i16) * 4), rs = *(LAS float*)(lds + L_RS + (r + i16) * 4);
      const f32x4 mu4 = *(LAS f32x4*)(lds + L_MU + (r + 4 * g) * 4), rs4 = *(LAS f32x4*)(lds + L_RS + (r + 4 * g) * 4);
      Q[m] = cat8(pk4((aq[m][0] - mu * csq[0]) * rs), pk4((aq[m][1] - mu * csq[1]) * rs));
      K[m + 1] = cat8(pk4((ak[m][0] - mu * csk[0]) * rs), pk4((ak[m][1] - mu * csk[1]) * rs));
      V[m + 1][0] = pk4((av[m][0] - mu4 * csv[0]) * rs4);
      V[m + 1][1] = pk4((av[m][1] - mu4 * csv[1]) * rs4);
    }
    LAS char* xch = lds + L_X + hd * 3072 + lane * 16;
    if (rh == 0) {
      const float mu = *(LAS float*)(lds + L_MU + (128 + i16) * 4), rs = *(LAS float*)(lds + L_RS + (128 + i16) * 4);
      K[0] = cat8(pk4((ah[0] - mu * csk[0]) * rs), pk4((ah[1] - mu * csk[1]) * rs));
      *(LAS bf16x8*)(xch) = K[4];
      *(LAS bf16x8*)(xch + 1024) = cat8(V[4][0], V[4][1]);
    } else {
      const f32x4 mu4 = *(LAS f32x4*)(lds + L_MU + (128 + 4 * g) * 4), rs4 = *(LAS f32x4*)(lds + L_RS + (128 + 4 * g) * 4);
      *(LAS bf16x8*)(xch + 2048) = cat8(pk4((ah[0] - mu4 * csv[0]) * rs4), pk4((ah[1] - mu4 * csv[1]) * rs4));
      K[0] = K[4];
    }
    wait_lgkm0(); bar();
    {
      u32x4 v = *(LAS u32x4*)(xch + (rh == 0 ? 2048 : 1024));
      V[0][0] = (u32x2){v[0], v[1]}; V[0][1] = (u32x2){v[2], v[3]};
      if (rh == 1) K[0] = *(LAS bf16x8*)(xch);
    }
    constexpr float SC = 0.17677669529663687f * 1.4426950408889634f;
    f32x4 sp[4], sc[4];
#pragma unroll
    for (int m = 0; m < 4; ++m) { sp[m] = mfma(K[m], Q[m], zero4); sc[m] = mfma(K[m + 1], Q[m], zero4); }
    f32x4 mp, mc;
#pragma unroll
    for (int r = 0; r < 4; ++r) {
      const int dc = i16 - (4 * g + r);
      mp[r] = ((dc & 1) == 0 && dc <= -2) ? 0.f : -INFINITY;
      mc[r] = ((dc & 1) == 0 && dc >= 0) ? 0.f : -INFINITY;
    }
    const float nohalo = (pm1 == 0 && rh == 0) ? -INFINITY : 0.f;
    float mx[4], sum[4];
#pragma unroll
    for (int m = 0; m < 4; ++m) {
      sp[m] = sp[m] * SC + (m == 0 ? mp + nohalo : mp);
      sc[m] = sc[m] * SC + mc;
      mx[m] = fmaxf(fmaxf(fmaxf(sp[m][0], sp[m][1]), fmaxf(sp[m][2], sp[m][3])), fmaxf(fmaxf(sc[m][0], sc[m][1]), fmaxf(sc[m][2], sc[m][3])));
    }
#pragma unroll
    for (int m = 0; m < 4; ++m) mx[m] = max4(mx[m]);
#pragma unroll
    for (int m = 0; m < 4; ++m) {
      sp[m] -= mx[m]; sc[m] -= mx[m];
#pragma unroll
      for (int r = 0; r < 4; ++r) { sp[m][r] = __builtin_amdgcn_exp2f(sp[m][r]); sc[m][r] = __builtin_amdgcn_exp2f(sc[m][r]); }
      const f32x4 t = sp[m] + sc[m];
      sum[m] = (t[0] + t[1]) + (t[2] + t[3]);
    }
#pragma unroll
    for (int m = 0; m < 4; ++m) sum[m] = __builtin_amdgcn_rcpf(sum4(sum[m]));
    f32x4 o[4][2];
#pragma unroll
    for (int m = 0; m < 4; ++m) {
      const bf16x8 P = cat8(pk4(sp[m] * sum[m]), pk4(sc[m] * sum[m]));
#pragma unroll
      for (int db = 0; db < 2; ++db) o[m][db] = mfma(cat8(V[m][db], V[m + 1][db]), P, zero4);
    }
#pragma unroll
    for (int m = 0; m < 4; ++m)
#pragma unroll
      for (int db = 0; db < 2; ++db)
        *(LAS u32x2*)(lds + L_H + img_off(rh * 64 + m * 16 + i16, hd * 4 + db * 2 + (g >> 1)) + (g & 1) * 8) = pk4(o[m][db]);
    wait_lgkm0(); bar();
    TS(2);
    copy16k(WS_O + (pm1 * 16 + pn1) * 16384);
    TS(3);
    publish(F_O + (pm1 * 16 + pn1) * 128);
  }
  const int wc = wid & 1, wr = wid >> 1;
  auto small_gemm = [&](auto NKC, auto FIRST) AI {
    using namespace gs;
    LANES;
    unsigned ab[2], bb[2];
#pragma unroll
    for (int kk = 0; kk < 2; ++kk) {
      ab[kk] = decltype(FIRST)::value ? a_base(lane, kk) + wr * 64 * 128 : (kk * 4 + g) * 2048 + (wr * 64 + i16) * 16;
      bb[kk] = AB + b128_base(lane, wc * 2 + kk);
    }
    constexpr int MS = decltype(FIRST)::value ? 2048 : 256;
    f32x4 acc[4][2];
#pragma unroll
    for (int m = 0; m < 4; ++m) acc[m][0] = acc[m][1] = zero4;
    auto load = [&](Fr& f, unsigned so, unsigned) {
#pragma unroll
      for (int kk = 0; kk < 2; ++kk) {
#pragma unroll
        for (int j = 0; j < 2; ++j)
#pragma unroll
          for (int hh = 0; hh < 2; ++hh) f.b[kk][j][hh] = lds_tr(lds + so + bb[j] + (kk * 32 + hh * 8) * 128);
#pragma unroll
        for (int m = 0; m < 4; ++m) f.a[kk][m] = lds_a(lds + so + ab[kk] + m * MS);
      }
    };
    auto mma = [&](Fr& f) {
#pragma unroll
      for (int kk = 0; kk < 2; ++kk)
#pragma unroll
        for (int m = 0; m < 4; ++m)
#pragma unroll
          for (int j = 0; j < 2; ++j) acc[m][j] = mfma(cat8(f.b[kk][j][0], f.b[kk][j][1]), f.a[kk][m], acc[m][j]);
    };
    Fr f0;
    unsigned sa = 0, sb = 0;
    bar();
    load(f0, 0u, 0u);
    deep_segment<decltype(NKC)::value, NS, ST, ST, 16, 16>(f0, sa, sb, load, mma);
    u32x2 res[4][2];
#pragma unroll
    for (int m = 0; m < 4; ++m)
#pragma unroll
      for (int j = 0; j < 2; ++j) res[m][j] = *(LAS u32x2*)(lds + L_H + img_off(wr * 64 + m * 16 + i16, wc * 4 + j * 2 + (g >> 1)) + (g & 1) * 8);
#pragma unroll
    for (int m = 0; m < 4; ++m)
#pragma unroll
      for (int j = 0; j < 2; ++j) {
        acc[m][j] += unpk4(res[m][j]);
        *(LAS u32x2*)(lds + L_H + img_off(wr * 64 + m * 16 + i16, wc * 4 + j * 2 + (g >> 1)) + (g & 1) * 8) = pk4(acc[m][j]);
      }
    if constexpr (decltype(FIRST)::value) {
      float s1[4], q1[4];
#pragma unroll
      for (int m = 0; m < 4; ++m) {
        const f32x4 sv = acc[m][0] + acc[m][1], qv = acc[m][0] * acc[m][0] + acc[m][1] * acc[m][1];
        s1[m] = (sv[0] + sv[1]) + (sv[2] + sv[3]); q1[m] = (qv[0] + qv[1]) + (qv[2] + qv[3]);
      }
#pragma unroll
      for (int m = 0; m < 4; ++m) { s1[m] = sum4(s1[m]); q1[m] = sum4(q1[m]); }
#pragma unroll
      for (int m = 0; m < 4; ++m) *(LAS f32x2*)(lds + L_P + (wc * 128 + wr * 64 + m * 16 + i16) * 8) = (f32x2){s1[m], q1[m]};
    }
    wait_lgkm0(); bar();
  };
  small_gemm(IC<16>{}, IC<1>{});
  TS(5);
  copy16k(WS_H + (pm2 * 16 + pn2) * 16384);
  if (wid == 0) {
    const f32x4 p = *(LAS f32x4*)(lds + L_P + lane * 16) + *(LAS f32x4*)(lds + L_P + 1024 + lane * 16);
    st16_dev(srdW, WS_S2R + (pm2 * 16 + pn2) * 1024 + lane * 16, __builtin_bit_cast(u32x4, p));
  }
  TS(6);
  publish(F_H + (pm2 * 16 + pn2) * 128);
  {
    using namespace gf;
    LANES;
    unsigned ab[2];
#pragma unroll
    for (int kk = 0; kk < 2; ++kk) ab[kk] = a_base(lane, kk) + wid * 32 * 128;
    const unsigned bb = B0 + b224_base(lane);
    f32x4 acc[2][7];
    LAS char *pa0 = lds + ab[0], *pa1 = lds + ab[1], *pb = lds + bb;
    unsigned sa = 0, sb = 0;
    auto rd = [&](Fr& f, int r) AI {
      const int kk = r >> 4, q = r & 15;
      if (q < 2) f.a[kk][q] = lds_a((kk ? pa1 : pa0) + q * 2048);
      else f.b[kk][(q - 2) >> 1][q & 1] = lds_tr(pb + ((q - 2) >> 1) * 32 + (kk * 32 + (q & 1) * 8) * 224);
    };
    auto advance = [&]() AI {
      sa += AB; sb += SEG; if (sa == NS * AB) { sa = 0; sb = 0; }
      pa0 = lds + (sa + ab[0]); pa1 = lds + (sa + ab[1]); pb = lds + (sb + bb);
    };
    auto tile = [&](Fr& c, Fr& n) AI {
      cbar();
#pragma unroll
      for (int i = 0; i < 28; ++i) {
        const int kk = i / 14, m = (i % 14) / 7, j = i % 7;
        asm volatile("v_mfma_f32_16x16x32_bf16 %0, %1, %2, %0" : "+v"(acc[m][j]) : "v"(cat8(c.b[kk][j][0], c.b[kk][j][1])), "v"(c.a[kk][m]));
        if (i < 16) { rd(n, 2 * i); rd(n, 2 * i + 1); }
        if (i == 17) { advance(); pin(pa0); pin(pa1); pin(pb); }
        __builtin_amdgcn_sched_barrier(0);
      }
    };
    Fr f0;
    auto segment = [&](int n) AI {
      Fr f1;
#pragma unroll 1
      for (int t = 0; t < n; t += 2) { tile(f0, f1); tile(f1, f0); }
      asm volatile("s_nop 7\n\ts_nop 1" ::: "memory");
      __builtin_amdgcn_sched_barrier(0);
    };
    auto act = [&](int seg) AI {
      float k1 = -2.302208198f, k2 = -0.1029432397f;
      pin(k1); pin(k2);
      float mu[2], rs[2];
#pragma unroll
      for (int m = 0; m < 2; ++m) { mu[m] = *(LAS float*)(lds + L_MU + (wid * 32 + m * 16 + i16) * 4); rs[m] = *(LAS float*)(lds + L_RS + (wid * 32 + m * 16 + i16) * 4); }
      f32x4 cs[7];
#pragma unroll
      for (int j = 0; j < 7; ++j) cs[j] = *(LAS f32x4*)(lds + L_CS + (seg * 112 + j * 16 + 4 * g) * 4);
      f32x2 e[14][2], xv[14][2];
#pragma unroll
      for (int u = 0; u < 14; ++u) { xv[u][0] = acc[u & 1][u >> 1].xy; xv[u][1] = acc[u & 1][u >> 1].zw; }
      auto pre = [&](int u, int st, int h) AI {
        if (u >= 14) return;
        if (st == 0) xv[u][h] = xv[u][h] - mu[u & 1] * (h ? cs[u >> 1].zw : cs[u >> 1].xy);
        if (st == 1) xv[u][h] = xv[u][h] * rs[u & 1];
        if (st == 2) e[u][h] = xv[u][h] * xv[u][h];
        if (st == 3) e[u][h] = e[u][h] * k2 + k1;
        if (st == 4) e[u][h] = e[u][h] * xv[u][h];
        if (st < 2) pin(xv[u][h]); else pin(e[u][h]);
      };
#pragma unroll
      for (int st = 0; st < 5; ++st) { pre(0, st, 0); pre(0, st, 1); }
#pragma unroll
      for (int u = 0; u < 16; ++u) {
        if (u >= 1 && u <= 14) { e[u - 1][0] = e[u - 1][0] + 1.0f; e[u - 1][1] = e[u - 1][1] + 1.0f; pin(e[u - 1][0]); pin(e[u - 1][1]); }
        __builtin_amdgcn_sched_barrier(0);
#pragma unroll
        for (int i = 0; i < 8; ++i) {
          if (i < 4) { if (u < 14) { e[u][i >> 1][i & 1] = __builtin_amdgcn_exp2f(e[u][i >> 1][i & 1]); pin(e[u][i >> 1]); } }
          else if (u >= 1 && u <= 14) { e[u - 1][(i - 4) >> 1][i & 1] = __builtin_amdgcn_rcpf(e[u - 1][(i - 4) >> 1][i & 1]); pin(e[u - 1][(i - 4) >> 1]); }
          if (i < 5) { pre(u + 1, i, 0); pre(u + 1, i, 1); }
          else if (i < 7 && u >= 2) { xv[u - 2][i - 5] = xv[u - 2][i - 5] * e[u - 2][i - 5]; pin(xv[u - 2][i - 5]); }
          __builtin_amdgcn_sched_barrier(0);
        }
      }
#pragma unroll
      for (int u = 0; u < 14; ++u) acc[u & 1][u >> 1] = (f32x4){xv[u][0][0], xv[u][0][1], xv[u][1][0], xv[u][1][1]};
    };
    u32x2 zz[2][7];
#pragma unroll
    for (int m = 0; m < 2; ++m)
#pragma unroll
      for (int j = 0; j < 7; ++j) acc[m][j] = zero4;
    bar();
#pragma unroll
    for (int r = 0; r < 32; ++r) rd(f0, r);
    advance();
    segment(16);
    TS(7);
    act(1);
    {
      float s1[2], q1[2];
#pragma unroll
      for (int m = 0; m < 2; ++m) {
        f32x4 sv = acc[m][0], qv = sv * sv;
#pragma unroll
        for (int j = 1; j < 7; ++j) { sv += acc[m][j]; qv += acc[m][j] * acc[m][j]; }
        s1[m] = sum4((sv[0] + sv[1]) + (sv[2] + sv[3])); q1[m] = sum4((qv[0] + qv[1]) + (qv[2] + qv[3]));
      }
      st8_dev(srdW, lane < 32 ? WS_S3 + (pm1 * 16 + pn1) * 1024 + (wid * 32 + lane) * 8 : 0xfffffff0u,
              (u32x2){min(__float_as_uint(g == 0 ? s1[0] : s1[1]), ~1u), min(__float_as_uint(g == 0 ? q1[0] : q1[1]), ~1u)});
    }
#pragma unroll
    for (int m = 0; m < 2; ++m)
#pragma unroll
      for (int j = 0; j < 7; ++j) { zz[m][j] = pk4(acc[m][j]); pin(zz[m][j]); acc[m][j] = zero4; }
    TS(8);
    segment(6);
    TS(9);
#pragma unroll
    for (int m = 0; m < 2; ++m) {
      const float m3 = *(LAS float*)(lds + L_M3 + (wid * 32 + m * 16 + i16) * 4), r3 = *(LAS float*)(lds + L_R3 + (wid * 32 + m * 16 + i16) * 4);
#pragma unroll
      for (int j = 0; j < 7; ++j)
        *(LAS u32x2*)(lds + L_ZS + (wid * 32 + m * 16 + swap23(i16)) * 240 + 8 * g + j * 32) = pk4((unpk4(zz[m][j]) - m3) * r3);
    }
    TS(10);
    segment(10);
    TS(11);
    act(0);
#pragma unroll
    for (int m = 0; m < 2; ++m) {
      const float b = (float)*(LAS __bf16*)(lds + L_C2 + (wid * 32 + m * 16 + i16) * 2);
#pragma unroll
      for (int j = 0; j < 7; ++j) { zz[m][j] = pk4(acc[m][j]); pin(zz[m][j]); acc[m][j] = zero4 + b; }
    }
    TS(12);
    segment(32);
    TS(13);
#pragma unroll
    for (int m = 0; m < 2; ++m)
#pragma unroll
      for (int j = 0; j < 7; ++j)
        *(LAS u32x2*)(lds + L_ZS + (j * 2 + (g >> 1)) * 2048 + (wid * 32 + m * 16 + i16) * 16 + (g & 1) * 8) = pk4(unpk4(zz[m][j]) * acc[m][j]);
    wait_lgkm0(); bar();
    TS(14);
#pragma unroll
    for (int i = 0; i < 7; ++i) st16_dev(srdW, WS_Y + (pm1 * 16 + pn1) * 28672 + (i * 256 + tid) * 16, *(LAS u32x4*)(lds + L_ZS + (i * 256 + tid) * 16));
    TS(15);
    publish(F_Y + (pm1 * 16 + pn1) * 128);
  }
  small_gemm(IC<28>{}, IC<0>{});
  TS(16);
#pragma unroll
  for (int i = 0; i < 4; ++i) {
    const int c = i * 256 + tid, r = c >> 3, ch = (c & 7) ^ ((r >> 1) & 7);
    *(u32x4*)(out + (size_t)(pm2 * 128 + r) * (DM * 2) + pn2 * 128 + ch * 16) = *(LAS u32x4*)(lds + L_H + c * 16);
  }
  if (blockIdx.x == 0 && tid == 0) __hip_atomic_store((gu64*)(ws + WS_EPOCH), tag, __ATOMIC_RELAXED, __HIP_MEMORY_SCOPE_AGENT);
  TS(17);
}

extern "C" void kernel_launch(void* const* d_in, const int* in_sizes, int n_in, void* d_out, int out_size, void* d_ws, size_t ws_size,
                              hipStream_t stream) {
  (void)hipFuncSetAttribute((const void*)k_mega, hipFuncAttributeMaxDynamicSharedMemorySize, LDS_BYTES);
  k_mega<<<256, 512, LDS_BYTES, stream>>>((const __bf16*)d_in[0], (const __bf16*)d_in[1], (const __bf16*)d_in[2], (const __bf16*)d_in[4],
                                          (const __bf16*)d_in[5], (const __bf16*)d_in[6], (const __bf16*)d_in[7], (char*)d_out, (char*)d_ws + (size_t)WSBASE * 1048576);
}
```

```cpp
#include <hip/hip_runtime.h>
#include <hip/hip_bf16.h>
#include <stdint.h>
#include <type_traits>

#define DI __device__ __forceinline__
#define LAS __attribute__((address_space(3)))
#define AI __attribute__((always_inline))
typedef __bf16 bf16x8 __attribute__((ext_vector_type(8)));
typedef __bf16 bf16x2 __attribute__((ext_vector_type(2)));
typedef short s16x4 __attribute__((ext_vector_type(4)));
typedef float f32x4 __attribute__((ext_vector_type(4)));
typedef float f32x2 __attribute__((ext_vector_type(2)));
typedef int i32x4 __attribute__((ext_vector_type(4)));
typedef unsigned u32x4 __attribute__((ext_vector_type(4)));
typedef unsigned u32x2 __attribute__((ext_vector_type(2)));
typedef unsigned long long u64;
typedef __attribute__((address_space(1))) u64 gu64;
typedef __attribute__((address_space(1))) unsigned gu32;
template <int V> using IC = std::integral_constant<int, V>;

#define POLL_SLEEP 1
#define CODEPF 1
#define XROT 1
#define WSBASE 0

constexpr int SEQ = 2048, DM = 1024, FF = 3584, FH = 1792;
constexpr float LN_EPS = 1e-5f;

constexpr unsigned WS_EPOCH = 0;
constexpr unsigned F_S1 = 32768, F_O = 2 * 32768, F_H = 3 * 32768, F_W3 = 4 * 32768, F_S3 = 5 * 32768, F_Z2 = 6 * 32768, F_Y = 7 * 32768;
constexpr unsigned WS_S1R = 1u << 18;
constexpr unsigned WS_S1C = WS_S1R + (1u << 18);
constexpr unsigned WS_S2R = WS_S1C + (1u << 18);
constexpr unsigned WS_S2C = WS_S2R + (1u << 18);
constexpr unsigned WS_S3 = WS_S2C + (1u << 18);
constexpr unsigned WS_O = 2u << 20;
constexpr unsigned WS_H = WS_O + (4u << 20);
constexpr unsigned WS_Z2 = WS_H + (4u << 20);
constexpr unsigned WS_Y = WS_Z2 + (8u << 20);
constexpr unsigned WS_W3 = WS_Y + (8u << 20);
constexpr unsigned WS_END = WS_W3 + (8u << 20);
constexpr size_t WS_TS = 64u << 20;
#define TS(i) do {} while (0)

constexpr unsigned L_WT = 98304;
constexpr unsigned L_ZS = 122880;
constexpr unsigned L_X = 129024;
constexpr unsigned L_H = 135168;
constexpr unsigned L_MISC = 153600;
constexpr unsigned L_MU = L_MISC, L_RS = L_MU + 576, L_CS = L_RS + 576, L_P = L_CS + 896, L_M3 = L_P + 2048, L_R3 = L_M3 + 512, L_TAG = L_R3 + 512,
                   L_C2 = L_TAG + 16;
constexpr unsigned LDS_BYTES = 163840;
static_assert(L_C2 + 2 * 896 <= LDS_BYTES, "lds");

DI i32x4 make_srd(const void* p, unsigned bytes) {
  u64 a = (u64)p;
  i32x4 r;
  r[0] = __builtin_amdgcn_readfirstlane((int)(unsigned)a);
  r[1] = __builtin_amdgcn_readfirstlane((int)(unsigned)(a >> 32));
  r[2] = (int)bytes;
  r[3] = 0x00020000;
  return r;
}
template <int POL> DI void dma16(i32x4 srd, unsigned voff, unsigned soff, unsigned ldsaddr) {
  if constexpr (POL == 2)
    asm volatile("s_mov_b32 m0, %0\n\ts_nop 0\n\tbuffer_load_dwordx4 %1, %2, %3 offen nt lds" :: "s"(ldsaddr), "v"(voff), "s"(srd), "s"(soff) : "memory");
  else if constexpr (POL == 1)
    asm volatile("s_mov_b32 m0, %0\n\ts_nop 0\n\tbuffer_load_dwordx4 %1, %2, %3 offen sc1 lds" :: "s"(ldsaddr), "v"(voff), "s"(srd), "s"(soff) : "memory");
  else
    asm volatile("s_mov_b32 m0, %0\n\ts_nop 0\n\tbuffer_load_dwordx4 %1, %2, %3 offen lds" :: "s"(ldsaddr), "v"(voff), "s"(srd), "s"(soff) : "memory");
}
template <int N> DI void dmaL(i32x4 srd, unsigned v, unsigned so, unsigned l) {
  if constexpr (N == 1) asm volatile("s_mov_b32 m0, %0\n\ts_nop 0\n\tbuffer_load_dwordx4 %1, %2, %3 offen sc1 lds" :: "s"(l), "v"(v), "s"(srd), "s"(so) : "memory");
  if constexpr (N == 2) asm volatile("s_mov_b32 m0, %0\n\ts_nop 0\n\tbuffer_load_dwordx4 %1, %2, %3 offen sc1 lds\n\tbuffer_load_dwordx4 %1, %2, %3 offen offset:1024 sc1 lds" :: "s"(l), "v"(v), "s"(srd), "s"(so) : "memory");
  if constexpr (N == 4) asm volatile("s_mov_b32 m0, %0\n\ts_nop 0\n\tbuffer_load_dwordx4 %1, %2, %3 offen sc1 lds\n\tbuffer_load_dwordx4 %1, %2, %3 offen offset:1024 sc1 lds\n\t"
                                     "buffer_load_dwordx4 %1, %2, %3 offen offset:2048 sc1 lds\n\tbuffer_load_dwordx4 %1, %2, %3 offen offset:3072 sc1 lds" :: "s"(l), "v"(v), "s"(srd), "s"(so) : "memory");
}
template <int N> DI void dmaS(i32x4 srd, const unsigned* v, unsigned so, unsigned l) {
  if constexpr (N == 1) asm volatile("s_mov_b32 m0, %0\n\ts_nop 0\n\tbuffer_load_dwordx4 %1, %2, %3 offen lds" :: "s"(l), "v"(v[0]), "s"(srd), "s"(so) : "memory");
  if constexpr (N == 2) asm volatile("s_mov_b32 m0, %0\n\ts_nop 0\n\tbuffer_load_dwordx4 %1, %2, %3 offen lds\n\tbuffer_load_dwordx4 %4, %2, %3 offen offset:1024 lds" :: "s"(l), "v"(v[0]), "s"(srd), "s"(so), "v"(v[1]) : "memory");
  if constexpr (N == 4) asm volatile("s_mov_b32 m0, %0\n\ts_nop 0\n\tbuffer_load_dwordx4 %1, %2, %3 offen lds\n\tbuffer_load_dwordx4 %4, %2, %3 offen offset:1024 lds\n\t"
                                     "buffer_load_dwordx4 %5, %2, %3 offen offset:2048 lds\n\tbuffer_load_dwordx4 %6, %2, %3 offen offset:3072 lds"
                                     :: "s"(l), "v"(v[0]), "s"(srd), "s"(so), "v"(v[1]), "v"(v[2]), "v"(v[3]) : "memory");
}
DI void ld16_async(u32x4& d, i32x4 srd, unsigned voff) { asm volatile("buffer_load_dwordx4 %0, %1, %2, 0 offen sc1" : "=v"(d) : "v"(voff), "s"(srd) : "memory"); }
DI void ld8_async(u32x2& d, i32x4 srd, unsigned voff) { asm volatile("buffer_load_dwordx2 %0, %1, %2, 0 offen sc1" : "=v"(d) : "v"(voff), "s"(srd) : "memory"); }
DI void touch(unsigned& d, i32x4 srd, unsigned voff) { asm volatile("buffer_load_dword %0, %1, %2, 0 offen" : "=v"(d) : "v"(voff), "s"(srd) : "memory"); }
template <int J0, int J1> DI void gather(u32x4 (&d)[16], i32x4 srd, unsigned off);
DI void ld16_plain(u32x4& d, i32x4 srd, unsigned voff, unsigned soff) { asm volatile("buffer_load_dwordx4 %0, %1, %2, %3 offen" : "=v"(d) : "v"(voff), "s"(srd), "s"(soff) : "memory"); }
template <class T> DI void pin(T& v) { asm volatile("" : "+v"(v)); }
DI void st16_dev(i32x4 srd, unsigned voff, u32x4 v) { asm volatile("buffer_store_dwordx4 %0, %1, %2, 0 offen sc1\n\ts_nop 1" :: "v"(v), "v"(voff), "s"(srd) : "memory"); }
DI void st8_dev(i32x4 srd, unsigned voff, u32x2 v) { asm volatile("buffer_store_dwordx2 %0, %1, %2, 0 offen sc1" :: "v"(v), "v"(voff), "s"(srd) : "memory"); }
DI void st4_dev(i32x4 srd, unsigned voff, unsigned v) { asm volatile("buffer_store_dword %0, %1, %2, 0 offen sc1" :: "v"(v), "v"(voff), "s"(srd) : "memory"); }
template <int N> DI void wait_vm() { asm volatile("s_waitcnt vmcnt(%0)" :: "i"(N) : "memory"); }
DI void wait_lgkm0() { __builtin_amdgcn_s_waitcnt(0xC07F); asm volatile("" ::: "memory"); }
DI void bar() { asm volatile("" ::: "memory"); __builtin_amdgcn_s_barrier(); asm volatile("" ::: "memory"); }
#define HBAR() bar()
#define ABAR() do { __builtin_amdgcn_sched_barrier(0); bar(); __builtin_amdgcn_sched_barrier(0); } while (0)
#define ABARS(N) do { _Pragma("unroll 1") for (int i_ = 0; i_ < (N); ++i_) ABAR(); } while (0)
DI unsigned pk2(float lo, float hi) { bf16x2 v = {(__bf16)lo, (__bf16)hi}; return __builtin_bit_cast(unsigned, v); }
DI u32x2 pk4(f32x4 v) { return (u32x2){pk2(v[0], v[1]), pk2(v[2], v[3])}; }
DI f32x2 unpk2(unsigned u) { return (f32x2){__uint_as_float(u << 16), __uint_as_float(u & 0xffff0000u)}; }
DI f32x4 unpk4(u32x2 u) { const f32x2 a = unpk2(u[0]), b = unpk2(u[1]); return (f32x4){a[0], a[1], b[0], b[1]}; }
DI bf16x8 cat8(u32x2 a, u32x2 b) { u32x4 v = {a[0], a[1], b[0], b[1]}; return __builtin_bit_cast(bf16x8, v); }
DI f32x4 mfma(bf16x8 a, bf16x8 b, f32x4 c) { return __builtin_amdgcn_mfma_f32_16x16x32_bf16(a, b, c, 0, 0, 0); }
DI bf16x8 lds_a(LAS char* p) { return *(LAS bf16x8*)p; }
DI u32x2 lds_tr(LAS char* p) { return __builtin_bit_cast(u32x2, __builtin_amdgcn_ds_read_tr16_b64_v4i16((LAS s16x4*)p)); }
#undef lds_tr
DI int swap23(int k) { return (k & ~12) | ((k & 4) << 1) | ((k & 8) >> 1); }
DI float sum4(float x) {
  auto r = __builtin_amdgcn_permlane16_swap(__float_as_uint(x), __float_as_uint(x), false, false);
  x = __uint_as_float(r[0]) + __uint_as_float(r[1]);
  auto s = __builtin_amdgcn_permlane32_swap(__float_as_uint(x), __float_as_uint(x), false, false);
  return __uint_as_float(s[0]) + __uint_as_float(s[1]);
}
DI float max4(float x) {
  auto r = __builtin_amdgcn_permlane16_swap(__float_as_uint(x), __float_as_uint(x), false, false);
  x = fmaxf(__uint_as_float(r[0]), __uint_as_float(r[1]));
  auto s = __builtin_amdgcn_permlane32_swap(__float_as_uint(x), __float_as_uint(x), false, false);
  return fmaxf(__uint_as_float(s[0]), __uint_as_float(s[1]));
}
DI f32x2 mean_rstd(f32x2 t, float inv_n) {
  const float mu = t[0] * inv_n, var = fmaxf(t[1] * inv_n - mu * mu, 0.f);
  return (f32x2){mu, __builtin_amdgcn_rsqf(var + LN_EPS)};
}
DI u64 uniform64(u64 v) {
  return ((u64)(unsigned)__builtin_amdgcn_readfirstlane((int)(unsigned)(v >> 32)) << 32) | (unsigned)__builtin_amdgcn_readfirstlane((int)(unsigned)v);
}
DI int fresh_tid() { int t = threadIdx.x; asm volatile("" : "+v"(t)); return t; }
DI void set_flag(char* ws, unsigned flag_off, u64 tag) { __hip_atomic_store((gu64*)(ws + flag_off), tag, __ATOMIC_RELAXED, __HIP_MEMORY_SCOPE_AGENT); }
DI void await(char* ws, unsigned flag_off, u64 tag) {
#pragma unroll 1
  for (unsigned spin = 0; spin < (1u << 17); ++spin) {
    const u64 f = __hip_atomic_load((gu64*)(ws + flag_off), __ATOMIC_RELAXED, __HIP_MEMORY_SCOPE_AGENT);
    if (__all(f == tag)) break;
    __builtin_amdgcn_s_sleep(POLL_SLEEP);
  }
  asm volatile("" ::: "memory");
}
DI unsigned row_flags(unsigned f, int pm, int lane) { return f + (pm * 16 + (lane & 15)) * 128; }
DI unsigned col_flags(unsigned f, int pn, int lane) { return f + ((lane & 15) * 16 + pn) * 128; }
template <int J0, int J1> DI void gather(u32x4 (&d)[16], i32x4 srd, unsigned off) {
#pragma unroll
  for (int j = J0; j < J1; ++j) ld16_async(d[j], srd, off + j * 1024);
}
template <int J0, int J1> DI void accum(f32x4& t, u32x4 (&d)[16]) {
#pragma unroll
  for (int j = J0; j < J1; ++j) { pin(d[j]); t = j == 0 ? __builtin_bit_cast(f32x4, d[j]) : t + __builtin_bit_cast(f32x4, d[j]); }
  pin(t);
}
DI void put_rows(LAS char* lds, unsigned l_mu, unsigned l_rs, int row2, f32x4 t, float inv_n, bool on = true) {
  const f32x2 a = mean_rstd((f32x2){t[0], t[1]}, inv_n), b = mean_rstd((f32x2){t[2], t[3]}, inv_n);
  if (on) { *(LAS f32x2*)(lds + l_mu + row2 * 8) = (f32x2){a[0], b[0]}; *(LAS f32x2*)(lds + l_rs + row2 * 8) = (f32x2){a[1], b[1]}; }
}
DI unsigned await_mask(char* ws, unsigned flag_off, u64 tag, unsigned need) {
  unsigned seen = 0;
#pragma unroll 1
  for (unsigned spin = 0; spin < (1u << 17); ++spin) {
    const u64 f = __hip_atomic_load((gu64*)(ws + flag_off), __ATOMIC_RELAXED, __HIP_MEMORY_SCOPE_AGENT);
    seen = (unsigned)__ballot(f == tag) & 0xffffu;
    if ((seen & need) == need) break;
    __builtin_amdgcn_s_sleep(POLL_SLEEP);
  }
  asm volatile("" ::: "memory");
  return seen;
}

DI int b_pos_lane(int lane) { const int g = lane >> 4, q = (lane & 15) >> 2; return (g >> 1) * 16 + (g & 1) * 4 + q; }
DI unsigned b128_base(int lane, int nb) { const int pos = b_pos_lane(lane); return pos * 128 + ((nb ^ ((pos >> 1) & 3)) * 32) + (lane & 3) * 8; }
DI unsigned b224_base(int lane) { return b_pos_lane(lane) * 224 + (lane & 3) * 8; }
DI unsigned a_base(int lane, int kk) { const int r = lane & 15, g = lane >> 4; return r * 128 + (((kk * 4 + g) ^ ((r >> 1) & 7)) * 16); }
DI unsigned img_off(int row, int chunk) { return row * 128 + ((chunk ^ ((row >> 1) & 7)) * 16); }

#define SGB(mask, n) __builtin_amdgcn_sched_group_barrier(mask, n, 0)
template <int NR, int NM> DI void interleave() {
  constexpr int RP = NR > NM - 2 ? 2 : 1;
  SGB(0x8, 1);
  SGB(0x2, 8);
#pragma unroll
  for (int i = 0; i < (NR + RP - 1) / RP; ++i) { SGB(0x100, RP); SGB(0x8, 1); }
  SGB(0x8, NM);
}
DI void cbar() {
  __builtin_amdgcn_sched_barrier(0);
  __builtin_amdgcn_s_waitcnt(0xC07F);
  bar();
  __builtin_amdgcn_sched_barrier(0);
}
template <int NK, int NS, int SA, int SB, int NR, int NM, class F, class L, class M>
DI void compute_loop(L load, M mma) {
  F f0, f1;
  bar();
  load(f0, 0u, 0u, 0);
  unsigned sa = 0, sb = 0;
  __builtin_amdgcn_sched_barrier(0);
#pragma unroll 1
  for (int t = 0; t < NK - 1; ++t) {
    load(f1, sa, sb, 1);
    mma(f0);
    interleave<NR, NM>();
    cbar();
    sa += SA; sb += SB; if (sa == NS * SA) { sa = 0; sb = 0; }
    load(f0, sa, sb, 0);
    mma(f1);
    interleave<NR, NM>();
    __builtin_amdgcn_sched_barrier(0);
  }
  load(f1, sa, sb, 1);
  mma(f0);
  interleave<NR, NM>();
  cbar();
  mma(f1);
}

template <int N, int NS, int SA, int SB, int NR, int NM, class F, class L, class M>
DI void deep_segment(F& f0, unsigned& sa, unsigned& sb, L load, M mma) {
  F f1;
#pragma unroll 1
  for (int t = 0; t < N; t += 2) {
    cbar();
    sa += SA; sb += SB; if (sa == NS * SA) { sa = 0; sb = 0; }
    load(f1, sa, sb);
    mma(f0);
    interleave<NR, NM>();
    cbar();
    sa += SA; sb += SB; if (sa == NS * SA) { sa = 0; sb = 0; }
    load(f0, sa, sb);
    mma(f1);
    interleave<NR, NM>();
  }
  __builtin_amdgcn_sched_barrier(0);
}

template <int NK_, int NS_, int XP_, int WP_> struct Pipe {
  static constexpr int NK = NK_, NS = NS_, XP = XP_, PW = XP_ + WP_;
  static constexpr int allowed(int t) {
    int n = 0;
    for (int s = t + 2; s <= t + NS - 1 && s < NK; ++s) n += s < NS ? XP : PW;
    return n;
  }
};
template <class P, int T, int EXTRA = 0, class IX, class IW> DI void hstep(IX& ix, IW& iw, int& slot) {
  wait_vm<P::allowed(T) + EXTRA>();
  HBAR();
  if constexpr (T + P::NS < P::NK) { iw(T + P::NS, slot); ix(T + P::NS, slot); }
  slot = slot + 1 == P::NS ? 0 : slot + 1;
}
template <class P, int T0, int T1, class IX, class IW> DI void hsteps(IX& ix, IW& iw, int& slot) {
  if constexpr (T0 < T1) {
    if constexpr (T0 >= P::NS - 2 && T1 <= P::NK - P::NS && T1 - T0 > 1) {
#pragma unroll 1
      for (int t = T0; t < T1; ++t) {
        wait_vm<(P::NS - 2) * P::PW>();
        HBAR();
        iw(t + P::NS, slot); ix(t + P::NS, slot);
        slot = slot + 1 == P::NS ? 0 : slot + 1;
      }
    } else if constexpr (T0 >= P::NS - 2 && T0 + 1 < P::NK - P::NS && T1 > P::NK - P::NS) {
      hsteps<P, T0, P::NK - P::NS>(ix, iw, slot);
      hsteps<P, P::NK - P::NS, T1>(ix, iw, slot);
    } else {
      hstep<P, T0>(ix, iw, slot);
      hsteps<P, T0 + 1, T1>(ix, iw, slot);
    }
  }
}
template <class P, class IX> DI void hstart(IX& ix) {
  ix(0, 0); ix(1, 1);
  wait_vm<P::XP>();
  HBAR();
#pragma unroll
  for (int s = 2; s < P::NS; ++s) ix(s, s);
}
struct Nx { unsigned a0, a1, b, f, la0, la1, lb, lf; };
template <class T> DI void spin(T& v) { asm volatile("" : "+s"(v)); }
template <int T0, int T1, int WAIT, class MK, class G0, class G1, class G2, class G3>
DI void steady(MK mk, G0 g0, G1 g1, G2 g2, G3 g3) {
  static_assert(((T1 - T0) & 1) == 0, "even");
  Nx c = mk(T0 + 4), n;
  auto one = [&](Nx& p, Nx& q, int u) AI {
    wait_vm<WAIT>();
    bar();
    g0(p.a0, p.la0);
    q = mk(u);
    spin(q.a0); spin(q.a1); spin(q.b); spin(q.f); spin(q.la0); spin(q.la1); spin(q.lb); spin(q.lf);
    g1(p.b, p.lb);
    g2(p.a1, p.la1);
    g3(p.f, p.lf);
  };
#pragma unroll 1
  for (int u = T0 + 5; u < T1 + 5; u += 2) { one(c, n, u); one(n, c, u + 1); }
}
DI int rot(int t, int r0, int nk) { const int k = t + r0; return k >= nk ? k - nk : k; }

namespace g1 { constexpr int NK = 16, NS = 3, AB = 144 * 128, ST = AB + 3 * 8192; struct Fr { bf16x8 a[5]; u32x2 b[6][2]; }; }
namespace gs { constexpr int NS = 4, AB = 16384, ST = AB + 8192; struct Fr { bf16x8 a[2][4]; u32x2 b[2][2][2]; }; }
namespace gf { constexpr int NK = 64, NS = 4, AB = 16384, SEG = 14336, B0 = NS * AB; struct Fr { bf16x8 a[2][2]; bf16x8 b[2][7]; }; }

__global__ __launch_bounds__(512, 2) void k_mega(const __bf16* __restrict__ x, const __bf16* __restrict__ wqkv, const __bf16* __restrict__ wo,
                                                 const __bf16* __restrict__ win, const __bf16* __restrict__ wsp, const __bf16* __restrict__ bsp,
                                                 const __bf16* __restrict__ wout, char* __restrict__ out, char* __restrict__ ws) {
  extern __shared__ __attribute__((aligned(16))) char smem_[];
  LAS char* lds = (LAS char*)smem_;
  const int tid = threadIdx.x, lane = tid & 63, wid = __builtin_amdgcn_readfirstlane(tid >> 6);
  const int xcd = blockIdx.x & 7, idx = blockIdx.x >> 3;
  const int pm1 = (idx & 7) * 2 + (xcd & 1), pn1 = (idx >> 3) * 4 + (xcd >> 1);
  const int pm2 = (idx & 3) * 4 + (xcd & 3), pn2 = (idx >> 2) * 2 + (xcd >> 2);
  const i32x4 srdW = make_srd(ws, WS_END);
  TS(0);

  const bf16x8 ones = __builtin_bit_cast(bf16x8, (u32x4){0x3f803f80u, 0x3f803f80u, 0x3f803f80u, 0x3f803f80u});
  const f32x4 zero4 = {0.f, 0.f, 0.f, 0.f};
  const int r1 = (((xcd >> 1) + (xcd & 1) * 2 + (idx & 1)) & 3) * 4;
  const int srow = idx >> 3, scol = (idx & 7) >> 1;
  auto rowstat = [&](bf16x8 a0, bf16x8 a1) AI {
    const f32x4 sm = mfma(ones, a1, mfma(ones, a0, zero4)), sq = mfma(a1, a1, mfma(a0, a0, zero4));
    return (f32x2){sm[0], (lane & 2) ? ((lane & 1) ? sq[3] : sq[2]) : ((lane & 1) ? sq[1] : sq[0])};
  };
  const bool diag_lane = ((lane & 15) >> 2) == (lane >> 4);
  auto g1_first_steps = [&](int hw, auto between) AI {
    using namespace g1;
#pragma unroll 1
    for (int t = 0; t < 4; ++t) {
      bf16x8 ra[2][2]; u32x2 rb[3][4];
      const unsigned so = (t == 3 ? 0 : t) * ST;
      if (t == srow) {
#pragma unroll
        for (int m = 0; m < 2; ++m)
#pragma unroll
          for (int kk = 0; kk < 2; ++kk) ra[m][kk] = lds_a(lds + so + a_base(lane, kk) + (hw * 2 + m) * 2048);
      }
      if (t == scol) {
#pragma unroll
        for (int i = 0; i < 3; ++i) {
          const int n = hw * 3 + i;
          const unsigned cb = so + AB + (n >> 2) * 8192 + b128_base(lane, n & 3);
#pragma unroll
          for (int j = 0; j < 4; ++j) rb[i][j] = lds_tr(lds + cb + ((j >> 1) * 32 + (j & 1) * 8) * 128);
        }
      }
      wait_lgkm0();
      between(t);
      if (t == srow) {
        const f32x2 r0 = rowstat(ra[0][0], ra[0][1]), r1 = rowstat(ra[1][0], ra[1][1]);
        if (diag_lane) { *(LAS f32x2*)(lds + L_P + (hw * 32 + (lane & 15)) * 8) = r0; *(LAS f32x2*)(lds + L_P + (hw * 32 + 16 + (lane & 15)) * 8) = r1; }
      }
      if (t == scol) {
#pragma unroll
        for (int i = 0; i < 3; ++i)
          *(LAS float*)(lds + L_P + 1024 + ((hw * 3 + i) * 16 + (lane & 15)) * 4) =
              mfma(ones, cat8(rb[i][2], rb[i][3]), mfma(ones, cat8(rb[i][0], rb[i][1]), zero4))[0];
      }
    }
    wait_lgkm0();
  };

  if (wid == 7) {
    const unsigned l16 = lane * 16;
    ABAR();
    const u64 tag = uniform64(*(LAS u64*)(lds + L_TAG));
    const u32x2 tag2 = __builtin_bit_cast(u32x2, tag);
    const unsigned myflag = lane < 16 ? (pm1 * 16 + pn1) * 128 + lane * 8 : 0xff000000u;
    g1_first_steps(3, [&](int) { ABAR(); });
    ABAR();
    st16_dev(srdW, WS_S1R + (pm1 * 16 + ((srow + r1) & 15)) * 1024 + l16, *(LAS u32x4*)(lds + L_P + l16));
    st16_dev(srdW, WS_S1C + (pn1 * 16 + ((scol + r1) & 15)) * 1024 + l16, *(LAS u32x4*)(lds + L_P + 1024 + l16));
    st16_dev(srdW, WS_S3 + (pm1 * 16 + pn1) * 1024 + l16, (u32x4){~0u, ~0u, ~0u, ~0u});
    ABARS(2);
    wait_vm<0>();
    st8_dev(srdW, F_S1 + myflag, tag2);
    ABARS(2);
    {
      const int pmh = pm1 > 0 ? pm1 - 1 : 0;
      const unsigned fo = lane < 16 ? row_flags(F_S1, pm1, lane) : lane < 32 ? row_flags(F_S1, pmh, lane) : col_flags(F_S1, pn1, lane);
      u32x2 f0;
      ld8_async(f0, srdW, fo);
      ABAR();
      wait_vm<0>();
      pin(f0);
      if (!__all(__builtin_bit_cast(u64, f0) == tag)) await(ws, fo, tag);
      const unsigned ro = WS_S1R + pm1 * 16384 + l16, co = lane < 48 ? WS_S1C + pn1 * 16384 + l16 : WS_S1R + pmh * 16384 + (56 + (lane & 7)) * 16;
      u32x4 gr[16], gc[16];
      f32x4 tr, tc;
      gather<0, 12>(gr, srdW, ro);
      ABAR();
      gather<12, 16>(gr, srdW, ro);
      gather<0, 8>(gc, srdW, co);
      ABAR();
      gather<8, 16>(gc, srdW, co);
      wait_vm<16>();
      accum<0, 16>(tr, gr);
      ABAR();
      put_rows(lds, L_MU, L_RS, lane, tr, 1.f / DM);
      wait_vm<0>();
      accum<0, 8>(tc, gc);
      ABAR();
      accum<8, 16>(tc, gc);
      if (lane < 48) *(LAS f32x4*)(lds + L_CS + l16) = tc;
      put_rows(lds, L_MU, L_RS, 64 + (lane & 7), tc, 1.f / DM, lane >= 48 && lane < 56);
      wait_lgkm0();
      ABAR();
    }
    u32x4 xt[16];
    {
      const i32x4 srdX = make_srd(x, SEQ * DM * 2);
#pragma unroll
      for (int i = 0; i < 16; ++i) {
        const int r = i * 8 + (lane >> 3);
        ld16_plain(xt[i], srdX, (lane >> 3) * (DM * 2) + (((lane & 7) ^ ((r >> 1) & 7)) * 16), (pm2 * 128 + i * 8) * (DM * 2) + pn2 * 128);
        if (i == 7) ABAR();
      }
    }
    {
      const i32x4 srdWin = make_srd(win, DM * FF * 2);
      int pos = lane / 14, gi = lane - pos * 14;
#pragma unroll
      for (int i = 0; i < 14; ++i) {
        const unsigned v = swap23(pos) * (FF * 2) + (pn1 * 112 + gi * 8) * 2;
        dma16<0>(srdWin, v, pm1 * (64u * FF * 2), L_WT + i * 1024);
        dma16<0>(srdWin, v, pm1 * (64u * FF * 2) + FH * 2, L_WT + 14336 + i * 1024);
        gi += 8; pos += 4; if (gi >= 14) { gi -= 14; pos += 1; }
        if (i == 5) ABAR();
      }
    }
    ABARS(2);
    wait_vm<28>();
#pragma unroll
    for (int i = 0; i < 16; ++i) { pin(xt[i]); *(LAS u32x4*)(lds + L_H + i * 1024 + l16) = xt[i]; }
    const int r2 = XROT * (xcd & 3) * 4;
    unsigned seen = await_mask(ws, row_flags(F_O, pm2, lane), tag, 0xfu << r2);
    wait_lgkm0();
    ABAR();
    auto gate = [&](unsigned fo, unsigned need) AI { if ((seen & need) != need) seen = await_mask(ws, fo, tag, need); };
    {
      const unsigned cb = L_WT + b224_base(lane);
      bf16x8 fr[2][2][2];
      float cs4[4];
      auto rd2 = [&](int q) AI {
#pragma unroll
        for (int u = 0; u < 2; ++u) {
          const int n = q * 2 + u;
          LAS char* p = lds + cb + (n / 7) * 14336 + (n % 7) * 32;
          fr[q & 1][u][0] = cat8(lds_tr(p), lds_tr(p + 8 * 224));
          fr[q & 1][u][1] = cat8(lds_tr(p + 32 * 224), lds_tr(p + 40 * 224));
        }
      };
      rd2(0);
#pragma unroll
      for (int q = 0; q < 7; ++q) {
        if (q > 0) gate(row_flags(F_O, pm2, lane), 1u << ((q + 3 + r2) & 15));
        ABAR();
#pragma unroll
        for (int u = 0; u < 2; ++u) {
          const int n = q * 2 + u;
#pragma unroll
          for (int kk = 0; kk < 2; ++kk)
            st16_dev(srdW, WS_W3 + (pn1 * 16 + pm1) * 28672 + (n / 7) * 14336 + (kk * 7 + n % 7) * 1024 + l16, __builtin_bit_cast(u32x4, fr[q & 1][u][kk]));
          cs4[n & 3] = mfma(ones, fr[q & 1][u][1], mfma(ones, fr[q & 1][u][0], zero4))[0];
        }
        if (q < 6) rd2(q + 1);
        if ((q & 1) || q == 6)
          st4_dev(srdW, WS_S2C + (pn1 * 16 + pm1) * 1024 + (q >> 1) * 256 + lane * 4,
                  __float_as_uint(lane < 32 ? (lane < 16 ? cs4[0] : cs4[1]) : (lane < 48 ? cs4[2] : cs4[3])));
      }
    }
    gate(row_flags(F_O, pm2, lane), 0xffffu);
    ABARS(2);
    wait_vm<0>();
    st8_dev(srdW, F_W3 + myflag, tag2);
    ABARS(5);
    {
      u32x2 f0;
      const unsigned fo = col_flags(F_W3, pn1, lane);
      ld8_async(f0, srdW, fo);
      ABARS(2);
      wait_vm<0>();
      pin(f0);
      if (!__all(__builtin_bit_cast(u64, f0) == tag)) await(ws, fo, tag);
    }
    ABAR();
    ABAR();
    u32x4 hpark[16];
#pragma unroll
    for (int i = 0; i < 16; ++i) hpark[i] = *(LAS u32x4*)(lds + L_H + i * 1024 + l16);
    wait_lgkm0();
    {
      u32x4 gc[16];
      f32x4 tc;
      gather<0, 16>(gc, srdW, WS_S2C + pn1 * 16384 + l16);
      dma16<0>(make_srd(bsp, SEQ * 2), lane < 16 ? pm1 * 256 + l16 : 0xfffffff0u, 0u, L_C2);
      ABAR();
      wait_vm<0>();
      accum<0, 16>(tc, gc);
      if (lane < 56) *(LAS f32x4*)(lds + L_CS + l16) = tc;
    }
    seen = await_mask(ws, row_flags(F_H, pm1, lane), tag, 0xfu);
    ABAR();
    ABAR();
#pragma unroll 1
    for (int t = 0; t < 4; ++t) { gate(row_flags(F_H, pm1, lane), 16u << t); ABAR(); }
    gate(row_flags(F_H, pm1, lane), 0xffffu);
    {
      u32x4 gr[16];
      f32x4 tr;
      const unsigned ro = WS_S2R + pm1 * 16384 + l16;
      ABAR();  gather<0, 4>(gr, srdW, ro);
      ABAR();  gather<4, 8>(gr, srdW, ro);
      ABAR();  gather<8, 12>(gr, srdW, ro);
      ABAR();  gather<12, 16>(gr, srdW, ro); wait_vm<8>(); accum<0, 8>(tr, gr);
      ABARS(2);
      wait_vm<0>(); accum<8, 16>(tr, gr);
      ABAR();
      put_rows(lds, L_MU, L_RS, lane, tr, 1.f / DM);
      wait_lgkm0();
    }
    ABARS(5);
    unsigned tch[4];
    {
      const i32x4 srdSp = make_srd(wsp, SEQ * SEQ * 2);
#pragma unroll
      for (int i = 0; i < 4; ++i)
        touch(tch[i], srdSp, (pm1 * 128 + (i & 1) * 64 + lane) * (SEQ * 2) + (XROT * (xcd >> 1) * 8 + (idx >> 3) * 2 + (i >> 1)) * 128);
    }
    ABARS(2);
    {
      u32x4 gv[16];
      f32x4 t;
      unsigned mx = 0;
      const unsigned so = WS_S3 + pm1 * 16384 + l16;
      gather<0, 8>(gv, srdW, so);
      ABAR();
      gather<8, 16>(gv, srdW, so);
      ABAR();
      wait_vm<8>();
#pragma unroll
      for (int i = 0; i < 4; ++i) pin(tch[i]);
      accum<0, 8>(t, gv);
#pragma unroll
      for (int j = 0; j < 8; ++j) mx = max(max(mx, gv[j][1]), gv[j][3]);
      pin(mx);
      ABAR();
      wait_vm<0>();
      accum<8, 16>(t, gv);
#pragma unroll
      for (int j = 8; j < 16; ++j) mx = max(max(mx, gv[j][1]), gv[j][3]);
      if (!__all(mx != ~0u)) {
#pragma unroll 1
        for (unsigned spin = 0; spin < (1u << 17); ++spin) {
          u32x4 gw[16];
          gather<0, 16>(gw, srdW, so);
          wait_vm<0>();
          accum<0, 16>(t, gw);
          mx = 0;
#pragma unroll
          for (int j = 0; j < 16; ++j) mx = max(max(mx, gw[j][1]), gw[j][3]);
          if (__all(mx != ~0u)) break;
        }
      }
      ABAR();
      put_rows(lds, L_M3, L_R3, lane, t, 1.f / FH);
      wait_lgkm0();
    }
    ABARS(4);
    st8_dev(srdW, F_Z2 + myflag, tag2);
    ABARS(3);
    {
      u32x2 f0;
      const unsigned fo = col_flags(F_Z2, pn1, lane);
      ld8_async(f0, srdW, fo);
      ABARS(2);
      wait_vm<0>();
      pin(f0);
      if (!__all(__builtin_bit_cast(u64, f0) == tag)) await(ws, fo, tag);
    }
    ABAR();
    {
      const i32x4 srdO = make_srd(wout, FH * DM * 2);
#pragma unroll
      for (int i = 0; i < 2; ++i) {
        const int k = (idx & 3) * 128 + i * 64 + lane;
        touch(tch[i], srdO, k < 448 ? (XROT * (xcd & 3) * 448 + k) * (DM * 2) + pn2 * 128 : 0xfffffff0u);
      }
    }
    ABARS(35);
#pragma unroll
    for (int i = 0; i < 16; ++i) *(LAS u32x4*)(lds + L_H + i * 1024 + l16) = hpark[i];
    auto ymask = [&](int u) AI { const int k = rot(u, XROT * (xcd & 3) * 7, 28); return (1u << (k * 8 / 14)) | (1u << ((k * 8 + 7) / 14)); };
    seen = await_mask(ws, row_flags(F_Y, pm2, lane), tag, ymask(0) | ymask(1) | ymask(2) | ymask(3));
    pin(tch[0]); pin(tch[1]);
    wait_lgkm0();
    ABARS(2);
#pragma unroll 1
    for (int t = 0; t < 8; ++t) { gate(row_flags(F_Y, pm2, lane), ymask(t + 4)); ABAR(); }
    gate(row_flags(F_Y, pm2, lane), 0xffffu);
    ABARS(21);
    return;
  }

  if (wid >= 4) {
    const int dw = wid - 4;
    int slot;
    const unsigned l16 = lane * 16;
    {
      using namespace g1;
      using P = Pipe<NK, NS, 14, 0>;
      const i32x4 srdA = make_srd(x, SEQ * DM * 2), srdB = make_srd(wqkv, DM * 3 * DM * 2);
      unsigned va[6], vb[8];
#pragma unroll
      for (int i = 0; i < 8; ++i) {
        const int p = dw * 8 + i, pos = (p & 7) * 8 + (lane >> 3), nb = ((lane & 7) >> 1) ^ ((pos >> 1) & 3);
        vb[i] = swap23(pos) * (3 * DM * 2) + (dw * DM + pn1 * 64 + nb * 16 + (lane & 1) * 8) * 2 - (i & 3) * 1024;
      }
#pragma unroll
      for (int i = 0; i < 6; ++i) {
        const int r = (dw * 6 + i) * 8 + (lane >> 3);
        int grow = r < 128 ? pm1 * 128 + r : pm1 * 128 - 16 + (r - 128);
        grow = grow < 0 ? 8 : grow;
        va[i] = grow * (DM * 2) + (((lane & 7) ^ ((r >> 1) & 7)) * 16) - (i & 3) * 1024;
      }
      auto ia = [&](int t, int s) {
        const unsigned kt = (t + r1) & 15;
        dmaS<4>(srdA, va, kt * 128u, s * ST + dw * 6144u);
        dmaS<2>(srdA, va + 4, kt * 128u, s * ST + dw * 6144u + 4096u);
      };
      auto ib = [&](int t, int s) {
        const unsigned kt = (t + r1) & 15;
        dmaS<4>(srdB, vb, kt * (64u * 3 * DM * 2), s * ST + AB + dw * 8192u);
        dmaS<4>(srdB, vb + 4, kt * (64u * 3 * DM * 2), s * ST + AB + dw * 8192u + 4096u);
      };
      ib(0, 0); ia(0, 0); ib(1, 1);
      wait_vm<8>();
      HBAR();
      ia(1, 1); ia(2, 2); ib(2, 2);
      TS(14);
      slot = 0;
      g1_first_steps(dw, [&](int t) {
        wait_vm<P::PW>();
        HBAR();
        ia(t + NS, slot); ib(t + NS, slot);
        slot = slot + 1 == NS ? 0 : slot + 1;
      });
      TS(15);
      hsteps<P, 4, NK>(ia, ib, slot);
    }
    TS(1);
    auto lin16 = [&](unsigned src, unsigned dst) AI {
      const unsigned g0 = dw * 4096u, g1 = 12288u + dw * 1024u;
      dmaL<4>(srdW, l16, src + g0, dst + g0);
      if (dw == 2) dmaL<2>(srdW, l16, src + g1, dst + g1);
      else dmaL<1>(srdW, l16, src + g1, dst + g1);
    };
    unsigned vbs[3];
#pragma unroll
    for (int i = 0; i < 3; ++i) {
      const int pos = (i < 2 ? dw * 2 + i : (6 + dw) & 7) * 8 + (lane >> 3), nb = ((lane & 7) >> 1) ^ ((pos >> 1) & 3);
      vbs[i] = swap23(pos) * (DM * 2) + (pn2 * 64 + nb * 16 + (lane & 1) * 8) * 2 - (i == 1 ? 1024 : 0);
    }
    auto w8 = [&](i32x4 srd, unsigned kt, int s) AI {
      dmaS<2>(srd, vbs, kt * (64u * DM * 2), s * gs::ST + gs::AB + dw * 2048u);
      if (dw != 2) dmaS<1>(srd, vbs + 2, kt * (64u * DM * 2), s * gs::ST + gs::AB + (6 + dw) * 1024u);
    };
    const unsigned vflex = dw == 2 ? l16 : vbs[2];
    auto gs_steady = [&](auto T1, i32x4 srdB, unsigned abase, int r0, int nk) AI {
      i32x4 srdF;
#pragma unroll
      for (int e = 0; e < 4; ++e) srdF[e] = dw == 2 ? srdW[e] : srdB[e];
      steady<2, decltype(T1)::value, 16>(
          [&](int u) AI {
            const unsigned kt = rot(u, r0, nk), a = abase + kt * 16384u, b = kt * (64u * DM * 2), l = (u & 3) * gs::ST;
            return Nx{a + dw * 4096u, a + 12288u + dw * 1024u, b, dw == 2 ? a + 15360u : b,
                      l + dw * 4096u, l + 12288u + dw * 1024u, l + gs::AB + dw * 2048u, l + (dw == 2 ? 15360u : gs::AB + (6 + dw) * 1024u)};
          },
          [&](unsigned so, unsigned l) AI { dmaL<4>(srdW, l16, so, l); }, [&](unsigned so, unsigned l) AI { dmaS<2>(srdB, vbs, so, l); },
          [&](unsigned so, unsigned l) AI { dmaL<1>(srdW, l16, so, l); }, [&](unsigned so, unsigned l) AI { dmaL<1>(srdF, vflex, so, l); });
    };
    {
      using namespace gs;
      constexpr int NK = 16;
      using P = Pipe<NK, NS, 5, 3>;
      const i32x4 srdB = make_srd(wo, DM * DM * 2);
      const int r0 = XROT * (xcd & 3) * 4;
      auto ix = [&](int t, int s) { lin16(WS_O + (pm2 * 16 + ((t + r0) & 15)) * 16384u, s * ST); };
      auto iw = [&](int t, int s) { w8(srdB, (t + r0) & 15, s); };
      iw(0, 0); iw(1, 1);
      HBAR();
      iw(2, 2); iw(3, 3);
      HBAR();
      HBAR();
      TS(2);
      HBAR();
      TS(3);
      hstart<P>(ix);
      slot = 0;
      hsteps<P, 0, 2>(ix, iw, slot);
      gs_steady(IC<NK - NS>{}, srdB, WS_O + pm2 * 16 * 16384u, r0, NK);
      hsteps<P, NK - NS, NK>(ix, iw, slot);
    }
    TS(4);
    {
      using namespace gf;
      using P = Pipe<NK, NS, 5, 5>;
      const i32x4 srdSp = make_srd(wsp, SEQ * SEQ * 2);
      auto lin14 = [&](unsigned src, int s) AI {
        const unsigned dst = B0 + s * SEG;
        dmaL<4>(srdW, l16, src + dw * 4096u, dst + dw * 4096u);
        if (dw != 2) dmaL<1>(srdW, l16, src + (12 + dw) * 1024u, dst + (12 + dw) * 1024u);
      };
      auto ia3 = [&](int t, int s) { lin16(WS_H + (pm1 * 16 + (t & 15)) * 16384u, s * AB); };
      auto ib3 = [&](int t, int s) { lin14(WS_W3 + (pn1 * 16 + (t & 15)) * 28672u + (t < 16 ? 14336u : 0u), s); };
      unsigned va4[6];
#pragma unroll
      for (int i = 0; i < 6; ++i) { const int r = (i < 4 ? dw * 4 + i : 8 + dw + i) * 8 + (lane >> 3); va4[i] = (pm1 * 128 + r) * (SEQ * 2) + (((lane & 7) ^ ((r >> 1) & 7)) * 16) - (i & 3) * 1024; }
      const int r4 = XROT * (xcd >> 1) * 8;
      auto ia4 = [&](int t, int s) {
        const unsigned kt = (t + r4) & 31;
        dmaS<4>(srdSp, va4, kt * 128u, s * AB + dw * 4096u);
        if (dw == 2) dmaS<2>(srdSp, va4 + 4, kt * 128u, s * AB + 14 * 1024u);
        else dmaS<1>(srdSp, va4 + 4, kt * 128u, s * AB + (12 + dw) * 1024u);
      };
      auto ib4 = [&](int t, int s) { lin14(WS_Z2 + (pn1 * 32 + ((t + r4) & 31)) * (unsigned)SEG, s); };
      ib3(0, 0); ib3(1, 1);
      HBAR();
      HBAR();
      ib3(2, 2); ib3(3, 3);
      TS(5);
      HBAR();
      TS(6);
      hstart<P>(ia3);
      slot = 0;
      hsteps<P, 0, 2>(ia3, ib3, slot);
      auto L4 = [&](unsigned so, unsigned l) AI { dmaL<4>(srdW, l16, so, l); };
      auto L1 = [&](unsigned so, unsigned l) AI { dmaL<1>(srdW, l16, so, l); };
      steady<2, 28, 20>(
          [&](int u) AI {
            const unsigned a = WS_H + (pm1 * 16 + (u & 15)) * 16384u, b = WS_W3 + (pn1 * 16 + (u & 15)) * 28672u + (u < 16 ? 14336u : 0u);
            const unsigned la = (u & 3) * AB, lb = B0 + (u & 3) * SEG;
            return Nx{a + dw * 4096u, a + 12288u + dw * 1024u, b + dw * 4096u, dw == 2 ? a + 15360u : b + 12288u + dw * 1024u,
                      la + dw * 4096u, la + 12288u + dw * 1024u, lb + dw * 4096u, dw == 2 ? la + 15360u : lb + 12288u + dw * 1024u};
          },
          L4, L4, L1, L1);
      TS(9);
      wait_vm<20>(); HBAR(); ia4(32, 0);
      wait_vm<15>(); HBAR(); ia4(33, 1);
      wait_vm<10>(); HBAR(); ia4(34, 2);
      HBAR(); ia4(35, 3);
      ib4(32, 0); ib4(33, 1); ib4(34, 2); ib4(35, 3);
      wait_vm<12>(); HBAR();
      wait_vm<8>(); HBAR(); ib4(36, 0); ia4(36, 0);
      wait_vm<14>(); HBAR(); ib4(37, 1); ia4(37, 1);
      i32x4 srdF;
#pragma unroll
      for (int e = 0; e < 4; ++e) srdF[e] = dw == 2 ? srdSp[e] : srdW[e];
      const unsigned vf4 = dw == 2 ? va4[5] + 1024u : l16;
      steady<34, 60, 20>(
          [&](int u) AI {
            const unsigned kt = (u + r4) & 31, a = kt * 128u, b = WS_Z2 + (pn1 * 32 + kt) * (unsigned)SEG;
            const unsigned la = (u & 3) * AB, lb = B0 + (u & 3) * SEG;
            return Nx{a, a, b + dw * 4096u, dw == 2 ? a : b + 12288u + dw * 1024u,
                      la + dw * 4096u, la + 12288u + dw * 1024u, lb + dw * 4096u, dw == 2 ? la + 15360u : lb + 12288u + dw * 1024u};
          },
          [&](unsigned so, unsigned l) AI { dmaS<4>(srdSp, va4, so, l); }, L4,
          [&](unsigned so, unsigned l) AI { dmaS<1>(srdSp, va4 + 4, so, l); }, [&](unsigned so, unsigned l) AI { dmaL<1>(srdF, vf4, so, l); });
      hsteps<P, 60, NK>(ia4, ib4, slot);
    }
    TS(10);
    {
      using namespace gs;
      constexpr int NK = 28;
      using P = Pipe<NK, NS, 5, 3>;
      const i32x4 srdB = make_srd(wout, FH * DM * 2);
      const int r0 = XROT * (xcd & 3) * 7;
      auto ix = [&](int t, int s) { lin16(WS_Y + (pm2 * 28 + rot(t, r0, NK)) * 16384u, s * ST); };
      auto iw = [&](int t, int s) { w8(srdB, rot(t, r0, NK), s); };
#pragma unroll
      for (int s = 0; s < NS; ++s) iw(s, s);
      HBAR();
      HBAR();
      TS(11);
      HBAR();
      TS(12);
      hstart<P>(ix);
      slot = 0;
      hsteps<P, 0, 2>(ix, iw, slot);
      gs_steady(IC<NK - NS>{}, srdB, WS_Y + pm2 * 28 * 16384u, r0, NK);
      hsteps<P, NK - NS, NK>(ix, iw, slot);
      HBAR();
    }
    TS(13);
    return;
  }

  const u64 tag = uniform64(__hip_atomic_load((gu64*)(ws + WS_EPOCH), __ATOMIC_RELAXED, __HIP_MEMORY_SCOPE_AGENT)) + 1;
  if (wid == 1) {
    u64 pc;
    asm volatile("s_getpc_b64 %0" : "=s"(pc));
    const unsigned v = *(const gu32*)((pc & ~127ull) + (idx * 14 + (lane < 14 ? lane : 0)) * 128);
    asm volatile("" :: "v"(v));
  }
  if (tid == 0) *(LAS u64*)(lds + L_TAG) = tag;
  wait_lgkm0();
#define LANES const int tid = fresh_tid(), lane = tid & 63, i16 = lane & 15, g = lane >> 4; (void)i16; (void)g
  auto publish = [&](unsigned flag) {
    wait_vm<0>();
    bar();
    if (threadIdx.x < 16) set_flag(ws, flag + threadIdx.x * 8, tag);
    bar();
  };
  auto copy16k = [&](unsigned dst) {
    LANES;
#pragma unroll
    for (int i = 0; i < 4; ++i) st16_dev(srdW, dst + (i * 256 + tid) * 16, *(LAS u32x4*)(lds + L_H + (i * 256 + tid) * 16));
  };
  {
    using namespace g1;
    LANES;
    const int hd = wid & 1, rh = wid >> 1;
    unsigned ab[2], hb[2], bb[2];
#pragma unroll
    for (int kk = 0; kk < 2; ++kk) { ab[kk] = a_base(lane, kk) + rh * 64 * 128; hb[kk] = a_base(lane, kk) + 128 * 128; bb[kk] = AB + b128_base(lane, hd * 2 + kk); }
    f32x4 aq[4][2], ak[4][2], av[4][2], ah[2];
#pragma unroll
    for (int m = 0; m < 4; ++m)
#pragma unroll
      for (int j = 0; j < 2; ++j) aq[m][j] = ak[m][j] = av[m][j] = zero4;
    ah[0] = ah[1] = zero4;
    auto load = [&](Fr& f, unsigned so, unsigned, int kk) {
#pragma unroll
      for (int m = 0; m < 4; ++m) f.a[m] = lds_a(lds + so + ab[kk] + m * 2048);
      f.a[4] = lds_a(lds + so + hb[kk]);
#pragma unroll
      for (int s = 0; s < 3; ++s)
#pragma unroll
        for (int j = 0; j < 2; ++j)
#pragma unroll
          for (int hh = 0; hh < 2; ++hh) f.b[s * 2 + j][hh] = lds_tr(lds + so + bb[j] + s * 8192 + (kk * 32 + hh * 8) * 128);
    };
    auto mma2 = [&](Fr& f, auto RH) {
      bf16x8 b[6];
#pragma unroll
      for (int i = 0; i < 6; ++i) b[i] = cat8(f.b[i][0], f.b[i][1]);
#pragma unroll
      for (int m = 0; m < 4; ++m)
#pragma unroll
        for (int j = 0; j < 2; ++j) {
          aq[m][j] = mfma(b[j], f.a[m], aq[m][j]);
          ak[m][j] = mfma(b[2 + j], f.a[m], ak[m][j]);
          av[m][j] = mfma(f.a[m], b[4 + j], av[m][j]);
        }
#pragma unroll
      for (int j = 0; j < 2; ++j) {
        if constexpr (decltype(RH)::value == 0) ah[j] = mfma(b[2 + j], f.a[4], ah[j]);
        else ah[j] = mfma(f.a[4], b[4 + j], ah[j]);
      }
    };
    if (rh == 0) compute_loop<NK, NS, ST, ST, 17, 26, Fr>(load, [&](Fr& f) { mma2(f, IC<0>{}); });
    else compute_loop<NK, NS, ST, ST, 17, 26, Fr>(load, [&](Fr& f) { mma2(f, IC<1>{}); });
    TS(1);
    f32x4 csq[2], csk[2]; float csv[2];
#pragma unroll
    for (int j = 0; j < 2; ++j) {
      csq[j] = *(LAS f32x4*)(lds + L_CS + (hd * 32 + j * 16 + 4 * g) * 4);
      csk[j] = *(LAS f32x4*)(lds + L_CS + (64 + hd * 32 + j * 16 + 4 * g) * 4);
      csv[j] = *(LAS float*)(lds + L_CS + (128 + hd * 32 + j * 16 + i16) * 4);
    }
    bf16x8 Q[4], K[5]; u32x2 V[5][2];
#pragma unroll
    for (int m = 0; m < 4; ++m) {
      const int r = rh * 64 + m * 16;
      const float mu = *(LAS float*)(lds + L_MU + (r + i16) * 4), rs = *(LAS float*)(lds + L_RS + (r + i16) * 4);
      const f32x4 mu4 = *(LAS f32x4*)(lds + L_MU + (r + 4 * g) * 4), rs4 = *(LAS f32x4*)(lds + L_RS + (r + 4 * g) * 4);
      Q[m] = cat8(pk4((aq[m][0] - mu * csq[0]) * rs), pk4((aq[m][1] - mu * csq[1]) * rs));
      K[m + 1] = cat8(pk4((ak[m][0] - mu * csk[0]) * rs), pk4((ak[m][1] - mu * csk[1]) * rs));
      V[m + 1][0] = pk4((av[m][0] - mu4 * csv[0]) * rs4);
      V[m + 1][1] = pk4((av[m][1] - mu4 * csv[1]) * rs4);
    }
    LAS char* xch = lds + L_X + hd * 3072 + lane * 16;
    if (rh == 0) {
      const float mu = *(LAS float*)(lds + L_MU + (128 + i16) * 4), rs = *(LAS float*)(lds + L_RS + (128 + i16) * 4);
      K[0] = cat8(pk4((ah[0] - mu * csk[0]) * rs), pk4((ah[1] - mu * csk[1]) * rs));
      *(LAS bf16x8*)(xch) = K[4];
      *(LAS bf16x8*)(xch + 1024) = cat8(V[4][0], V[4][1]);
    } else {
      const f32x4 mu4 = *(LAS f32x4*)(lds + L_MU + (128 + 4 * g) * 4), rs4 = *(LAS f32x4*)(lds + L_RS + (128 + 4 * g) * 4);
      *(LAS bf16x8*)(xch + 2048) = cat8(pk4((ah[0] - mu4 * csv[0]) * rs4), pk4((ah[1] - mu4 * csv[1]) * rs4));
      K[0] = K[4];
    }
    wait_lgkm0(); bar();
    {
      u32x4 v = *(LAS u32x4*)(xch + (rh == 0 ? 2048 : 1024));
      V[0][0] = (u32x2){v[0], v[1]}; V[0][1] = (u32x2){v[2], v[3]};
      if (rh == 1) K[0] = *(LAS bf16x8*)(xch);
    }
    constexpr float SC = 0.17677669529663687f * 1.4426950408889634f;
    f32x4 sp[4], sc[4];
#pragma unroll
    for (int m = 0; m < 4; ++m) { sp[m] = mfma(K[m], Q[m], zero4); sc[m] = mfma(K[m + 1], Q[m], zero4); }
    f32x4 mp, mc;
#pragma unroll
    for (int r = 0; r < 4; ++r) {
      const int dc = i16 - (4 * g + r);
      mp[r] = ((dc & 1) == 0 && dc <= -2) ? 0.f : -INFINITY;
      mc[r] = ((dc & 1) == 0 && dc >= 0) ? 0.f : -INFINITY;
    }
    const float nohalo = (pm1 == 0 && rh == 0) ? -INFINITY : 0.f;
    float mx[4], sum[4];
#pragma unroll
    for (int m = 0; m < 4; ++m) {
      sp[m] = sp[m] * SC + (m == 0 ? mp + nohalo : mp);
      sc[m] = sc[m] * SC + mc;
      mx[m] = fmaxf(fmaxf(fmaxf(sp[m][0], sp[m][1]), fmaxf(sp[m][2], sp[m][3])), fmaxf(fmaxf(sc[m][0], sc[m][1]), fmaxf(sc[m][2], sc[m][3])));
    }
#pragma unroll
    for (int m = 0; m < 4; ++m) mx[m] = max4(mx[m]);
#pragma unroll
    for (int m = 0; m < 4; ++m) {
      sp[m] -= mx[m]; sc[m] -= mx[m];
#pragma unroll
      for (int r = 0; r < 4; ++r) { sp[m][r] = __builtin_amdgcn_exp2f(sp[m][r]); sc[m][r] = __builtin_amdgcn_exp2f(sc[m][r]); }
      const f32x4 t = sp[m] + sc[m];
      sum[m] = (t[0] + t[1]) + (t[2] + t[3]);
    }
#pragma unroll
    for (int m = 0; m < 4; ++m) sum[m] = __builtin_amdgcn_rcpf(sum4(sum[m]));
    f32x4 o[4][2];
#pragma unroll
    for (int m = 0; m < 4; ++m) {
      const bf16x8 P = cat8(pk4(sp[m] * sum[m]), pk4(sc[m] * sum[m]));
#pragma unroll
      for (int db = 0; db < 2; ++db) o[m][db] = mfma(cat8(V[m][db], V[m + 1][db]), P, zero4);
    }
#pragma unroll
    for (int m = 0; m < 4; ++m)
#pragma unroll
      for (int db = 0; db < 2; ++db)
        *(LAS u32x2*)(lds + L_H + img_off(rh * 64 + m * 16 + i16, hd * 4 + db * 2 + (g >> 1)) + (g & 1) * 8) = pk4(o[m][db]);
    wait_lgkm0(); bar();
    TS(2);
    copy16k(WS_O + (pm1 * 16 + pn1) * 16384);
    TS(3);
    publish(F_O + (pm1 * 16 + pn1) * 128);
  }
  const int wc = wid & 1, wr = wid >> 1;
  auto small_gemm = [&](auto NKC, auto FIRST) AI {
    using namespace gs;
    LANES;
    unsigned ab[2], bb[2];
#pragma unroll
    for (int kk = 0; kk < 2; ++kk) {
      ab[kk] = decltype(FIRST)::value ? a_base(lane, kk) + wr * 64 * 128 : (kk * 4 + g) * 2048 + (wr * 64 + i16) * 16;
      bb[kk] = AB + b128_base(lane, wc * 2 + kk);
    }
    constexpr int MS = decltype(FIRST)::value ? 2048 : 256;
    f32x4 acc[4][2];
    LAS char* tp = lds + L_H + (wr * 64 + i16) * 128 + (g & 1) * 8;
    unsigned tc[2];
#pragma unroll
    for (int j = 0; j < 2; ++j) tc[j] = ((wc * 4 + j * 2 + (g >> 1)) ^ ((i16 >> 1) & 7)) * 16;
#pragma unroll
    for (int m = 0; m < 4; ++m)
#pragma unroll
      for (int j = 0; j < 2; ++j) acc[m][j] = unpk4(*(LAS u32x2*)(tp + tc[j] + m * 2048));
    auto load = [&](Fr& f, unsigned so, unsigned) {
#pragma unroll
      for (int kk = 0; kk < 2; ++kk) {
#pragma unroll
        for (int j = 0; j < 2; ++j)
#pragma unroll
          for (int hh = 0; hh < 2; ++hh) f.b[kk][j][hh] = lds_tr(lds + so + bb[j] + (kk * 32 + hh * 8) * 128);
#pragma unroll
        for (int m = 0; m < 4; ++m) f.a[kk][m] = lds_a(lds + so + ab[kk] + m * MS);
      }
    };
    auto mma = [&](Fr& f) {
#pragma unroll
      for (int kk = 0; kk < 2; ++kk)
#pragma unroll
        for (int m = 0; m < 4; ++m)
#pragma unroll
          for (int j = 0; j < 2; ++j) acc[m][j] = mfma(cat8(f.b[kk][j][0], f.b[kk][j][1]), f.a[kk][m], acc[m][j]);
    };
    Fr f0;
    unsigned sa = 0, sb = 0;
    bar();
    load(f0, 0u, 0u);
    deep_segment<decltype(NKC)::value, NS, ST, ST, 16, 16>(f0, sa, sb, load, mma);
#pragma unroll
    for (int m = 0; m < 4; ++m)
#pragma unroll
      for (int j = 0; j < 2; ++j) *(LAS u32x2*)(tp + tc[j] + m * 2048) = pk4(acc[m][j]);
    wait_lgkm0(); bar();
  };
  small_gemm(IC<16>{}, IC<1>{});
  TS(5);
  copy16k(WS_H + (pm2 * 16 + pn2) * 16384);
  {
    bf16x8 ha[2][2];
#pragma unroll
    for (int m = 0; m < 2; ++m)
#pragma unroll
      for (int kk = 0; kk < 2; ++kk) ha[m][kk] = lds_a(lds + L_H + a_base(lane, kk) + (wid * 2 + m) * 2048);
    const unsigned o = diag_lane ? WS_S2R + (pm2 * 16 + pn2) * 1024 + (wid * 32 + (lane & 15)) * 8 : 0xffff0000u;
    st8_dev(srdW, o, __builtin_bit_cast(u32x2, rowstat(ha[0][0], ha[0][1])));
    st8_dev(srdW, o + 128, __builtin_bit_cast(u32x2, rowstat(ha[1][0], ha[1][1])));
  }
  TS(6);
  publish(F_H + (pm2 * 16 + pn2) * 128);
  {
    using namespace gf;
    LANES;
    unsigned ab[2];
#pragma unroll
    for (int kk = 0; kk < 2; ++kk) ab[kk] = a_base(lane, kk) + wid * 32 * 128;
    const unsigned bb = B0 + lane * 16;
    f32x4 acc[2][7];
    LAS char *pa0 = lds + ab[0], *pa1 = lds + ab[1], *pb = lds + bb;
    unsigned sa = 0, sb = 0;
    auto rd = [&](Fr& f, int r) AI {
      const int kk = r / 9, q = r % 9;
      if (q < 2) f.a[kk][q] = lds_a((kk ? pa1 : pa0) + q * 2048);
      else f.b[kk][q - 2] = lds_a(pb + (kk * 7 + q - 2) * 1024);
    };
    auto advance = [&]() AI {
      sa += AB; sb += SEG; if (sa == NS * AB) { sa = 0; sb = 0; }
      pa0 = lds + (sa + ab[0]); pa1 = lds + (sa + ab[1]); pb = lds + (sb + bb);
    };
    auto tile = [&](Fr& c, Fr& n) AI {
      cbar();
#pragma unroll
      for (int i = 0; i < 28; ++i) {
        const int kk = i / 14, m = (i % 14) / 7, j = i % 7;
        asm volatile("v_mfma_f32_16x16x32_bf16 %0, %1, %2, %0" : "+v"(acc[m][j]) : "v"(c.b[kk][j]), "v"(c.a[kk][m]));
        if (i < 18) rd(n, i);
        if (i == 19) { advance(); pin(pa0); pin(pa1); pin(pb); }
        __builtin_amdgcn_sched_barrier(0);
      }
    };
    Fr f0;
    auto segment = [&](int n) AI {
      Fr f1;
#pragma unroll 1
      for (int t = 0; t < n; t += 2) { tile(f0, f1); tile(f1, f0); }
      asm volatile("s_nop 7\n\ts_nop 1" ::: "memory");
      __builtin_amdgcn_sched_barrier(0);
    };
    u32x2 zz[2][7];
    float mu[2], c1[2], c2[2], ir[2];
    auto rowconst = [&]() AI {
#pragma unroll
      for (int m = 0; m < 2; ++m) {
        mu[m] = -*(LAS float*)(lds + L_MU + (wid * 32 + m * 16 + i16) * 4);
        const float rs = *(LAS float*)(lds + L_RS + (wid * 32 + m * 16 + i16) * 4);
        c1[m] = -2.302208198f * rs; c2[m] = -0.1029432397f * rs * rs * rs; ir[m] = __builtin_amdgcn_rcpf(rs);
      }
    };
    auto act = [&](bool centre) AI {
#pragma unroll
      for (int j0 = 0; j0 < 7; j0 += 2) {
        f32x4 e[4], cs[2];
        const int n = j0 < 6 ? 4 : 2;
        if (centre) {
          cs[0] = *(LAS f32x4*)(lds + L_CS + (112 + j0 * 16 + 4 * g) * 4);
          cs[1] = *(LAS f32x4*)(lds + L_CS + (112 + (j0 < 6 ? j0 + 1 : j0) * 16 + 4 * g) * 4);
#pragma unroll
          for (int u = 0; u < 4; ++u) if (u < n) acc[u & 1][j0 + (u >> 1)] += mu[u & 1] * cs[u >> 1];
        }
#pragma unroll
        for (int u = 0; u < 4; ++u) if (u < n) e[u] = acc[u & 1][j0 + (u >> 1)] * acc[u & 1][j0 + (u >> 1)];
#pragma unroll
        for (int u = 0; u < 4; ++u) if (u < n) e[u] = e[u] * c2[u & 1] + c1[u & 1];
#pragma unroll
        for (int u = 0; u < 4; ++u) if (u < n) e[u] = e[u] * acc[u & 1][j0 + (u >> 1)];
#pragma unroll
        for (int u = 0; u < 4; ++u) if (u < n)
#pragma unroll
          for (int i = 0; i < 4; ++i) e[u][i] = __builtin_amdgcn_exp2f(e[u][i]);
#pragma unroll
        for (int u = 0; u < 4; ++u) if (u < n) e[u] = e[u] * ir[u & 1] + ir[u & 1];
#pragma unroll
        for (int u = 0; u < 4; ++u) if (u < n)
#pragma unroll
          for (int i = 0; i < 4; ++i) e[u][i] = __builtin_amdgcn_rcpf(e[u][i]);
#pragma unroll
        for (int u = 0; u < 4; ++u) if (u < n) { zz[u & 1][j0 + (u >> 1)] = pk4(acc[u & 1][j0 + (u >> 1)] * e[u]); pin(zz[u & 1][j0 + (u >> 1)]); }
        __builtin_amdgcn_sched_barrier(0);
      }
    };
#pragma unroll
    for (int m = 0; m < 2; ++m)
#pragma unroll
      for (int j = 0; j < 7; ++j) acc[m][j] = zero4;
    bar();
#pragma unroll
    for (int r = 0; r < 18; ++r) rd(f0, r);
    advance();
    segment(16);
    TS(7);
    rowconst();
    act(true);
    {
      f32x2 st[2];
#pragma unroll
      for (int m = 0; m < 2; ++m)
        st[m] = rowstat(cat8(zz[m][0], zz[m][1]), cat8(zz[m][2], zz[m][3])) + rowstat(cat8(zz[m][4], zz[m][5]), cat8(zz[m][6], (u32x2){0u, 0u}));
      const unsigned o = diag_lane ? WS_S3 + (pm1 * 16 + pn1) * 1024 + (wid * 32 + i16) * 8 : 0xffff0000u;
#pragma unroll
      for (int m = 0; m < 2; ++m) st8_dev(srdW, o + m * 128, (u32x2){__float_as_uint(st[m][0]), min(__float_as_uint(st[m][1]), ~1u)});
    }
#pragma unroll
    for (int j = 0; j < 7; ++j) {
      const f32x4 cs = *(LAS f32x4*)(lds + L_CS + (j * 16 + 4 * g) * 4);
      acc[0][j] = mu[0] * cs; acc[1][j] = mu[1] * cs;
    }
    asm volatile("s_nop 1" ::: "memory");
    TS(8);
    Fr f1;
    segment(6);
    tile(f0, f1);
    TS(9);
#pragma unroll
    for (int m = 0; m < 2; ++m) {
      const float r3 = *(LAS float*)(lds + L_R3 + (wid * 32 + m * 16 + i16) * 4), m3 = -r3 * *(LAS float*)(lds + L_M3 + (wid * 32 + m * 16 + i16) * 4);
#pragma unroll
      for (int j = 0; j < 7; ++j)
        *(LAS u32x2*)(lds + L_ZS + (wid * 32 + m * 16 + swap23(i16)) * 232 + 8 * g + j * 32) = pk4(unpk4(zz[m][j]) * r3 + m3);
    }
    wait_lgkm0();
    {
      LAS char* p = lds + L_ZS + (wid * 32 + b_pos_lane(lane)) * 232 + (lane & 3) * 8;
      u32x2 lo[7], hi[7];
#pragma unroll
      for (int j = 0; j < 7; ++j) { lo[j] = lds_tr(p + j * 32); hi[j] = lds_tr(p + j * 32 + 8 * 232); }
#pragma unroll
      for (int j = 0; j < 7; ++j)
        st16_dev(srdW, WS_Z2 + (pn1 * 32 + pm1 * 2) * SEG + (wid * 7 + j) * 1024 + lane * 16, (u32x4){lo[j][0], lo[j][1], hi[j][0], hi[j][1]});
    }
    TS(10);
    tile(f1, f0);
    tile(f0, f1);
    wait_vm<0>();
    tile(f1, f0);
    segment(6);
    TS(11);
    rowconst();
    act(false);
#pragma unroll
    for (int m = 0; m < 2; ++m) {
      const float b = (float)*(LAS __bf16*)(lds + L_C2 + (wid * 32 + m * 16 + i16) * 2);
#pragma unroll
      for (int j = 0; j < 7; ++j) acc[m][j] = zero4 + b;
    }
    TS(12);
    bar();
    pa0 = lds + ab[0]; pa1 = lds + ab[1]; pb = lds + bb; sa = 0; sb = 0;
#pragma unroll
    for (int r = 0; r < 18; ++r) rd(f0, r);
    advance();
    segment(32);
    TS(13);
#pragma unroll
    for (int m = 0; m < 2; ++m)
#pragma unroll
      for (int j = 0; j < 7; ++j)
        *(LAS u32x2*)(lds + L_ZS + (j * 2 + (g >> 1)) * 2048 + (wid * 32 + m * 16 + i16) * 16 + (g & 1) * 8) = pk4(unpk4(zz[m][j]) * acc[m][j]);
    wait_lgkm0(); bar();
    TS(14);
#pragma unroll
    for (int i = 0; i < 7; ++i) st16_dev(srdW, WS_Y + (pm1 * 16 + pn1) * 28672 + (i * 256 + tid) * 16, *(LAS u32x4*)(lds + L_ZS + (i * 256 + tid) * 16));
    TS(15);
    publish(F_Y + (pm1 * 16 + pn1) * 128);
  }
  small_gemm(IC<28>{}, IC<0>{});
  TS(16);
#pragma unroll
  for (int i = 0; i < 4; ++i) {
    const int c = i * 256 + tid, r = c >> 3, ch = (c & 7) ^ ((r >> 1) & 7);
    *(u32x4*)(out + (size_t)(pm2 * 128 + r) * (DM * 2) + pn2 * 128 + ch * 16) = *(LAS u32x4*)(lds + L_H + c * 16);
  }
  if (blockIdx.x == 0 && tid == 0) __hip_atomic_store((gu64*)(ws + WS_EPOCH), tag, __ATOMIC_RELAXED, __HIP_MEMORY_SCOPE_AGENT);
  TS(17);
}

extern "C" void kernel_launch(void* const* d_in, const int* in_sizes, int n_in, void* d_out, int out_size, void* d_ws, size_t ws_size,
                              hipStream_t stream) {
  (void)hipFuncSetAttribute((const void*)k_mega, hipFuncAttributeMaxDynamicSharedMemorySize, LDS_BYTES);
  k_mega<<<256, 512, LDS_BYTES, stream>>>((const __bf16*)d_in[0], (const __bf16*)d_in[1], (const __bf16*)d_in[2], (const __bf16*)d_in[4],
                                          (const __bf16*)d_in[5], (const __bf16*)d_in[6], (const __bf16*)d_in[7], (char*)d_out, (char*)d_ws + (size_t)WSBASE * 1048576);
}
```

```cpp
#include <hip/hip_runtime.h>
#include <hip/hip_bf16.h>
#include <stdint.h>
#include <type_traits>

#define DI __device__ __forceinline__
#define LAS __attribute__((address_space(3)))
#define AI __attribute__((always_inline))
typedef __bf16 bf16x8 __attribute__((ext_vector_type(8)));
typedef __bf16 bf16x2 __attribute__((ext_vector_type(2)));
typedef short s16x4 __attribute__((ext_vector_type(4)));
typedef float f32x4 __attribute__((ext_vector_type(4)));
typedef float f32x2 __attribute__((ext_vector_type(2)));
typedef int i32x4 __attribute__((ext_vector_type(4)));
typedef unsigned u32x4 __attribute__((ext_vector_type(4)));
typedef unsigned u32x2 __attribute__((ext_vector_type(2)));
typedef unsigned long long u64;
typedef __attribute__((address_space(1))) u64 gu64;
typedef __attribute__((address_space(1))) unsigned gu32;
template <int V> using IC = std::integral_constant<int, V>;

#define POLL_SLEEP 1
#define CODEPF 1
#define FLAGPF 1
#define XROT 1
#define WSBASE 0

constexpr int SEQ = 2048, DM = 1024, FF = 3584, FH = 1792;
constexpr float LN_EPS = 1e-5f;

constexpr unsigned WS_EPOCH = 0;
constexpr unsigned F_S1 = 32768, F_O = 2 * 32768, F_H = 3 * 32768, F_W3 = 4 * 32768, F_S3 = 5 * 32768, F_Z2 = 6 * 32768, F_Y = 7 * 32768;
constexpr unsigned WS_S1R = 1u << 18;
constexpr unsigned WS_S1C = WS_S1R + (1u << 18);
constexpr unsigned WS_S2R = WS_S1C + (1u << 18);
constexpr unsigned WS_S2C = WS_S2R + (1u << 18);
constexpr unsigned WS_S3 = WS_S2C + (1u << 18);
constexpr unsigned WS_O = 2u << 20;
constexpr unsigned WS_H = WS_O + (4u << 20);
constexpr unsigned WS_Z2 = WS_H + (4u << 20);
constexpr unsigned WS_Y = WS_Z2 + (8u << 20);
constexpr unsigned WS_W3 = WS_Y + (8u << 20);
constexpr unsigned WS_END = WS_W3 + (8u << 20);
constexpr size_t WS_TS = 64u << 20;
#define TS(i) do {} while (0)

constexpr unsigned L_WT = 98304;
constexpr unsigned L_ZS = 122880;
constexpr unsigned L_X = 129024;
constexpr unsigned L_H = 135168;
constexpr unsigned L_MISC = 153600;
constexpr unsigned L_MU = L_MISC, L_RS = L_MU + 576, L_CS = L_RS + 576, L_P = L_CS + 896, L_M3 = L_P + 2048, L_R3 = L_M3 + 512, L_TAG = L_R3 + 512,
                   L_C2 = L_TAG + 16;
constexpr unsigned LDS_BYTES = 163840;
static_assert(L_C2 + 2 * 896 <= LDS_BYTES, "lds");

DI i32x4 make_srd(const void* p, unsigned bytes) {
  u64 a = (u64)p;
  i32x4 r;
  r[0] = __builtin_amdgcn_readfirstlane((int)(unsigned)a);
  r[1] = __builtin_amdgcn_readfirstlane((int)(unsigned)(a >> 32));
  r[2] = (int)bytes;
  r[3] = 0x00020000;
  return r;
}
template <int POL> DI void dma16(i32x4 srd, unsigned voff, unsigned soff, unsigned ldsaddr) {
  if constexpr (POL == 2)
    asm volatile("s_mov_b32 m0, %0\n\ts_nop 0\n\tbuffer_load_dwordx4 %1, %2, %3 offen nt lds" :: "s"(ldsaddr), "v"(voff), "s"(srd), "s"(soff) : "memory");
  else if constexpr (POL == 1)
    asm volatile("s_mov_b32 m0, %0\n\ts_nop 0\n\tbuffer_load_dwordx4 %1, %2, %3 offen sc1 lds" :: "s"(ldsaddr), "v"(voff), "s"(srd), "s"(soff) : "memory");
  else
    asm volatile("s_mov_b32 m0, %0\n\ts_nop 0\n\tbuffer_load_dwordx4 %1, %2, %3 offen lds" :: "s"(ldsaddr), "v"(voff), "s"(srd), "s"(soff) : "memory");
}
template <int N> DI void dmaL(i32x4 srd, unsigned v, unsigned so, unsigned l) {
  if constexpr (N == 1) asm volatile("s_mov_b32 m0, %0\n\ts_nop 0\n\tbuffer_load_dwordx4 %1, %2, %3 offen sc1 lds" :: "s"(l), "v"(v), "s"(srd), "s"(so) : "memory");
  if constexpr (N == 2) asm volatile("s_mov_b32 m0, %0\n\ts_nop 0\n\tbuffer_load_dwordx4 %1, %2, %3 offen sc1 lds\n\tbuffer_load_dwordx4 %1, %2, %3 offen offset:1024 sc1 lds" :: "s"(l), "v"(v), "s"(srd), "s"(so) : "memory");
  if constexpr (N == 4) asm volatile("s_mov_b32 m0, %0\n\ts_nop 0\n\tbuffer_load_dwordx4 %1, %2, %3 offen sc1 lds\n\tbuffer_load_dwordx4 %1, %2, %3 offen offset:1024 sc1 lds\n\t"
                                     "buffer_load_dwordx4 %1, %2, %3 offen offset:2048 sc1 lds\n\tbuffer_load_dwordx4 %1, %2, %3 offen offset:3072 sc1 lds" :: "s"(l), "v"(v), "s"(srd), "s"(so) : "memory");
}
template <int N> DI void dmaS(i32x4 srd, const unsigned* v, unsigned so, unsigned l) {
  if constexpr (N == 1) asm volatile("s_mov_b32 m0, %0\n\ts_nop 0\n\tbuffer_load_dwordx4 %1, %2, %3 offen lds" :: "s"(l), "v"(v[0]), "s"(srd), "s"(so) : "memory");
  if constexpr (N == 2) asm volatile("s_mov_b32 m0, %0\n\ts_nop 0\n\tbuffer_load_dwordx4 %1, %2, %3 offen lds\n\tbuffer_load_dwordx4 %4, %2, %3 offen offset:1024 lds" :: "s"(l), "v"(v[0]), "s"(srd), "s"(so), "v"(v[1]) : "memory");
  if constexpr (N == 4) asm volatile("s_mov_b32 m0, %0\n\ts_nop 0\n\tbuffer_load_dwordx4 %1, %2, %3 offen lds\n\tbuffer_load_dwordx4 %4, %2, %3 offen offset:1024 lds\n\t"
                                     "buffer_load_dwordx4 %5, %2, %3 offen offset:2048 lds\n\tbuffer_load_dwordx4 %6, %2, %3 offen offset:3072 lds"
                                     :: "s"(l), "v"(v[0]), "s"(srd), "s"(so), "v"(v[1]), "v"(v[2]), "v"(v[3]) : "memory");
}
DI void ld16_async(u32x4& d, i32x4 srd, unsigned voff) { asm volatile("buffer_load_dwordx4 %0, %1, %2, 0 offen sc1" : "=v"(d) : "v"(voff), "s"(srd) : "memory"); }
DI void ld8_async(u32x2& d, i32x4 srd, unsigned voff) { asm volatile("buffer_load_dwordx2 %0, %1, %2, 0 offen sc1" : "=v"(d) : "v"(voff), "s"(srd) : "memory"); }
DI void touch(unsigned& d, i32x4 srd, unsigned voff) { asm volatile("buffer_load_dword %0, %1, %2, 0 offen" : "=v"(d) : "v"(voff), "s"(srd) : "memory"); }
template <int J0, int J1> DI void gather(u32x4 (&d)[16], i32x4 srd, unsigned off);
DI void ld16_nt(u32x4& d, i32x4 srd, unsigned voff, unsigned soff) { asm volatile("buffer_load_dwordx4 %0, %1, %2, %3 offen nt" : "=v"(d) : "v"(voff), "s"(srd), "s"(soff) : "memory"); }
DI void ld16_plain(u32x4& d, i32x4 srd, unsigned voff, unsigned soff) { asm volatile("buffer_load_dwordx4 %0, %1, %2, %3 offen" : "=v"(d) : "v"(voff), "s"(srd), "s"(soff) : "memory"); }
template <class T> DI void pin(T& v) { asm volatile("" : "+v"(v)); }
DI void st16_dev(i32x4 srd, unsigned voff, u32x4 v) { asm volatile("buffer_store_dwordx4 %0, %1, %2, 0 offen sc1\n\ts_nop 1" :: "v"(v), "v"(voff), "s"(srd) : "memory"); }
DI void st8_dev(i32x4 srd, unsigned voff, u32x2 v) { asm volatile("buffer_store_dwordx2 %0, %1, %2, 0 offen sc1" :: "v"(v), "v"(voff), "s"(srd) : "memory"); }
DI void st4_dev(i32x4 srd, unsigned voff, unsigned v) { asm volatile("buffer_store_dword %0, %1, %2, 0 offen sc1" :: "v"(v), "v"(voff), "s"(srd) : "memory"); }
template <int N> DI void wait_vm() { asm volatile("s_waitcnt vmcnt(%0)" :: "i"(N) : "memory"); }
DI void wait_lgkm0() { __builtin_amdgcn_s_waitcnt(0xC07F); asm volatile("" ::: "memory"); }
DI void bar() { asm volatile("" ::: "memory"); __builtin_amdgcn_s_barrier(); asm volatile("" ::: "memory"); }
#define HBAR() bar()
#define ABAR() do { __builtin_amdgcn_sched_barrier(0); bar(); __builtin_amdgcn_sched_barrier(0); } while (0)
#define ABARS(N) do { _Pragma("unroll 1") for (int i_ = 0; i_ < (N); ++i_) ABAR(); } while (0)
DI unsigned pk2(float lo, float hi) { bf16x2 v = {(__bf16)lo, (__bf16)hi}; return __builtin_bit_cast(unsigned, v); }
DI u32x2 pk4(f32x4 v) { return (u32x2){pk2(v[0], v[1]), pk2(v[2], v[3])}; }
DI f32x2 unpk2(unsigned u) { return (f32x2){__uint_as_float(u << 16), __uint_as_float(u & 0xffff0000u)}; }
DI f32x4 unpk4(u32x2 u) { const f32x2 a = unpk2(u[0]), b = unpk2(u[1]); return (f32x4){a[0], a[1], b[0], b[1]}; }
DI bf16x8 cat8(u32x2 a, u32x2 b) { u32x4 v = {a[0], a[1], b[0], b[1]}; return __builtin_bit_cast(bf16x8, v); }
DI f32x4 mfma(bf16x8 a, bf16x8 b, f32x4 c) { return __builtin_amdgcn_mfma_f32_16x16x32_bf16(a, b, c, 0, 0, 0); }
DI bf16x8 lds_a(LAS char* p) { return *(LAS bf16x8*)p; }
DI u32x2 lds_tr(LAS char* p) { return __builtin_bit_cast(u32x2, __builtin_amdgcn_ds_read_tr16_b64_v4i16((LAS s16x4*)p)); }
#undef lds_tr
DI int swap23(int k) { return (k & ~12) | ((k & 4) << 1) | ((k & 8) >> 1); }
DI float sum4(float x) {
  auto r = __builtin_amdgcn_permlane16_swap(__float_as_uint(x), __float_as_uint(x), false, false);
  x = __uint_as_float(r[0]) + __uint_as_float(r[1]);
  auto s = __builtin_amdgcn_permlane32_swap(__float_as_uint(x), __float_as_uint(x), false, false);
  return __uint_as_float(s[0]) + __uint_as_float(s[1]);
}
DI float bsel(unsigned m, float a, float b) { return __uint_as_float((m & __float_as_uint(a)) | (~m & __float_as_uint(b))); }
DI float vmax(float a, float b) { float r; asm("v_max_f32 %0, %1, %2" : "=v"(r) : "v"(a), "v"(b)); return r; }
DI float max4(float x) {
  auto r = __builtin_amdgcn_permlane16_swap(__float_as_uint(x), __float_as_uint(x), false, false);
  x = vmax(__uint_as_float(r[0]), __uint_as_float(r[1]));
  auto s = __builtin_amdgcn_permlane32_swap(__float_as_uint(x), __float_as_uint(x), false, false);
  return vmax(__uint_as_float(s[0]), __uint_as_float(s[1]));
}
DI u32x4 chunk16(u32x2 a, u32x2 b) {
  auto r0 = __builtin_amdgcn_permlane16_swap(a[0], b[0], false, false);
  auto r1 = __builtin_amdgcn_permlane16_swap(a[1], b[1], false, false);
  return (u32x4){r0[0], r1[0], r0[1], r1[1]};
}
DI f32x2 mean_rstd(f32x2 t, float inv_n) {
  const float mu = t[0] * inv_n, var = fmaxf(t[1] * inv_n - mu * mu, 0.f);
  return (f32x2){mu, __builtin_amdgcn_rsqf(var + LN_EPS)};
}
DI u64 uniform64(u64 v) {
  return ((u64)(unsigned)__builtin_amdgcn_readfirstlane((int)(unsigned)(v >> 32)) << 32) | (unsigned)__builtin_amdgcn_readfirstlane((int)(unsigned)v);
}
DI int fresh_tid() { int t = threadIdx.x; asm volatile("" : "+v"(t)); return t; }
DI void set_flag(char* ws, unsigned flag_off, u64 tag) { __hip_atomic_store((gu64*)(ws + flag_off), tag, __ATOMIC_RELAXED, __HIP_MEMORY_SCOPE_AGENT); }
DI void await(char* ws, unsigned flag_off, u64 tag) {
#pragma unroll 1
  for (unsigned spin = 0; spin < (1u << 17); ++spin) {
    const u64 f = __hip_atomic_load((gu64*)(ws + flag_off), __ATOMIC_RELAXED, __HIP_MEMORY_SCOPE_AGENT);
    if (__all(f == tag)) break;
    __builtin_amdgcn_s_sleep(POLL_SLEEP);
  }
  asm volatile("" ::: "memory");
}
DI unsigned row_flags(unsigned f, int pm, int lane) { return f + (pm * 16 + (lane & 15)) * 128; }
DI unsigned col_flags(unsigned f, int pn, int lane) { return f + ((lane & 15) * 16 + pn) * 128; }
template <int J0, int J1> DI void gather(u32x4 (&d)[16], i32x4 srd, unsigned off) {
#pragma unroll
  for (int j = J0; j < J1; ++j) ld16_async(d[j], srd, off + j * 1024);
}
template <int J0, int J1> DI void accum(f32x4& t, u32x4 (&d)[16]) {
#pragma unroll
  for (int j = J0; j < J1; ++j) { pin(d[j]); t = j == 0 ? __builtin_bit_cast(f32x4, d[j]) : t + __builtin_bit_cast(f32x4, d[j]); }
  pin(t);
}
DI void put_rows(LAS char* lds, unsigned l_mu, unsigned l_rs, int row2, f32x4 t, float inv_n, bool on = true) {
  const f32x2 a = mean_rstd((f32x2){t[0], t[1]}, inv_n), b = mean_rstd((f32x2){t[2], t[3]}, inv_n);
  if (on) { *(LAS f32x2*)(lds + l_mu + row2 * 8) = (f32x2){a[0], b[0]}; *(LAS f32x2*)(lds + l_rs + row2 * 8) = (f32x2){a[1], b[1]}; }
}
DI unsigned await_mask(char* ws, unsigned flag_off, u64 tag, unsigned need) {
  unsigned seen = 0;
#pragma unroll 1
  for (unsigned spin = 0; spin < (1u << 17); ++spin) {
    const u64 f = __hip_atomic_load((gu64*)(ws + flag_off), __ATOMIC_RELAXED, __HIP_MEMORY_SCOPE_AGENT);
    seen = (unsigned)__ballot(f == tag) & 0xffffu;
    if ((seen & need) == need) break;
    __builtin_amdgcn_s_sleep(POLL_SLEEP);
  }
  asm volatile("" ::: "memory");
  return seen;
}

DI int b_pos_lane(int lane) { const int g = lane >> 4, q = (lane & 15) >> 2; return (g >> 1) * 16 + (g & 1) * 4 + q; }
DI unsigned b128_base(int lane, int nb) { const int pos = b_pos_lane(lane); return pos * 128 + ((nb ^ ((pos >> 1) & 3)) * 32) + (lane & 3) * 8; }
DI unsigned b224_base(int lane) { return b_pos_lane(lane) * 224 + (lane & 3) * 8; }
DI unsigned a_base(int lane, int kk) { const int r = lane & 15, g = lane >> 4; return r * 128 + (((kk * 4 + g) ^ ((r >> 1) & 7)) * 16); }
DI unsigned img_off(int row, int chunk) { return row * 128 + ((chunk ^ ((row >> 1) & 7)) * 16); }

#define SGB(mask, n) __builtin_amdgcn_sched_group_barrier(mask, n, 0)
template <int NR, int NM> DI void interleave() {
  constexpr int RP = NR > NM - 2 ? 2 : 1;
  SGB(0x8, 1);
  SGB(0x2, 8);
#pragma unroll
  for (int i = 0; i < (NR + RP - 1) / RP; ++i) { SGB(0x100, RP); SGB(0x8, 1); }
  SGB(0x8, NM);
}
DI void cbar() {
  __builtin_amdgcn_sched_barrier(0);
  __builtin_amdgcn_s_waitcnt(0xC07F);
  bar();
  __builtin_amdgcn_sched_barrier(0);
}
template <int NK, int NS, int SA, int SB, int NR, int NM, class F, class L, class M>
DI void compute_loop(L load, M mma) {
  F f0, f1;
  bar();
  load(f0, 0u, 0u, 0);
  unsigned sa = 0, sb = 0;
  __builtin_amdgcn_sched_barrier(0);
#pragma unroll 1
  for (int t = 0; t < NK - 1; ++t) {
    load(f1, sa, sb, 1);
    mma(f0);
    interleave<NR, NM>();
    cbar();
    sa += SA; sb += SB; if (sa == NS * SA) { sa = 0; sb = 0; }
    load(f0, sa, sb, 0);
    mma(f1);
    interleave<NR, NM>();
    __builtin_amdgcn_sched_barrier(0);
  }
  load(f1, sa, sb, 1);
  mma(f0);
  interleave<NR, NM>();
  cbar();
  mma(f1);
}

template <int N, int NS, int SA, int SB, int NR, int NM, class F, class L, class M>
DI void deep_segment(F& f0, unsigned& sa, unsigned& sb, L load, M mma) {
  F f1;
#pragma unroll 1
  for (int t = 0; t < N; t += 2) {
    cbar();
    sa += SA; sb += SB; if (sa == NS * SA) { sa = 0; sb = 0; }
    load(f1, sa, sb);
    mma(f0);
    interleave<NR, NM>();
    cbar();
    sa += SA; sb += SB; if (sa == NS * SA) { sa = 0; sb = 0; }
    load(f0, sa, sb);
    mma(f1);
    interleave<NR, NM>();
  }
  __builtin_amdgcn_sched_barrier(0);
}

template <int NK_, int NS_, int XP_, int WP_> struct Pipe {
  static constexpr int NK = NK_, NS = NS_, XP = XP_, PW = XP_ + WP_;
  static constexpr int allowed(int t) {
    int n = 0;
    for (int s = t + 2; s <= t + NS - 1 && s < NK; ++s) n += s < NS ? XP : PW;
    return n;
  }
};
template <class P, int T, int EXTRA = 0, class IX, class IW> DI void hstep(IX& ix, IW& iw, int& slot) {
  wait_vm<P::allowed(T) + EXTRA>();
  HBAR();
  if constexpr (T + P::NS < P::NK) { iw(T + P::NS, slot); ix(T + P::NS, slot); }
  slot = slot + 1 == P::NS ? 0 : slot + 1;
}
template <class P, int T0, int T1, class IX, class IW> DI void hsteps(IX& ix, IW& iw, int& slot) {
  if constexpr (T0 < T1) {
    if constexpr (T0 >= P::NS - 2 && T1 <= P::NK - P::NS && T1 - T0 > 1) {
#pragma unroll 1
      for (int t = T0; t < T1; ++t) {
        wait_vm<(P::NS - 2) * P::PW>();
        HBAR();
        iw(t + P::NS, slot); ix(t + P::NS, slot);
        slot = slot + 1 == P::NS ? 0 : slot + 1;
      }
    } else if constexpr (T0 >= P::NS - 2 && T0 + 1 < P::NK - P::NS && T1 > P::NK - P::NS) {
      hsteps<P, T0, P::NK - P::NS>(ix, iw, slot);
      hsteps<P, P::NK - P::NS, T1>(ix, iw, slot);
    } else {
      hstep<P, T0>(ix, iw, slot);
      hsteps<P, T0 + 1, T1>(ix, iw, slot);
    }
  }
}
template <class P, class IX> DI void hstart(IX& ix) {
  ix(0, 0); ix(1, 1);
  wait_vm<P::XP>();
  HBAR();
#pragma unroll
  for (int s = 2; s < P::NS; ++s) ix(s, s);
}
struct Nx { unsigned a0, a1, b, f, la0, la1, lb, lf; };
template <class T> DI void spin(T& v) { asm volatile("" : "+s"(v)); }
template <int T0, int T1, int WAIT, class MK, class G0, class G1, class G2, class G3>
DI void steady(MK mk, G0 g0, G1 g1, G2 g2, G3 g3) {
  static_assert(((T1 - T0) & 1) == 0, "even");
  Nx c = mk(T0 + 4), n;
  auto one = [&](Nx& p, Nx& q, int u) AI {
    wait_vm<WAIT>();
    bar();
    g0(p.a0, p.la0);
    q = mk(u);
    spin(q.a0); spin(q.a1); spin(q.b); spin(q.f); spin(q.la0); spin(q.la1); spin(q.lb); spin(q.lf);
    g1(p.b, p.lb);
    g2(p.a1, p.la1);
    g3(p.f, p.lf);
  };
#pragma unroll 1
  for (int u = T0 + 5; u < T1 + 5; u += 2) { one(c, n, u); one(n, c, u + 1); }
}
DI int rot(int t, int r0, int nk) { const int k = t + r0; return k >= nk ? k - nk : k; }

namespace g1 { constexpr int NK = 16, NS = 3, AB = 144 * 128, ST = AB + 3 * 8192; struct Fr { bf16x8 a[5]; u32x2 b[6][2]; }; }
namespace gs { constexpr int NS = 4, AB = 16384, ST = AB + 8192; struct Fr { bf16x8 a[2][2]; u32x2 b[2][4][2]; }; }
namespace gf { constexpr int NK = 64, NS = 4, AB = 16384, SEG = 14336, B0 = NS * AB; struct Fr { bf16x8 a[2][2]; bf16x8 b[2][7]; }; }

__global__ __launch_bounds__(512, 2) void k_mega(const __bf16* __restrict__ x, const __bf16* __restrict__ wqkv, const __bf16* __restrict__ wo,
                                                 const __bf16* __restrict__ win, const __bf16* __restrict__ wsp, const __bf16* __restrict__ bsp,
                                                 const __bf16* __restrict__ wout, char* __restrict__ out, char* __restrict__ ws) {
  extern __shared__ __attribute__((aligned(16))) char smem_[];
  LAS char* lds = (LAS char*)smem_;
  const int tid = threadIdx.x, lane = tid & 63, wid = __builtin_amdgcn_readfirstlane(tid >> 6);
  const int xcd = blockIdx.x & 7, idx = blockIdx.x >> 3;
  const int pm1 = (idx & 7) * 2 + (xcd & 1), pn1 = (idx >> 3) * 4 + (xcd >> 1);
  const int pm2 = (idx & 3) * 4 + (xcd & 3), pn2 = (idx >> 2) * 2 + (xcd >> 2);
  const i32x4 srdW = make_srd(ws, WS_END);
  TS(0);

  const bf16x8 ones = __builtin_bit_cast(bf16x8, (u32x4){0x3f803f80u, 0x3f803f80u, 0x3f803f80u, 0x3f803f80u});
  const f32x4 zero4 = {0.f, 0.f, 0.f, 0.f};
  const int r1 = (((xcd >> 1) + (xcd & 1) * 2 + (idx & 1)) & 3) * 4;
  const int srow = idx >> 3, scol = (idx & 7) >> 1;
  auto rowstat = [&](bf16x8 a0, bf16x8 a1) AI {
    const f32x4 sm = mfma(ones, a1, mfma(ones, a0, zero4)), sq = mfma(a1, a1, mfma(a0, a0, zero4));
    return (f32x2){sm[0], (lane & 2) ? ((lane & 1) ? sq[3] : sq[2]) : ((lane & 1) ? sq[1] : sq[0])};
  };
  const bool diag_lane = ((lane & 15) >> 2) == (lane >> 4);
  auto g1_first_steps = [&](int hw, auto between) AI {
    using namespace g1;
#pragma unroll 1
    for (int t = 0; t < 4; ++t) {
      bf16x8 ra[2][2]; u32x2 rb[3][4];
      const unsigned so = (t == 3 ? 0 : t) * ST;
      if (t == srow) {
#pragma unroll
        for (int m = 0; m < 2; ++m)
#pragma unroll
          for (int kk = 0; kk < 2; ++kk) ra[m][kk] = lds_a(lds + so + a_base(lane, kk) + (hw * 2 + m) * 2048);
      }
      if (t == scol) {
#pragma unroll
        for (int i = 0; i < 3; ++i) {
          const int n = hw * 3 + i;
          const unsigned cb = so + AB + (n >> 2) * 8192 + b128_base(lane, n & 3);
#pragma unroll
          for (int j = 0; j < 4; ++j) rb[i][j] = lds_tr(lds + cb + ((j >> 1) * 32 + (j & 1) * 8) * 128);
        }
      }
      wait_lgkm0();
      between(t);
      if (t == srow) {
        const f32x2 r0 = rowstat(ra[0][0], ra[0][1]), r1 = rowstat(ra[1][0], ra[1][1]);
        if (diag_lane) { *(LAS f32x2*)(lds + L_P + (hw * 32 + (lane & 15)) * 8) = r0; *(LAS f32x2*)(lds + L_P + (hw * 32 + 16 + (lane & 15)) * 8) = r1; }
      }
      if (t == scol) {
#pragma unroll
        for (int i = 0; i < 3; ++i)
          *(LAS float*)(lds + L_P + 1024 + ((hw * 3 + i) * 16 + (lane & 15)) * 4) =
              mfma(ones, cat8(rb[i][2], rb[i][3]), mfma(ones, cat8(rb[i][0], rb[i][1]), zero4))[0];
      }
    }
    wait_lgkm0();
  };

  if (wid == 7) {
    const unsigned l16 = lane * 16;
    ABAR();
    const u64 tag = uniform64(*(LAS u64*)(lds + L_TAG));
    const u32x2 tag2 = __builtin_bit_cast(u32x2, tag);
    const unsigned myflag = lane < 16 ? (pm1 * 16 + pn1) * 128 + lane * 8 : 0xff000000u;
    g1_first_steps(3, [&](int) { ABAR(); });
    ABAR();
    st16_dev(srdW, WS_S1R + (pm1 * 16 + ((srow + r1) & 15)) * 1024 + l16, *(LAS u32x4*)(lds + L_P + l16));
    st16_dev(srdW, WS_S1C + (pn1 * 16 + ((scol + r1) & 15)) * 1024 + l16, *(LAS u32x4*)(lds + L_P + 1024 + l16));
    st16_dev(srdW, WS_S3 + (pm1 * 16 + pn1) * 1024 + l16, (u32x4){~0u, ~0u, ~0u, ~0u});
    unsigned tch[4] = {0u, 0u, 0u, 0u};
    unsigned tcw[2];
    {
      const i32x4 srdO = make_srd(wout, FH * DM * 2);
#pragma unroll
      for (int i = 0; i < 2; ++i) {
        const int k = (idx & 3) * 128 + i * 64 + lane;
        touch(tcw[i], srdO, k < 448 ? (XROT * (xcd & 3) * 448 + k) * (DM * 2) + pn2 * 128 : 0xfffffff0u);
      }
    }
    touch(tch[3], make_srd(bsp, SEQ * 2), pm1 * 256 + (lane & 1) * 128);
    touch(tch[0], srdW, lane < 16 ? row_flags(F_O, pm2, lane) : lane < 32 ? row_flags(F_H, pm1, lane) : lane < 48 ? col_flags(F_Z2, pn1, lane) : row_flags(F_Y, pm2, lane));
    touch(tch[1], srdW, lane < 16 ? row_flags(F_S1, pm1, lane) : lane < 32 ? col_flags(F_S1, pn1, lane) : col_flags(F_W3, pn1, lane));
    ABARS(2);
    wait_vm<0>();
    pin(tch[0]); pin(tch[1]); pin(tch[3]); pin(tcw[0]); pin(tcw[1]);
    st8_dev(srdW, F_S1 + myflag, tag2);
    ABARS(2);
    {
      const int pmh = pm1 > 0 ? pm1 - 1 : 0;
      const unsigned fo = lane < 16 ? row_flags(F_S1, pm1, lane) : lane < 32 ? row_flags(F_S1, pmh, lane) : col_flags(F_S1, pn1, lane);
      u32x2 f0;
      ld8_async(f0, srdW, fo);
      ABAR();
      wait_vm<0>();
      pin(f0);
      if (!__all(__builtin_bit_cast(u64, f0) == tag)) await(ws, fo, tag);
      const unsigned ro = WS_S1R + pm1 * 16384 + l16, co = lane < 48 ? WS_S1C + pn1 * 16384 + l16 : WS_S1R + pmh * 16384 + (56 + (lane & 7)) * 16;
      u32x4 gr[16], gc[16];
      f32x4 tr, tc;
      gather<0, 12>(gr, srdW, ro);
      ABAR();
      gather<12, 16>(gr, srdW, ro);
      gather<0, 8>(gc, srdW, co);
      ABAR();
      gather<8, 16>(gc, srdW, co);
      wait_vm<16>();
      accum<0, 16>(tr, gr);
      ABAR();
      put_rows(lds, L_MU, L_RS, 8 + lane, tr, 1.f / DM);
      wait_vm<0>();
      accum<0, 8>(tc, gc);
      accum<8, 16>(tc, gc);
      ABAR();
      if (lane < 48) *(LAS f32x4*)(lds + L_CS + l16) = tc;
      put_rows(lds, L_MU, L_RS, lane & 7, tc, 1.f / DM, lane >= 48 && lane < 56);
      wait_lgkm0();
    }
    {
      u32x4 wt[28], xt[16];
      const i32x4 srdWin = make_srd(win, DM * FF * 2), srdX = make_srd(x, SEQ * DM * 2);
      int pos = lane / 14, gi = lane - pos * 14;
#pragma unroll
      for (int i = 0; i < 14; ++i) {
        if (i == 3 || i == 9) ABAR();
        const unsigned v = swap23(pos) * (FF * 2) + (pn1 * 112 + gi * 8) * 2;
        ld16_nt(wt[i], srdWin, v, pm1 * (64u * FF * 2));
        ld16_nt(wt[14 + i], srdWin, v, pm1 * (64u * FF * 2) + FH * 2);
        gi += 8; pos += 4; if (gi >= 14) { gi -= 14; pos += 1; }
      }
      ABAR();
#pragma unroll
      for (int i = 0; i < 16; ++i) {
        const int r = i * 8 + (lane >> 3);
        ld16_plain(xt[i], srdX, (lane >> 3) * (DM * 2) + (((lane & 7) ^ ((r >> 1) & 7)) * 16), (pm2 * 128 + i * 8) * (DM * 2) + pn2 * 128);
      }
      wait_vm<16>();
#pragma unroll
      for (int i = 0; i < 28; ++i) { pin(wt[i]); *(LAS u32x4*)(lds + L_WT + i * 1024 + l16) = wt[i]; }
      wait_vm<0>();
#pragma unroll
      for (int i = 0; i < 16; ++i) { pin(xt[i]); *(LAS u32x4*)(lds + L_H + i * 1024 + l16) = xt[i]; }
    }
    ABAR();
    const int r2 = XROT * (xcd & 3) * 4;
    unsigned seen = await_mask(ws, row_flags(F_O, pm2, lane), tag, 0xfu << r2);
    wait_lgkm0();
    ABAR();
    auto gate = [&](unsigned fo, unsigned need) AI { if ((seen & need) != need) seen = await_mask(ws, fo, tag, need); };
    {
      const unsigned cb = L_WT + b224_base(lane);
      bf16x8 fr[2][2][2];
      float cs4[4];
      auto rd2 = [&](int q) AI {
#pragma unroll
        for (int u = 0; u < 2; ++u) {
          const int n = q * 2 + u;
          LAS char* p = lds + cb + (n / 7) * 14336 + (n % 7) * 32;
          fr[q & 1][u][0] = cat8(lds_tr(p), lds_tr(p + 8 * 224));
          fr[q & 1][u][1] = cat8(lds_tr(p + 32 * 224), lds_tr(p + 40 * 224));
        }
      };
      rd2(0);
#pragma unroll
      for (int q = 0; q < 7; ++q) {
        if (q > 0) gate(row_flags(F_O, pm2, lane), 1u << ((q + 3 + r2) & 15));
        ABAR();
#pragma unroll
        for (int u = 0; u < 2; ++u) {
          const int n = q * 2 + u;
#pragma unroll
          for (int kk = 0; kk < 2; ++kk)
            st16_dev(srdW, WS_W3 + (pn1 * 16 + pm1) * 28672 + (n / 7) * 14336 + (kk * 7 + n % 7) * 1024 + l16, __builtin_bit_cast(u32x4, fr[q & 1][u][kk]));
          cs4[n & 3] = mfma(ones, fr[q & 1][u][1], mfma(ones, fr[q & 1][u][0], zero4))[0];
        }
        if (q < 6) rd2(q + 1);
        if ((q & 1) || q == 6)
          st4_dev(srdW, WS_S2C + (pn1 * 16 + pm1) * 1024 + (q >> 1) * 256 + lane * 4,
                  __float_as_uint(lane < 32 ? (lane < 16 ? cs4[0] : cs4[1]) : (lane < 48 ? cs4[2] : cs4[3])));
      }
    }
    gate(row_flags(F_O, pm2, lane), 0xffffu);
    ABARS(2);
    wait_vm<0>();
    st8_dev(srdW, F_W3 + myflag, tag2);
    ABARS(5);
    {
      u32x2 f0;
      const unsigned fo = col_flags(F_W3, pn1, lane);
      ld8_async(f0, srdW, fo);
      ABARS(2);
      wait_vm<0>();
      pin(f0);
      if (!__all(__builtin_bit_cast(u64, f0) == tag)) await(ws, fo, tag);
    }
    ABAR();
    u32x4 hpark[16];
    {
      u32x4 gc[16];
      f32x4 tc;
      ABAR();
      gather<0, 16>(gc, srdW, WS_S2C + pn1 * 16384 + l16);
#pragma unroll
      for (int i = 0; i < 16; ++i) hpark[i] = *(LAS u32x4*)(lds + L_H + i * 1024 + l16);
      wait_vm<0>();
      accum<0, 16>(tc, gc);
      if (lane < 56) *(LAS f32x4*)(lds + L_CS + l16) = tc;
    }
    seen = await_mask(ws, row_flags(F_H, pm1, lane), tag, 0xfu);
    ABAR();
    ABAR();
#pragma unroll 1
    for (int t = 0; t < 4; ++t) { gate(row_flags(F_H, pm1, lane), 16u << t); ABAR(); }
    gate(row_flags(F_H, pm1, lane), 0xffffu);
    {
      u32x4 gr[16];
      f32x4 tr;
      const unsigned ro = WS_S2R + pm1 * 16384 + l16;
      ABAR();  gather<0, 4>(gr, srdW, ro);
      ABAR();  gather<4, 8>(gr, srdW, ro);
      ABAR();  gather<8, 12>(gr, srdW, ro);
      ABAR();  gather<12, 16>(gr, srdW, ro); wait_vm<8>(); accum<0, 8>(tr, gr);
      ABARS(2);
      wait_vm<0>(); accum<8, 16>(tr, gr);
      ABAR();
      put_rows(lds, L_MU, L_RS, lane, tr, 1.f / DM);
      wait_lgkm0();
    }
    ABARS(5);
    {
      const i32x4 srdSp = make_srd(wsp, SEQ * SEQ * 2);
#pragma unroll
      for (int i = 0; i < 2; ++i) touch(tch[i], srdSp, (pm1 * 128 + i * 64 + lane) * (SEQ * 2) + (XROT * (xcd >> 1) * 8 + 4 + (idx >> 3)) * 128);
    }
    dma16<0>(make_srd(bsp, SEQ * 2), lane < 16 ? pm1 * 256 + l16 : 0xfffffff0u, 0u, L_C2);
    ABARS(2);
    {
      u32x4 gv[16];
      f32x4 t;
      unsigned mx = 0;
      const unsigned so = WS_S3 + pm1 * 16384 + l16;
      gather<0, 8>(gv, srdW, so);
      ABAR();
      gather<8, 16>(gv, srdW, so);
      ABAR();
      wait_vm<8>();
      pin(tch[0]); pin(tch[1]);
      accum<0, 8>(t, gv);
#pragma unroll
      for (int j = 0; j < 8; ++j) mx = max(max(mx, gv[j][1]), gv[j][3]);
      pin(mx);
      ABAR();
      wait_vm<0>();
      accum<8, 16>(t, gv);
#pragma unroll
      for (int j = 8; j < 16; ++j) mx = max(max(mx, gv[j][1]), gv[j][3]);
      if (!__all(mx != ~0u)) {
#pragma unroll 1
        for (unsigned spin = 0; spin < (1u << 17); ++spin) {
          u32x4 gw[16];
          gather<0, 16>(gw, srdW, so);
          wait_vm<0>();
          accum<0, 16>(t, gw);
          mx = 0;
#pragma unroll
          for (int j = 0; j < 16; ++j) mx = max(max(mx, gw[j][1]), gw[j][3]);
          if (__all(mx != ~0u)) break;
        }
      }
      ABAR();
      put_rows(lds, L_M3, L_R3, lane, t, 1.f / FH);
      wait_lgkm0();
    }
    ABAR();
    ABARS(3);
    st8_dev(srdW, F_Z2 + myflag, tag2);
    ABARS(3);
    {
      u32x2 f0;
      const unsigned fo = col_flags(F_Z2, pn1, lane);
      ld8_async(f0, srdW, fo);
      ABARS(2);
      wait_vm<0>();
      pin(f0);
      if (!__all(__builtin_bit_cast(u64, f0) == tag)) await(ws, fo, tag);
    }
    ABAR();
#pragma unroll
    for (int i = 0; i < 16; ++i) *(LAS u32x4*)(lds + L_H + i * 1024 + l16) = hpark[i];
    ABARS(34);
    auto ymask = [&](int u) AI { const int k = rot(u, XROT * (xcd & 3) * 7, 28); return (1u << (k * 8 / 14)) | (1u << ((k * 8 + 7) / 14)); };
    seen = await_mask(ws, row_flags(F_Y, pm2, lane), tag, ymask(0) | ymask(1) | ymask(2) | ymask(3));
    wait_lgkm0();
    ABARS(2);
#pragma unroll 1
    for (int t = 0; t < 8; ++t) { gate(row_flags(F_Y, pm2, lane), ymask(t + 4)); ABAR(); }
    gate(row_flags(F_Y, pm2, lane), 0xffffu);
    ABARS(20);
    return;
  }

  if (wid >= 4) {
    const int dw = wid - 4;
    int slot;
    const unsigned l16 = lane * 16;
    {
      using namespace g1;
      using P = Pipe<NK, NS, 14, 0>;
      const i32x4 srdA = make_srd(x, SEQ * DM * 2), srdB = make_srd(wqkv, DM * 3 * DM * 2);
      unsigned va[6], vb[8];
#pragma unroll
      for (int i = 0; i < 8; ++i) {
        const int p = dw * 8 + i, pos = (p & 7) * 8 + (lane >> 3), nb = ((lane & 7) >> 1) ^ ((pos >> 1) & 3);
        vb[i] = swap23(pos) * (3 * DM * 2) + (dw * DM + pn1 * 64 + nb * 16 + (lane & 1) * 8) * 2 - (i & 3) * 1024;
      }
#pragma unroll
      for (int i = 0; i < 6; ++i) {
        const int r = (dw * 6 + i) * 8 + (lane >> 3);
        int grow = r < 128 ? pm1 * 128 + r : pm1 * 128 - 16 + (r - 128);
        grow = grow < 0 ? 8 : grow;
        va[i] = grow * (DM * 2) + (((lane & 7) ^ ((r >> 1) & 7)) * 16) - (i & 3) * 1024;
      }
      auto ia = [&](int t, int s) {
        const unsigned kt = (t + r1) & 15;
        dmaS<4>(srdA, va, kt * 128u, s * ST + dw * 6144u);
        dmaS<2>(srdA, va + 4, kt * 128u, s * ST + dw * 6144u + 4096u);
      };
      auto ib = [&](int t, int s) {
        const unsigned kt = (t + r1) & 15;
        dmaS<4>(srdB, vb, kt * (64u * 3 * DM * 2), s * ST + AB + dw * 8192u);
        dmaS<4>(srdB, vb + 4, kt * (64u * 3 * DM * 2), s * ST + AB + dw * 8192u + 4096u);
      };
      ib(0, 0); ia(0, 0); ib(1, 1);
      wait_vm<8>();
      HBAR();
      ia(1, 1); ia(2, 2); ib(2, 2);
      TS(14);
      slot = 0;
      g1_first_steps(dw, [&](int t) {
        wait_vm<P::PW>();
        HBAR();
        ia(t + NS, slot); ib(t + NS, slot);
        slot = slot + 1 == NS ? 0 : slot + 1;
      });
      TS(15);
      hsteps<P, 4, NK>(ia, ib, slot);
    }
    TS(1);
    auto lin16 = [&](unsigned src, unsigned dst) AI {
      const unsigned g0 = dw * 4096u, g1 = 12288u + dw * 1024u;
      dmaL<4>(srdW, l16, src + g0, dst + g0);
      if (dw == 2) dmaL<2>(srdW, l16, src + g1, dst + g1);
      else dmaL<1>(srdW, l16, src + g1, dst + g1);
    };
    unsigned vbs[3];
#pragma unroll
    for (int i = 0; i < 3; ++i) {
      const int pos = (i < 2 ? dw * 2 + i : (6 + dw) & 7) * 8 + (lane >> 3), nb = ((lane & 7) >> 1) ^ ((pos >> 1) & 3);
      vbs[i] = swap23(pos) * (DM * 2) + (pn2 * 64 + nb * 16 + (lane & 1) * 8) * 2 - (i == 1 ? 1024 : 0);
    }
    auto w8 = [&](i32x4 srd, unsigned kt, int s) AI {
      dmaS<2>(srd, vbs, kt * (64u * DM * 2), s * gs::ST + gs::AB + dw * 2048u);
      if (dw != 2) dmaS<1>(srd, vbs + 2, kt * (64u * DM * 2), s * gs::ST + gs::AB + (6 + dw) * 1024u);
    };
    const unsigned vflex = dw == 2 ? l16 : vbs[2];
    auto gs_steady = [&](auto T1, i32x4 srdB, unsigned abase, int r0, int nk) AI {
      i32x4 srdF;
#pragma unroll
      for (int e = 0; e < 4; ++e) srdF[e] = dw == 2 ? srdW[e] : srdB[e];
      steady<2, decltype(T1)::value, 16>(
          [&](int u) AI {
            const unsigned kt = rot(u, r0, nk), a = abase + kt * 16384u, b = kt * (64u * DM * 2), l = (u & 3) * gs::ST;
            return Nx{a + dw * 4096u, a + 12288u + dw * 1024u, b, dw == 2 ? a + 15360u : b,
                      l + dw * 4096u, l + 12288u + dw * 1024u, l + gs::AB + dw * 2048u, l + (dw == 2 ? 15360u : gs::AB + (6 + dw) * 1024u)};
          },
          [&](unsigned so, unsigned l) AI { dmaL<4>(srdW, l16, so, l); }, [&](unsigned so, unsigned l) AI { dmaS<2>(srdB, vbs, so, l); },
          [&](unsigned so, unsigned l) AI { dmaL<1>(srdW, l16, so, l); }, [&](unsigned so, unsigned l) AI { dmaL<1>(srdF, vflex, so, l); });
    };
    {
      using namespace gs;
      constexpr int NK = 16;
      using P = Pipe<NK, NS, 5, 3>;
      const i32x4 srdB = make_srd(wo, DM * DM * 2);
      const int r0 = XROT * (xcd & 3) * 4;
      auto ix = [&](int t, int s) { lin16(WS_O + (pm2 * 16 + ((t + r0) & 15)) * 16384u, s * ST); };
      auto iw = [&](int t, int s) { w8(srdB, (t + r0) & 15, s); };
      HBAR();
      iw(0, 0); iw(1, 1); iw(2, 2); iw(3, 3);
      HBAR();
      TS(2);
      HBAR();
      TS(3);
      hstart<P>(ix);
      slot = 0;
      hsteps<P, 0, 2>(ix, iw, slot);
      gs_steady(IC<NK - NS>{}, srdB, WS_O + pm2 * 16 * 16384u, r0, NK);
      hsteps<P, NK - NS, NK>(ix, iw, slot);
    }
    TS(4);
    {
      using namespace gf;
      using P = Pipe<NK, NS, 5, 5>;
      const i32x4 srdSp = make_srd(wsp, SEQ * SEQ * 2);
      auto lin14 = [&](unsigned src, int s) AI {
        const unsigned dst = B0 + s * SEG;
        dmaL<4>(srdW, l16, src + dw * 4096u, dst + dw * 4096u);
        if (dw != 2) dmaL<1>(srdW, l16, src + (12 + dw) * 1024u, dst + (12 + dw) * 1024u);
      };
      auto ia3 = [&](int t, int s) { lin16(WS_H + (pm1 * 16 + (t & 15)) * 16384u, s * AB); };
      auto ib3 = [&](int t, int s) { lin14(WS_W3 + (pn1 * 16 + (t & 15)) * 28672u + (t < 16 ? 14336u : 0u), s); };
      unsigned va4[6];
#pragma unroll
      for (int i = 0; i < 6; ++i) { const int r = (i < 4 ? dw * 4 + i : 8 + dw + i) * 8 + (lane >> 3); va4[i] = (pm1 * 128 + r) * (SEQ * 2) + (((lane & 7) ^ ((r >> 1) & 7)) * 16) - (i & 3) * 1024; }
      const int r4 = XROT * (xcd >> 1) * 8;
      auto ia4 = [&](int t, int s) {
        const unsigned kt = (t + r4) & 31;
        dmaS<4>(srdSp, va4, kt * 128u, s * AB + dw * 4096u);
        if (dw == 2) dmaS<2>(srdSp, va4 + 4, kt * 128u, s * AB + 14 * 1024u);
        else dmaS<1>(srdSp, va4 + 4, kt * 128u, s * AB + (12 + dw) * 1024u);
      };
      auto ib4 = [&](int t, int s) { lin14(WS_Z2 + (pn1 * 32 + ((t + r4) & 31)) * (unsigned)SEG, s); };
      HBAR();
      ib3(0, 0); ib3(1, 1); ib3(2, 2); ib3(3, 3);
      TS(5);
      HBAR();
      TS(6);
      hstart<P>(ia3);
      slot = 0;
      hsteps<P, 0, 2>(ia3, ib3, slot);
      auto L4 = [&](unsigned so, unsigned l) AI { dmaL<4>(srdW, l16, so, l); };
      auto L1 = [&](unsigned so, unsigned l) AI { dmaL<1>(srdW, l16, so, l); };
      steady<2, 28, 20>(
          [&](int u) AI {
            const unsigned a = WS_H + (pm1 * 16 + (u & 15)) * 16384u, b = WS_W3 + (pn1 * 16 + (u & 15)) * 28672u + (u < 16 ? 14336u : 0u);
            const unsigned la = (u & 3) * AB, lb = B0 + (u & 3) * SEG;
            return Nx{a + dw * 4096u, a + 12288u + dw * 1024u, b + dw * 4096u, dw == 2 ? a + 15360u : b + 12288u + dw * 1024u,
                      la + dw * 4096u, la + 12288u + dw * 1024u, lb + dw * 4096u, dw == 2 ? la + 15360u : lb + 12288u + dw * 1024u};
          },
          L4, L4, L1, L1);
      TS(9);
      wait_vm<20>(); HBAR(); ia4(32, 0);
      wait_vm<15>(); HBAR(); ia4(33, 1);
      wait_vm<10>(); HBAR(); ia4(34, 2);
      HBAR(); ia4(35, 3);
      ib4(32, 0); ib4(33, 1); ib4(34, 2); ib4(35, 3);
      wait_vm<12>(); HBAR();
      wait_vm<8>(); HBAR(); ib4(36, 0); ia4(36, 0);
      wait_vm<14>(); HBAR(); ib4(37, 1); ia4(37, 1);
      i32x4 srdF;
#pragma unroll
      for (int e = 0; e < 4; ++e) srdF[e] = dw == 2 ? srdSp[e] : srdW[e];
      const unsigned vf4 = dw == 2 ? va4[5] + 1024u : l16;
      steady<34, 60, 20>(
          [&](int u) AI {
            const unsigned kt = (u + r4) & 31, a = kt * 128u, b = WS_Z2 + (pn1 * 32 + kt) * (unsigned)SEG;
            const unsigned la = (u & 3) * AB, lb = B0 + (u & 3) * SEG;
            return Nx{a, a, b + dw * 4096u, dw == 2 ? a : b + 12288u + dw * 1024u,
                      la + dw * 4096u, la + 12288u + dw * 1024u, lb + dw * 4096u, dw == 2 ? la + 15360u : lb + 12288u + dw * 1024u};
          },
          [&](unsigned so, unsigned l) AI { dmaS<4>(srdSp, va4, so, l); }, L4,
          [&](unsigned so, unsigned l) AI { dmaS<1>(srdSp, va4 + 4, so, l); }, [&](unsigned so, unsigned l) AI { dmaL<1>(srdF, vf4, so, l); });
      hsteps<P, 60, NK>(ia4, ib4, slot);
    }
    TS(10);
    {
      using namespace gs;
      constexpr int NK = 28;
      using P = Pipe<NK, NS, 5, 3>;
      const i32x4 srdB = make_srd(wout, FH * DM * 2);
      const int r0 = XROT * (xcd & 3) * 7;
      auto ix = [&](int t, int s) { lin16(WS_Y + (pm2 * 28 + rot(t, r0, NK)) * 16384u, s * ST); };
      auto iw = [&](int t, int s) { w8(srdB, rot(t, r0, NK), s); };
      iw(0, 0); iw(1, 1);
      HBAR();
      iw(2, 2); iw(3, 3);
      TS(11);
      HBAR();
      TS(12);
      hstart<P>(ix);
      slot = 0;
      hsteps<P, 0, 2>(ix, iw, slot);
      gs_steady(IC<NK - NS>{}, srdB, WS_Y + pm2 * 28 * 16384u, r0, NK);
      hsteps<P, NK - NS, NK>(ix, iw, slot);
    }
    TS(13);
    return;
  }

  const u64 tag = uniform64(__hip_atomic_load((gu64*)(ws + WS_EPOCH), __ATOMIC_RELAXED, __HIP_MEMORY_SCOPE_AGENT)) + 1;
  if (wid == 1) {
    u64 pc;
    asm volatile("s_getpc_b64 %0" : "=s"(pc));
    const unsigned v = *(const gu32*)((pc & ~127ull) + (idx * 14 + (lane < 14 ? lane : 0)) * 128);
    asm volatile("" :: "v"(v));
  }
  if (tid == 0) *(LAS u64*)(lds + L_TAG) = tag;
  wait_lgkm0();
#define LANES const int tid = fresh_tid(), lane = tid & 63, i16 = lane & 15, g = lane >> 4; (void)i16; (void)g
  auto publish = [&](unsigned flag) {
    wait_vm<0>();
    bar();
    if (threadIdx.x < 16) set_flag(ws, flag + threadIdx.x * 8, tag);
    bar();
  };
  {
    using namespace g1;
    LANES;
    const int hd = wid & 1, rh = wid >> 1;
    unsigned ab[2], hb[2], bb[2];
#pragma unroll
    for (int kk = 0; kk < 2; ++kk) { ab[kk] = a_base(lane, kk) + rh * 64 * 128; hb[kk] = a_base(lane, kk) + 128 * 128; bb[kk] = AB + b128_base(lane, hd * 2 + kk); }
    f32x4 aq[4][2], ak[4][2], av[4][2], ah[2];
#pragma unroll
    for (int m = 0; m < 4; ++m)
#pragma unroll
      for (int j = 0; j < 2; ++j) aq[m][j] = ak[m][j] = av[m][j] = zero4;
    ah[0] = ah[1] = zero4;
    auto load = [&](Fr& f, unsigned so, unsigned, int kk) {
#pragma unroll
      for (int m = 0; m < 4; ++m) f.a[m] = lds_a(lds + so + ab[kk] + m * 2048);
      f.a[4] = lds_a(lds + so + hb[kk]);
#pragma unroll
      for (int s = 0; s < 3; ++s)
#pragma unroll
        for (int j = 0; j < 2; ++j)
#pragma unroll
          for (int hh = 0; hh < 2; ++hh) f.b[s * 2 + j][hh] = lds_tr(lds + so + bb[j] + s * 8192 + (kk * 32 + hh * 8) * 128);
    };
    auto mma2 = [&](Fr& f, auto RH) {
      bf16x8 b[6];
#pragma unroll
      for (int i = 0; i < 6; ++i) b[i] = cat8(f.b[i][0], f.b[i][1]);
#pragma unroll
      for (int m = 0; m < 4; ++m)
#pragma unroll
        for (int j = 0; j < 2; ++j) {
          aq[m][j] = mfma(b[j], f.a[m], aq[m][j]);
          ak[m][j] = mfma(b[2 + j], f.a[m], ak[m][j]);
          av[m][j] = mfma(f.a[m], b[4 + j], av[m][j]);
        }
#pragma unroll
      for (int j = 0; j < 2; ++j) {
        if constexpr (decltype(RH)::value == 0) ah[j] = mfma(b[2 + j], f.a[4], ah[j]);
        else ah[j] = mfma(f.a[4], b[4 + j], ah[j]);
      }
    };
    const int ra = i16 & 1;
    const bool curA = i16 - 4 * g - ra >= 0, curB = i16 - 4 * g - ra - 2 >= 0;
    LAS char* t16 = lds + L_MU + (16 + rh * 64 + i16) * 4;
    LAS char* t4g = lds + L_MU + (16 + rh * 64 + 4 * g) * 4;
    LAS char* tka = lds + L_RS + (rh * 64 + 4 * g + ra + (curA ? 16 : 0)) * 4;
    LAS char* tkb = lds + L_RS + (rh * 64 + 4 * g + ra + 2 + (curB ? 16 : 0)) * 4;
    LAS char* xch = lds + L_X + hd * 3072 + lane * 16;
    unsigned oaddr = WS_O + (pm1 * 16 + pn1) * 16384 + (hd * 4 + (g >> 1)) * 2048 + (rh * 64 + (g & 1) * 16 + i16) * 16;
    unsigned mA = curA ? ~0u : 0u, mB = curB ? ~0u : 0u;
    pin(t16); pin(t4g); pin(tka); pin(tkb); pin(xch); pin(oaddr); pin(mA); pin(mB);
    if (rh == 0) compute_loop<NK, NS, ST, ST, 17, 26, Fr>(load, [&](Fr& f) { mma2(f, IC<0>{}); });
    else compute_loop<NK, NS, ST, ST, 17, 26, Fr>(load, [&](Fr& f) { mma2(f, IC<1>{}); });
    TS(1);
    f32x4 csq[2], csk[2]; float csv[2];
    const int co = L_CS - L_MU + (hd * 32 - 16 - rh * 64) * 4;
#pragma unroll
    for (int j = 0; j < 2; ++j) {
      csq[j] = *(LAS f32x4*)(t4g + co + j * 64);
      csk[j] = *(LAS f32x4*)(t4g + co + 256 + j * 64);
      csv[j] = *(LAS float*)(t16 + co + 512 + j * 64);
    }
    bf16x8 Q[4], K[5]; u32x2 V[5][2];
    float rq[4], rka[4], rkb[4];
#pragma unroll
    for (int m = 3; m >= 0; --m) {
      const float mu = *(LAS float*)(t16 + m * 64);
      const f32x4 mu4 = *(LAS f32x4*)(t4g + m * 64);
      rq[m] = *(LAS float*)(t16 + (L_RS - L_MU) + m * 64);
      rka[m] = *(LAS float*)(tka + m * 64);
      rkb[m] = *(LAS float*)(tkb + m * 64);
      K[m + 1] = cat8(pk4(ak[m][0] - mu * csk[0]), pk4(ak[m][1] - mu * csk[1]));
      V[m + 1][0] = pk4(av[m][0] - mu4 * csv[0]);
      V[m + 1][1] = pk4(av[m][1] - mu4 * csv[1]);
      Q[m] = cat8(pk4(aq[m][0] - mu * csq[0]), pk4(aq[m][1] - mu * csq[1]));
    }
    if (rh == 0) {
      const float mu = *(LAS float*)(t16 - 64);
      K[0] = cat8(pk4(ah[0] - mu * csk[0]), pk4(ah[1] - mu * csk[1]));
      *(LAS bf16x8*)(xch) = K[4];
      *(LAS bf16x8*)(xch + 1024) = cat8(V[4][0], V[4][1]);
    } else {
      const f32x4 mu4 = *(LAS f32x4*)(t4g - 320);
      *(LAS bf16x8*)(xch + 2048) = cat8(pk4(ah[0] - mu4 * csv[0]), pk4(ah[1] - mu4 * csv[1]));
      K[0] = K[4];
    }
    wait_lgkm0(); bar();
    {
      u32x4 v = *(LAS u32x4*)(xch + (rh == 0 ? 2048 : 1024));
      V[0][0] = (u32x2){v[0], v[1]}; V[0][1] = (u32x2){v[2], v[3]};
      if (rh == 1) K[0] = *(LAS bf16x8*)(xch);
    }
    constexpr float SC = 0.17677669529663687f * 1.4426950408889634f;
    f32x4 sp[4], sc[4];
#pragma unroll
    for (int m = 3; m >= 0; --m) { sc[m] = mfma(K[m + 1], Q[m], zero4); sp[m] = mfma(K[m], Q[m], zero4); }
    const bool nohalo = pm1 == 0 && rh == 0;
    const unsigned selA = ra ? 0x01000c0cu : 0x0c0c0100u, selB = ra ? 0x03020c0cu : 0x0c0c0302u;
    const unsigned mR = ra ? ~0u : 0u;
    float la[4], lb[4], mx[4];
#pragma unroll
    for (int m = 0; m < 4; ++m) {
      const float a = bsel(mA, bsel(mR, sc[m][1], sc[m][0]), bsel(mR, sp[m][1], sp[m][0]));
      const float b = bsel(mB, bsel(mR, sc[m][3], sc[m][2]), bsel(mR, sp[m][3], sp[m][2]));
      const float f = SC * rq[m];
      la[m] = a * (f * rka[m]); lb[m] = b * (f * rkb[m]);
    }
    la[0] = bsel(nohalo ? mA : ~0u, la[0], -INFINITY); lb[0] = bsel(nohalo ? mB : ~0u, lb[0], -INFINITY);
#pragma unroll
    for (int m = 0; m < 4; ++m) mx[m] = max4(vmax(la[m], lb[m]));
    f32x4 o[4][2];
#pragma unroll
    for (int m = 0; m < 4; ++m) { la[m] = __builtin_amdgcn_exp2f(la[m] - mx[m]); lb[m] = __builtin_amdgcn_exp2f(lb[m] - mx[m]); }
#pragma unroll
    for (int m = 0; m < 4; ++m) mx[m] = __builtin_amdgcn_rcpf(sum4(la[m] + lb[m]));
#pragma unroll
    for (int m = 0; m < 4; ++m) {
      const unsigned w = pk2(la[m] * (mx[m] * rka[m]), lb[m] * (mx[m] * rkb[m]));
      const unsigned wa = __builtin_amdgcn_perm(0u, w, selA), wb = __builtin_amdgcn_perm(0u, w, selB);
      const bf16x8 P = __builtin_bit_cast(bf16x8, (u32x4){wa & ~mA, wb & ~mB, wa & mA, wb & mB});
#pragma unroll
      for (int db = 0; db < 2; ++db) o[m][db] = mfma(cat8(V[m][db], V[m + 1][db]), P, zero4);
    }
    TS(2);
#pragma unroll
    for (int p = 0; p < 2; ++p)
#pragma unroll
      for (int db = 0; db < 2; ++db)
        st16_dev(srdW, oaddr + db * 4096 + p * 512, chunk16(pk4(o[2 * p][db]), pk4(o[2 * p + 1][db])));
    TS(3);
    publish(F_O + (pm1 * 16 + pn1) * 128);
  }
  auto small_gemm = [&](auto NKC, u32x2 (&res)[2][4]) AI {
    using namespace gs;
    LANES;
    unsigned ab[2], bb[4];
#pragma unroll
    for (int kk = 0; kk < 2; ++kk) ab[kk] = (kk * 4 + g) * 2048 + (wid * 32 + i16) * 16;
#pragma unroll
    for (int j = 0; j < 4; ++j) bb[j] = AB + b128_base(lane, j);
    f32x4 acc[2][4];
#pragma unroll
    for (int m = 0; m < 2; ++m)
#pragma unroll
      for (int j = 0; j < 4; ++j) acc[m][j] = unpk4(*(LAS u32x2*)(lds + L_H + img_off(wid * 32 + m * 16 + i16, j * 2 + (g >> 1)) + (g & 1) * 8));
    auto load = [&](Fr& f, unsigned so, unsigned) {
#pragma unroll
      for (int kk = 0; kk < 2; ++kk) {
#pragma unroll
        for (int m = 0; m < 2; ++m) f.a[kk][m] = lds_a(lds + so + ab[kk] + m * 256);
#pragma unroll
        for (int j = 0; j < 4; ++j)
#pragma unroll
          for (int hh = 0; hh < 2; ++hh) f.b[kk][j][hh] = lds_tr(lds + so + bb[j] + (kk * 32 + hh * 8) * 128);
      }
    };
    auto mma = [&](Fr& f) {
#pragma unroll
      for (int kk = 0; kk < 2; ++kk)
#pragma unroll
        for (int m = 0; m < 2; ++m)
#pragma unroll
          for (int j = 0; j < 4; ++j) acc[m][j] = mfma(cat8(f.b[kk][j][0], f.b[kk][j][1]), f.a[kk][m], acc[m][j]);
    };
    Fr f0;
    unsigned sa = 0, sb = 0;
    bar();
    load(f0, 0u, 0u);
    deep_segment<decltype(NKC)::value, NS, ST, ST, 20, 16>(f0, sa, sb, load, mma);
#pragma unroll
    for (int m = 0; m < 2; ++m)
#pragma unroll
      for (int j = 0; j < 4; ++j) res[m][j] = pk4(acc[m][j]);
  };
  {
    u32x2 h[2][4];
    small_gemm(IC<16>{}, h);
    TS(5);
    LANES;
#pragma unroll
    for (int j = 0; j < 4; ++j)
      st16_dev(srdW, WS_H + (pm2 * 16 + pn2) * 16384 + (j * 2 + (g >> 1)) * 2048 + (wid * 32 + (g & 1) * 16 + i16) * 16, chunk16(h[0][j], h[1][j]));
    const unsigned o = diag_lane ? WS_S2R + (pm2 * 16 + pn2) * 1024 + (wid * 32 + i16) * 8 : 0xffff0000u;
#pragma unroll
    for (int m = 0; m < 2; ++m) st8_dev(srdW, o + m * 128, __builtin_bit_cast(u32x2, rowstat(cat8(h[m][0], h[m][1]), cat8(h[m][2], h[m][3]))));
#pragma unroll
    for (int m = 0; m < 2; ++m)
#pragma unroll
      for (int j = 0; j < 4; ++j) *(LAS u32x2*)(lds + L_H + img_off(wid * 32 + m * 16 + i16, j * 2 + (g >> 1)) + (g & 1) * 8) = h[m][j];
    wait_lgkm0();
    TS(6);
    publish(F_H + (pm2 * 16 + pn2) * 128);
  }
  {
    using namespace gf;
    LANES;
    unsigned ab[2];
#pragma unroll
    for (int kk = 0; kk < 2; ++kk) ab[kk] = (kk * 4 + g) * 2048 + (wid * 32 + i16) * 16;
    const unsigned bb = B0 + lane * 16;
    f32x4 acc[2][7];
    LAS char *pa0 = lds + ab[0], *pa1 = lds + ab[1], *pb = lds + bb;
    unsigned sa = 0, sb = 0;
    auto rd = [&](Fr& f, int r, int ms) AI {
      const int kk = r / 9, q = r % 9;
      if (q < 2) f.a[kk][q] = lds_a((kk ? pa1 : pa0) + q * ms);
      else f.b[kk][q - 2] = lds_a(pb + (kk * 7 + q - 2) * 1024);
    };
    auto advance = [&]() AI {
      sa += AB; sb += SEG; if (sa == NS * AB) { sa = 0; sb = 0; }
      pa0 = lds + (sa + ab[0]); pa1 = lds + (sa + ab[1]); pb = lds + (sb + bb);
    };
    auto tile = [&](Fr& c, Fr& n, int ms) AI {
      cbar();
#pragma unroll
      for (int i = 0; i < 28; ++i) {
        const int kk = i / 14, m = (i % 14) / 7, j = i % 7;
        asm volatile("v_mfma_f32_16x16x32_bf16 %0, %1, %2, %0" : "+v"(acc[m][j]) : "v"(c.b[kk][j]), "v"(c.a[kk][m]));
        if (i < 18) rd(n, i, ms);
        if (i == 19) { advance(); pin(pa0); pin(pa1); pin(pb); }
        __builtin_amdgcn_sched_barrier(0);
      }
    };
    Fr f0;
    auto segment = [&](int n, int ms) AI {
      Fr f1;
#pragma unroll 1
      for (int t = 0; t < n; t += 2) { tile(f0, f1, ms); tile(f1, f0, ms); }
      asm volatile("s_nop 7\n\ts_nop 1" ::: "memory");
      __builtin_amdgcn_sched_barrier(0);
    };
    u32x2 zz[2][7];
    float mu[2], c1[2], c2[2], ir[2];
    auto rowconst = [&]() AI {
#pragma unroll
      for (int m = 0; m < 2; ++m) {
        mu[m] = -*(LAS float*)(lds + L_MU + (wid * 32 + m * 16 + i16) * 4);
        const float rs = *(LAS float*)(lds + L_RS + (wid * 32 + m * 16 + i16) * 4);
        c1[m] = -2.302208198f * rs; c2[m] = -0.1029432397f * rs * rs * rs; ir[m] = __builtin_amdgcn_rcpf(rs);
      }
    };
    auto act = [&](bool centre) AI {
#pragma unroll
      for (int j0 = 0; j0 < 7; j0 += 2) {
        f32x4 e[4], cs[2];
        const int n = j0 < 6 ? 4 : 2;
        if (centre) {
          cs[0] = *(LAS f32x4*)(lds + L_CS + (112 + j0 * 16 + 4 * g) * 4);
          cs[1] = *(LAS f32x4*)(lds + L_CS + (112 + (j0 < 6 ? j0 + 1 : j0) * 16 + 4 * g) * 4);
#pragma unroll
          for (int u = 0; u < 4; ++u) if (u < n) acc[u & 1][j0 + (u >> 1)] += mu[u & 1] * cs[u >> 1];
        }
#pragma unroll
        for (int u = 0; u < 4; ++u) if (u < n) e[u] = acc[u & 1][j0 + (u >> 1)] * acc[u & 1][j0 + (u >> 1)];
#pragma unroll
        for (int u = 0; u < 4; ++u) if (u < n) e[u] = e[u] * c2[u & 1] + c1[u & 1];
#pragma unroll
        for (int u = 0; u < 4; ++u) if (u < n) e[u] = e[u] * acc[u & 1][j0 + (u >> 1)];
#pragma unroll
        for (int u = 0; u < 4; ++u) if (u < n)
#pragma unroll
          for (int i = 0; i < 4; ++i) e[u][i] = __builtin_amdgcn_exp2f(e[u][i]);
#pragma unroll
        for (int u = 0; u < 4; ++u) if (u < n) e[u] = e[u] * ir[u & 1] + ir[u & 1];
#pragma unroll
        for (int u = 0; u < 4; ++u) if (u < n)
#pragma unroll
          for (int i = 0; i < 4; ++i) e[u][i] = __builtin_amdgcn_rcpf(e[u][i]);
#pragma unroll
        for (int u = 0; u < 4; ++u) if (u < n) { zz[u & 1][j0 + (u >> 1)] = pk4(acc[u & 1][j0 + (u >> 1)] * e[u]); pin(zz[u & 1][j0 + (u >> 1)]); }
        __builtin_amdgcn_sched_barrier(0);
      }
    };
#pragma unroll
    for (int m = 0; m < 2; ++m)
#pragma unroll
      for (int j = 0; j < 7; ++j) acc[m][j] = zero4;
    bar();
#pragma unroll
    for (int r = 0; r < 18; ++r) rd(f0, r, 256);
    advance();
    segment(16, 256);
    TS(7);
    rowconst();
    act(true);
    {
      f32x2 st[2];
#pragma unroll
      for (int m = 0; m < 2; ++m)
        st[m] = rowstat(cat8(zz[m][0], zz[m][1]), cat8(zz[m][2], zz[m][3])) + rowstat(cat8(zz[m][4], zz[m][5]), cat8(zz[m][6], (u32x2){0u, 0u}));
      const unsigned o = diag_lane ? WS_S3 + (pm1 * 16 + pn1) * 1024 + (wid * 32 + i16) * 8 : 0xffff0000u;
#pragma unroll
      for (int m = 0; m < 2; ++m) st8_dev(srdW, o + m * 128, (u32x2){__float_as_uint(st[m][0]), min(__float_as_uint(st[m][1]), ~1u)});
    }
#pragma unroll
    for (int j = 0; j < 7; ++j) {
      const f32x4 cs = *(LAS f32x4*)(lds + L_CS + (j * 16 + 4 * g) * 4);
      acc[0][j] = mu[0] * cs; acc[1][j] = mu[1] * cs;
    }
    asm volatile("s_nop 1" ::: "memory");
    TS(8);
    Fr f1;
    segment(6, 256);
    tile(f0, f1, 256);
    TS(9);
#pragma unroll
    for (int m = 0; m < 2; ++m) {
      const float r3 = *(LAS float*)(lds + L_R3 + (wid * 32 + m * 16 + i16) * 4), m3 = -r3 * *(LAS float*)(lds + L_M3 + (wid * 32 + m * 16 + i16) * 4);
#pragma unroll
      for (int j = 0; j < 7; ++j)
        *(LAS u32x2*)(lds + L_ZS + (wid * 32 + m * 16 + swap23(i16)) * 232 + 8 * g + j * 32) = pk4(unpk4(zz[m][j]) * r3 + m3);
    }
    wait_lgkm0();
    {
      LAS char* p = lds + L_ZS + (wid * 32 + b_pos_lane(lane)) * 232 + (lane & 3) * 8;
      u32x2 lo[7], hi[7];
#pragma unroll
      for (int j = 0; j < 7; ++j) { lo[j] = lds_tr(p + j * 32); hi[j] = lds_tr(p + j * 32 + 8 * 232); }
#pragma unroll
      for (int j = 0; j < 7; ++j)
        st16_dev(srdW, WS_Z2 + (pn1 * 32 + pm1 * 2) * SEG + (wid * 7 + j) * 1024 + lane * 16, (u32x4){lo[j][0], lo[j][1], hi[j][0], hi[j][1]});
    }
    TS(10);
    tile(f1, f0, 256);
    tile(f0, f1, 256);
    wait_vm<0>();
    tile(f1, f0, 256);
    segment(6, 256);
    TS(11);
    rowconst();
    act(false);
#pragma unroll
    for (int m = 0; m < 2; ++m) {
      const float b = (float)*(LAS __bf16*)(lds + L_C2 + (wid * 32 + m * 16 + i16) * 2);
#pragma unroll
      for (int j = 0; j < 7; ++j) acc[m][j] = zero4 + b;
    }
    TS(12);
    bar();
    ab[0] = a_base(lane, 0) + wid * 32 * 128; ab[1] = a_base(lane, 1) + wid * 32 * 128;
    pa0 = lds + ab[0]; pa1 = lds + ab[1]; pb = lds + bb; sa = 0; sb = 0;
#pragma unroll
    for (int r = 0; r < 18; ++r) rd(f0, r, 2048);
    advance();
    segment(32, 2048);
    TS(13);
#pragma unroll
    for (int j = 0; j < 7; ++j)
      st16_dev(srdW, WS_Y + (pm1 * 16 + pn1) * 28672 + (j * 2 + (g >> 1)) * 2048 + (wid * 32 + (g & 1) * 16 + i16) * 16,
               chunk16(pk4(unpk4(zz[0][j]) * acc[0][j]), pk4(unpk4(zz[1][j]) * acc[1][j])));
    TS(15);
    publish(F_Y + (pm1 * 16 + pn1) * 128);
  }
  {
    u32x2 r[2][4];
    small_gemm(IC<28>{}, r);
    TS(16);
    LANES;
#pragma unroll
    for (int m = 0; m < 2; ++m)
#pragma unroll
      for (int p = 0; p < 2; ++p)
        *(u32x4*)(out + (size_t)(pm2 * 128 + wid * 32 + m * 16 + i16) * (DM * 2) + pn2 * 128 + (p * 4 + (g & 1) * 2 + (g >> 1)) * 16) = chunk16(r[m][2 * p], r[m][2 * p + 1]);
  }
  if (blockIdx.x == 0 && tid == 0) __hip_atomic_store((gu64*)(ws + WS_EPOCH), tag, __ATOMIC_RELAXED, __HIP_MEMORY_SCOPE_AGENT);
  TS(17);
}

extern "C" void kernel_launch(void* const* d_in, const int* in_sizes, int n_in, void* d_out, int out_size, void* d_ws, size_t ws_size,
                              hipStream_t stream) {
  (void)hipFuncSetAttribute((const void*)k_mega, hipFuncAttributeMaxDynamicSharedMemorySize, LDS_BYTES);
  k_mega<<<256, 512, LDS_BYTES, stream>>>((const __bf16*)d_in[0], (const __bf16*)d_in[1], (const __bf16*)d_in[2], (const __bf16*)d_in[4],
                                          (const __bf16*)d_in[5], (const __bf16*)d_in[6], (const __bf16*)d_in[7], (char*)d_out, (char*)d_ws + (size_t)WSBASE * 1048576);
}
```

```cpp
#include <hip/hip_runtime.h>
#include <hip/hip_bf16.h>
#include <stdint.h>
#include <type_traits>

#define DI __device__ __forceinline__
#define LAS __attribute__((address_space(3)))
#define AI __attribute__((always_inline))
typedef __bf16 bf16x8 __attribute__((ext_vector_type(8)));
typedef __bf16 bf16x2 __attribute__((ext_vector_type(2)));
typedef short s16x4 __attribute__((ext_vector_type(4)));
typedef float f32x4 __attribute__((ext_vector_type(4)));
typedef float f32x2 __attribute__((ext_vector_type(2)));
typedef int i32x4 __attribute__((ext_vector_type(4)));
typedef unsigned u32x4 __attribute__((ext_vector_type(4)));
typedef unsigned u32x2 __attribute__((ext_vector_type(2)));
typedef unsigned u32x32 __attribute__((ext_vector_type(32)));
typedef unsigned long long u64;
typedef __attribute__((address_space(1))) u64 gu64;
typedef __attribute__((address_space(1))) unsigned gu32;
template <int V> using IC = std::integral_constant<int, V>;

#define POLL_SLEEP 1
#define CODEPF 1
#define FLAGPF 1
#define WEARLY 0
#define W2POS 2
#define AUXLATE 0
#define WOTOUCH 0
#define XROT 1
#define WSBASE 0

#define CODE_BYTES 66000
constexpr int SEQ = 2048, DM = 1024, FF = 3584, FH = 1792;
constexpr float LN_EPS = 1e-5f;

constexpr unsigned WS_EPOCH = 0;
constexpr unsigned F_S1 = 32768, F_O = 2 * 32768, F_H = 3 * 32768, F_W3 = 4 * 32768, F_S3 = 5 * 32768, F_Z2 = 6 * 32768, F_Y = 7 * 32768;
constexpr unsigned WS_S1R = 1u << 18;
constexpr unsigned WS_S1C = WS_S1R + (1u << 18);
constexpr unsigned WS_S2R = WS_S1C + (1u << 18);
constexpr unsigned WS_S2C = WS_S2R + (1u << 18);
constexpr unsigned WS_S3 = WS_S2C + (1u << 18);
constexpr unsigned WS_O = 2u << 20;
constexpr unsigned WS_H = WS_O + (4u << 20);
constexpr unsigned WS_Z2 = WS_H + (4u << 20);
constexpr unsigned WS_Y = WS_Z2 + (8u << 20);
constexpr unsigned WS_W3 = WS_Y + (8u << 20);
constexpr unsigned WS_END = WS_W3 + (8u << 20);
constexpr size_t WS_TS = 64u << 20;
#define TS(i) do {} while (0)

constexpr unsigned L_WT = 98304;
constexpr unsigned L_ZS = 122880;
constexpr unsigned L_X = 129024;
constexpr unsigned L_H = 135168;
constexpr unsigned L_MISC = 153600;
constexpr unsigned L_MU = L_MISC, L_RS = L_MU + 576, L_CS = L_RS + 576, L_P = L_CS + 896, L_M3 = L_P + 2048, L_R3 = L_M3 + 512, L_TAG = L_R3 + 512,
                   L_C2 = L_TAG + 16;
constexpr unsigned LDS_BYTES = 163840;
static_assert(L_C2 + 2 * 896 <= LDS_BYTES, "lds");

DI i32x4 make_srd(const void* p, unsigned bytes) {
  u64 a = (u64)p;
  i32x4 r;
  r[0] = __builtin_amdgcn_readfirstlane((int)(unsigned)a);
  r[1] = __builtin_amdgcn_readfirstlane((int)(unsigned)(a >> 32));
  r[2] = (int)bytes;
  r[3] = 0x00020000;
  return r;
}
template <int POL> DI void dma16(i32x4 srd, unsigned voff, unsigned soff, unsigned ldsaddr) {
  if constexpr (POL == 2)
    asm volatile("s_mov_b32 m0, %0\n\ts_nop 0\n\tbuffer_load_dwordx4 %1, %2, %3 offen nt lds" :: "s"(ldsaddr), "v"(voff), "s"(srd), "s"(soff) : "memory");
  else if constexpr (POL == 1)
    asm volatile("s_mov_b32 m0, %0\n\ts_nop 0\n\tbuffer_load_dwordx4 %1, %2, %3 offen sc1 lds" :: "s"(ldsaddr), "v"(voff), "s"(srd), "s"(soff) : "memory");
  else
    asm volatile("s_mov_b32 m0, %0\n\ts_nop 0\n\tbuffer_load_dwordx4 %1, %2, %3 offen lds" :: "s"(ldsaddr), "v"(voff), "s"(srd), "s"(soff) : "memory");
}
template <int N> DI void dmaL(i32x4 srd, unsigned v, unsigned so, unsigned l) {
  if constexpr (N == 1) asm volatile("s_mov_b32 m0, %0\n\ts_nop 0\n\tbuffer_load_dwordx4 %1, %2, %3 offen sc1 lds" :: "s"(l), "v"(v), "s"(srd), "s"(so) : "memory");
  if constexpr (N == 2) asm volatile("s_mov_b32 m0, %0\n\ts_nop 0\n\tbuffer_load_dwordx4 %1, %2, %3 offen sc1 lds\n\tbuffer_load_dwordx4 %1, %2, %3 offen offset:1024 sc1 lds" :: "s"(l), "v"(v), "s"(srd), "s"(so) : "memory");
  if constexpr (N == 4) asm volatile("s_mov_b32 m0, %0\n\ts_nop 0\n\tbuffer_load_dwordx4 %1, %2, %3 offen sc1 lds\n\tbuffer_load_dwordx4 %1, %2, %3 offen offset:1024 sc1 lds\n\t"
                                     "buffer_load_dwordx4 %1, %2, %3 offen offset:2048 sc1 lds\n\tbuffer_load_dwordx4 %1, %2, %3 offen offset:3072 sc1 lds" :: "s"(l), "v"(v), "s"(srd), "s"(so) : "memory");
}
template <int N> DI void dmaS(i32x4 srd, const unsigned* v, unsigned so, unsigned l) {
  if constexpr (N == 1) asm volatile("s_mov_b32 m0, %0\n\ts_nop 0\n\tbuffer_load_dwordx4 %1, %2, %3 offen lds" :: "s"(l), "v"(v[0]), "s"(srd), "s"(so) : "memory");
  if constexpr (N == 2) asm volatile("s_mov_b32 m0, %0\n\ts_nop 0\n\tbuffer_load_dwordx4 %1, %2, %3 offen lds\n\tbuffer_load_dwordx4 %4, %2, %3 offen offset:1024 lds" :: "s"(l), "v"(v[0]), "s"(srd), "s"(so), "v"(v[1]) : "memory");
  if constexpr (N == 4) asm volatile("s_mov_b32 m0, %0\n\ts_nop 0\n\tbuffer_load_dwordx4 %1, %2, %3 offen lds\n\tbuffer_load_dwordx4 %4, %2, %3 offen offset:1024 lds\n\t"
                                     "buffer_load_dwordx4 %5, %2, %3 offen offset:2048 lds\n\tbuffer_load_dwordx4 %6, %2, %3 offen offset:3072 lds"
                                     :: "s"(l), "v"(v[0]), "s"(srd), "s"(so), "v"(v[1]), "v"(v[2]), "v"(v[3]) : "memory");
}
DI void ld16_async(u32x4& d, i32x4 srd, unsigned voff) { asm volatile("buffer_load_dwordx4 %0, %1, %2, 0 offen sc1" : "=v"(d) : "v"(voff), "s"(srd) : "memory"); }
DI void ld8_async(u32x2& d, i32x4 srd, unsigned voff) { asm volatile("buffer_load_dwordx2 %0, %1, %2, 0 offen sc1" : "=v"(d) : "v"(voff), "s"(srd) : "memory"); }
DI void touch(unsigned& d, i32x4 srd, unsigned voff) { asm volatile("buffer_load_dword %0, %1, %2, 0 offen" : "=v"(d) : "v"(voff), "s"(srd) : "memory"); }
template <int J0, int J1> DI void gather(u32x4 (&d)[16], i32x4 srd, unsigned off);
DI void ld16_nt(u32x4& d, i32x4 srd, unsigned voff, unsigned soff) { asm volatile("buffer_load_dwordx4 %0, %1, %2, %3 offen nt" : "=v"(d) : "v"(voff), "s"(srd), "s"(soff) : "memory"); }
DI void ld16_plain(u32x4& d, i32x4 srd, unsigned voff, unsigned soff) { asm volatile("buffer_load_dwordx4 %0, %1, %2, %3 offen" : "=v"(d) : "v"(voff), "s"(srd), "s"(soff) : "memory"); }
template <class T> DI void pin(T& v) { asm volatile("" : "+v"(v)); }
DI void st16_dev(i32x4 srd, unsigned voff, u32x4 v) { asm volatile("buffer_store_dwordx4 %0, %1, %2, 0 offen sc1\n\ts_nop 1" :: "v"(v), "v"(voff), "s"(srd) : "memory"); }
DI void st8_dev(i32x4 srd, unsigned voff, u32x2 v) { asm volatile("buffer_store_dwordx2 %0, %1, %2, 0 offen sc1" :: "v"(v), "v"(voff), "s"(srd) : "memory"); }
DI void st4_dev(i32x4 srd, unsigned voff, unsigned v) { asm volatile("buffer_store_dword %0, %1, %2, 0 offen sc1" :: "v"(v), "v"(voff), "s"(srd) : "memory"); }
template <int N> DI void wait_vm() { asm volatile("s_waitcnt vmcnt(%0)" :: "i"(N) : "memory"); }
DI void wait_lgkm0() { __builtin_amdgcn_s_waitcnt(0xC07F); asm volatile("" ::: "memory"); }
DI void bar() { asm volatile("" ::: "memory"); __builtin_amdgcn_s_barrier(); asm volatile("" ::: "memory"); }
#define HBAR() bar()
#define ABAR() do { __builtin_amdgcn_sched_barrier(0); bar(); __builtin_amdgcn_sched_barrier(0); } while (0)
#define ABARS(N) do { _Pragma("unroll 1") for (int i_ = 0; i_ < (N); ++i_) ABAR(); } while (0)
DI unsigned pk2(float lo, float hi) { bf16x2 v = {(__bf16)lo, (__bf16)hi}; return __builtin_bit_cast(unsigned, v); }
DI u32x2 pk4(f32x4 v) { return (u32x2){pk2(v[0], v[1]), pk2(v[2], v[3])}; }
DI f32x2 unpk2(unsigned u) { return (f32x2){__uint_as_float(u << 16), __uint_as_float(u & 0xffff0000u)}; }
DI f32x4 unpk4(u32x2 u) { const f32x2 a = unpk2(u[0]), b = unpk2(u[1]); return (f32x4){a[0], a[1], b[0], b[1]}; }
DI bf16x8 cat8(u32x2 a, u32x2 b) { u32x4 v = {a[0], a[1], b[0], b[1]}; return __builtin_bit_cast(bf16x8, v); }
DI f32x4 mfma(bf16x8 a, bf16x8 b, f32x4 c) { return __builtin_amdgcn_mfma_f32_16x16x32_bf16(a, b, c, 0, 0, 0); }
DI bf16x8 lds_a(LAS char* p) { return *(LAS bf16x8*)p; }
DI u32x2 lds_tr(LAS char* p) { return __builtin_bit_cast(u32x2, __builtin_amdgcn_ds_read_tr16_b64_v4i16((LAS s16x4*)p)); }
#undef lds_tr
DI int swap23(int k) { return (k & ~12) | ((k & 4) << 1) | ((k & 8) >> 1); }
DI float sum4(float x) {
  auto r = __builtin_amdgcn_permlane16_swap(__float_as_uint(x), __float_as_uint(x), false, false);
  x = __uint_as_float(r[0]) + __uint_as_float(r[1]);
  auto s = __builtin_amdgcn_permlane32_swap(__float_as_uint(x), __float_as_uint(x), false, false);
  return __uint_as_float(s[0]) + __uint_as_float(s[1]);
}
DI float bsel(unsigned m, float a, float b) { return __uint_as_float((m & __float_as_uint(a)) | (~m & __float_as_uint(b))); }
DI float vmax(float a, float b) { float r; asm("v_max_f32 %0, %1, %2" : "=v"(r) : "v"(a), "v"(b)); return r; }
DI float max4(float x) {
  auto r = __builtin_amdgcn_permlane16_swap(__float_as_uint(x), __float_as_uint(x), false, false);
  x = vmax(__uint_as_float(r[0]), __uint_as_float(r[1]));
  auto s = __builtin_amdgcn_permlane32_swap(__float_as_uint(x), __float_as_uint(x), false, false);
  return vmax(__uint_as_float(s[0]), __uint_as_float(s[1]));
}
DI u32x4 chunk16(u32x2 a, u32x2 b) {
  auto r0 = __builtin_amdgcn_permlane16_swap(a[0], b[0], false, false);
  auto r1 = __builtin_amdgcn_permlane16_swap(a[1], b[1], false, false);
  return (u32x4){r0[0], r1[0], r0[1], r1[1]};
}
DI f32x2 mean_rstd(f32x2 t, float inv_n) {
  float mu = t[0] * inv_n, ex = t[1] * inv_n;
  pin(mu); pin(ex);
  float var = fmaxf(ex - mu * mu, 0.f);
  pin(var);
  return (f32x2){mu, __builtin_amdgcn_rsqf(var + LN_EPS)};
}
DI u64 uniform64(u64 v) {
  return ((u64)(unsigned)__builtin_amdgcn_readfirstlane((int)(unsigned)(v >> 32)) << 32) | (unsigned)__builtin_amdgcn_readfirstlane((int)(unsigned)v);
}
DI int fresh_tid() { int t = threadIdx.x; asm volatile("" : "+v"(t)); return t; }
DI void set_flag(char* ws, unsigned flag_off, u64 tag) { __hip_atomic_store((gu64*)(ws + flag_off), tag, __ATOMIC_RELAXED, __HIP_MEMORY_SCOPE_AGENT); }
DI void await(char* ws, unsigned flag_off, u64 tag) {
#pragma unroll 1
  for (unsigned spin = 0; spin < (1u << 17); ++spin) {
    const u64 f = __hip_atomic_load((gu64*)(ws + flag_off), __ATOMIC_RELAXED, __HIP_MEMORY_SCOPE_AGENT);
    if (__all(f == tag)) break;
    __builtin_amdgcn_s_sleep(POLL_SLEEP);
  }
  asm volatile("" ::: "memory");
}
DI unsigned row_flags(unsigned f, int pm, int lane) { return f + (pm * 16 + (lane & 15)) * 128; }
DI unsigned col_flags(unsigned f, int pn, int lane) { return f + ((lane & 15) * 16 + pn) * 128; }
template <int J0, int J1> DI void gather(u32x4 (&d)[16], i32x4 srd, unsigned off) {
#pragma unroll
  for (int j = J0; j < J1; ++j) ld16_async(d[j], srd, off + j * 1024);
}
template <int J0, int J1> DI void accum(f32x4& t, u32x4 (&d)[16]) {
#pragma unroll
  for (int j = J0; j < J1; ++j) {
    pin(d[j]);
    if (j == 0) t = __builtin_bit_cast(f32x4, d[j]);
    else asm volatile("v_add_f32 %0, %0, %4\n\tv_add_f32 %1, %1, %5\n\tv_add_f32 %2, %2, %6\n\tv_add_f32 %3, %3, %7"
                      : "+v"(t[0]), "+v"(t[1]), "+v"(t[2]), "+v"(t[3]) : "v"(d[j][0]), "v"(d[j][1]), "v"(d[j][2]), "v"(d[j][3]));
  }
}
DI void put_rows(LAS char* lds, unsigned l_mu, unsigned l_rs, int row2, f32x4 t, float inv_n, bool on = true) {
  const f32x2 a = mean_rstd((f32x2){t[0], t[1]}, inv_n), b = mean_rstd((f32x2){t[2], t[3]}, inv_n);
  if (on) { *(LAS f32x2*)(lds + l_mu + row2 * 8) = (f32x2){a[0], b[0]}; *(LAS f32x2*)(lds + l_rs + row2 * 8) = (f32x2){a[1], b[1]}; }
}
DI unsigned await_mask(char* ws, unsigned flag_off, u64 tag, unsigned need) {
  unsigned seen = 0;
#pragma unroll 1
  for (unsigned spin = 0; spin < (1u << 17); ++spin) {
    const u64 f = __hip_atomic_load((gu64*)(ws + flag_off), __ATOMIC_RELAXED, __HIP_MEMORY_SCOPE_AGENT);
    seen = (unsigned)__ballot(f == tag) & 0xffffu;
    if ((seen & need) == need) break;
    __builtin_amdgcn_s_sleep(POLL_SLEEP);
  }
  asm volatile("" ::: "memory");
  return seen;
}

DI int b_pos_lane(int lane) { const int g = lane >> 4, q = (lane & 15) >> 2; return (g >> 1) * 16 + (g & 1) * 4 + q; }
DI unsigned b128_base(int lane, int nb) { const int pos = b_pos_lane(lane); return pos * 128 + ((nb ^ ((pos >> 1) & 3)) * 32) + (lane & 3) * 8; }
DI unsigned b224_base(int lane) { return b_pos_lane(lane) * 224 + (lane & 3) * 8; }
DI unsigned a_base(int lane, int kk) { const int r = lane & 15, g = lane >> 4; return r * 128 + (((kk * 4 + g) ^ ((r >> 1) & 7)) * 16); }
DI unsigned img_off(int row, int chunk) { return row * 128 + ((chunk ^ ((row >> 1) & 7)) * 16); }

#define SGB(mask, n) __builtin_amdgcn_sched_group_barrier(mask, n, 0)
template <int NR, int NM> DI void interleave() {
  constexpr int RP = NR > NM - 2 ? 2 : 1;
  SGB(0x8, 1);
  SGB(0x2, 8);
#pragma unroll
  for (int i = 0; i < (NR + RP - 1) / RP; ++i) { SGB(0x100, RP); SGB(0x8, 1); }
  SGB(0x8, NM);
}
DI void cbar() {
  __builtin_amdgcn_sched_barrier(0);
  __builtin_amdgcn_s_waitcnt(0xC07F);
  bar();
  __builtin_amdgcn_sched_barrier(0);
}
template <int NK, int NS, int SA, int SB, int NR, int NM, class F, class L, class M>
DI void compute_loop(L load, M mma) {
  F f0, f1;
  bar();
  load(f0, 0u, 0u, 0);
  unsigned sa = 0, sb = 0;
  __builtin_amdgcn_sched_barrier(0);
#pragma unroll 1
  for (int t = 0; t < NK - 1; ++t) {
    load(f1, sa, sb, 1);
    mma(f0);
    interleave<NR, NM>();
    cbar();
    sa += SA; sb += SB; if (sa == NS * SA) { sa = 0; sb = 0; }
    load(f0, sa, sb, 0);
    mma(f1);
    interleave<NR, NM>();
    __builtin_amdgcn_sched_barrier(0);
  }
  load(f1, sa, sb, 1);
  mma(f0);
  interleave<NR, NM>();
  cbar();
  mma(f1);
}

template <int N, int NS, int SA, int SB, int NR, int NM, class F, class L, class M>
DI void deep_segment(F& f0, unsigned& sa, unsigned& sb, L load, M mma) {
  F f1;
#pragma unroll 1
  for (int t = 0; t < N; t += 2) {
    cbar();
    sa += SA; sb += SB; if (sa == NS * SA) { sa = 0; sb = 0; }
    load(f1, sa, sb);
    mma(f0);
    interleave<NR, NM>();
    cbar();
    sa += SA; sb += SB; if (sa == NS * SA) { sa = 0; sb = 0; }
    load(f0, sa, sb);
    mma(f1);
    interleave<NR, NM>();
  }
  __builtin_amdgcn_sched_barrier(0);
}

template <int NK_, int NS_, int XP_, int WP_> struct Pipe {
  static constexpr int NK = NK_, NS = NS_, XP = XP_, PW = XP_ + WP_;
  static constexpr int allowed(int t) {
    int n = 0;
    for (int s = t + 2; s <= t + NS - 1 && s < NK; ++s) n += s < NS ? XP : PW;
    return n;
  }
};
template <class P, int T, int EXTRA = 0, class IX, class IW> DI void hstep(IX& ix, IW& iw, int& slot) {
  wait_vm<P::allowed(T) + EXTRA>();
  HBAR();
  if constexpr (T + P::NS < P::NK) { iw(T + P::NS, slot); ix(T + P::NS, slot); }
  slot = slot + 1 == P::NS ? 0 : slot + 1;
}
template <class P, int T0, int T1, class IX, class IW> DI void hsteps(IX& ix, IW& iw, int& slot) {
  if constexpr (T0 < T1) {
    if constexpr (T0 >= P::NS - 2 && T1 <= P::NK - P::NS && T1 - T0 > 1) {
#pragma unroll 1
      for (int t = T0; t < T1; ++t) {
        wait_vm<(P::NS - 2) * P::PW>();
        HBAR();
        iw(t + P::NS, slot); ix(t + P::NS, slot);
        slot = slot + 1 == P::NS ? 0 : slot + 1;
      }
    } else if constexpr (T0 >= P::NS - 2 && T0 + 1 < P::NK - P::NS && T1 > P::NK - P::NS) {
      hsteps<P, T0, P::NK - P::NS>(ix, iw, slot);
      hsteps<P, P::NK - P::NS, T1>(ix, iw, slot);
    } else {
      hstep<P, T0>(ix, iw, slot);
      hsteps<P, T0 + 1, T1>(ix, iw, slot);
    }
  }
}
template <class P, class IX> DI void hstart(IX& ix) {
  ix(0, 0); ix(1, 1);
  wait_vm<P::XP>();
  HBAR();
#pragma unroll
  for (int s = 2; s < P::NS; ++s) ix(s, s);
}
struct Nx { unsigned a0, a1, b, f, la0, la1, lb, lf; };
template <class T> DI void spin(T& v) { asm volatile("" : "+s"(v)); }
template <int T0, int T1, int WAIT, class MK, class G0, class G1, class G2, class G3>
DI void steady(MK mk, G0 g0, G1 g1, G2 g2, G3 g3) {
  static_assert(((T1 - T0) & 1) == 0, "even");
  Nx c = mk(T0 + 4), n;
  auto one = [&](Nx& p, Nx& q, int u) AI {
    wait_vm<WAIT>();
    bar();
    g0(p.a0, p.la0);
    q = mk(u);
    spin(q.a0); spin(q.a1); spin(q.b); spin(q.f); spin(q.la0); spin(q.la1); spin(q.lb); spin(q.lf);
    g1(p.b, p.lb);
    g2(p.a1, p.la1);
    g3(p.f, p.lf);
  };
#pragma unroll 1
  for (int u = T0 + 5; u < T1 + 5; u += 2) { one(c, n, u); one(n, c, u + 1); }
}
DI int rot(int t, int r0, int nk) { const int k = t + r0; return k >= nk ? k - nk : k; }

namespace g1 { constexpr int NK = 16, NS = 3, AB = 144 * 128, ST = AB + 3 * 8192; struct Fr { bf16x8 a[5]; u32x2 b[6][2]; }; }
namespace gs { constexpr int NS = 4, AB = 16384, ST = AB + 8192; struct Fr { bf16x8 a[2][2]; u32x2 b[2][4][2]; }; }
namespace gf { constexpr int NK = 64, NS = 4, AB = 16384, SEG = 14336, B0 = NS * AB; struct Fr { bf16x8 a[2][2]; bf16x8 b[2][7]; }; }

__global__ __launch_bounds__(512, 2) void k_mega(const __bf16* __restrict__ x, const __bf16* __restrict__ wqkv, const __bf16* __restrict__ wo,
                                                 const __bf16* __restrict__ win, const __bf16* __restrict__ wsp, const __bf16* __restrict__ bsp,
                                                 const __bf16* __restrict__ wout, char* __restrict__ out, char* __restrict__ ws) {
  extern __shared__ __attribute__((aligned(16))) char smem_[];
  LAS char* lds = (LAS char*)smem_;
  const int tid = threadIdx.x, lane = tid & 63, wid = __builtin_amdgcn_readfirstlane(tid >> 6);
  const int xcd = blockIdx.x & 7, idx = blockIdx.x >> 3;
  const int pm1 = (idx & 7) * 2 + (xcd & 1), pn1 = (xcd >> 1) * 4 + (idx >> 3);
  const int pm2 = (idx & 3) * 4 + (xcd & 3), pn2 = (idx >> 2) * 2 + (xcd >> 2);
  const i32x4 srdW = make_srd(ws, WS_END);
  TS(0);
  u64 pc0;
  asm volatile("s_getpc_b64 %0" : "=s"(pc0));
  const i32x4 srdC = make_srd((const void*)(pc0 & ~127ull), CODE_BYTES);
  const unsigned code_off = lane < 18 ? (idx * 18 + lane) * 128 : 0xfffffff0u;
  if (CODEPF && wid == 1) { unsigned v; touch(v, srdC, code_off); wait_vm<0>(); pin(v); }

  const bf16x8 ones = __builtin_bit_cast(bf16x8, (u32x4){0x3f803f80u, 0x3f803f80u, 0x3f803f80u, 0x3f803f80u});
  const f32x4 zero4 = {0.f, 0.f, 0.f, 0.f};
  const int r1 = (((xcd >> 1) + (xcd & 1) * 2 + (idx & 1)) & 3) * 4;
  const int srow = idx >> 3, scol = (idx & 7) >> 1;
  auto rowstat = [&](bf16x8 a0, bf16x8 a1) AI {
    const f32x4 sm = mfma(ones, a1, mfma(ones, a0, zero4)), sq = mfma(a1, a1, mfma(a0, a0, zero4));
    return (f32x2){sm[0], (lane & 2) ? ((lane & 1) ? sq[3] : sq[2]) : ((lane & 1) ? sq[1] : sq[0])};
  };
  const bool diag_lane = ((lane & 15) >> 2) == (lane >> 4);
  auto g1_first_steps = [&](int hw, auto between) AI {
    using namespace g1;
#pragma unroll 1
    for (int t = 0; t < 4; ++t) {
      bf16x8 ra[2][2]; u32x2 rb[3][4];
      const unsigned so = (t == 3 ? 0 : t) * ST;
      if (t == srow) {
#pragma unroll
        for (int m = 0; m < 2; ++m)
#pragma unroll
          for (int kk = 0; kk < 2; ++kk) ra[m][kk] = lds_a(lds + so + a_base(lane, kk) + (hw * 2 + m) * 2048);
      }
      if (t == scol) {
#pragma unroll
        for (int i = 0; i < 3; ++i) {
          const int n = hw * 3 + i;
          const unsigned cb = so + AB + (n >> 2) * 8192 + b128_base(lane, n & 3);
#pragma unroll
          for (int j = 0; j < 4; ++j) rb[i][j] = lds_tr(lds + cb + ((j >> 1) * 32 + (j & 1) * 8) * 128);
        }
      }
      wait_lgkm0();
      between(t);
      if (t == srow) {
        const f32x2 r0 = rowstat(ra[0][0], ra[0][1]), r1 = rowstat(ra[1][0], ra[1][1]);
        if (diag_lane) { *(LAS f32x2*)(lds + L_P + (hw * 32 + (lane & 15)) * 8) = r0; *(LAS f32x2*)(lds + L_P + (hw * 32 + 16 + (lane & 15)) * 8) = r1; }
      }
      if (t == scol) {
#pragma unroll
        for (int i = 0; i < 3; ++i)
          *(LAS float*)(lds + L_P + 1024 + ((hw * 3 + i) * 16 + (lane & 15)) * 4) =
              mfma(ones, cat8(rb[i][2], rb[i][3]), mfma(ones, cat8(rb[i][0], rb[i][1]), zero4))[0];
      }
    }
    wait_lgkm0();
  };

  if (wid == 7) {
    const unsigned l16 = lane * 16;
    ABAR();
    const u64 tag = uniform64(*(LAS u64*)(lds + L_TAG));
    const u32x2 tag2 = __builtin_bit_cast(u32x2, tag);
    const unsigned myflag = lane < 16 ? (pm1 * 16 + pn1) * 128 + lane * 8 : 0xff000000u;
    g1_first_steps(3, [&](int) { ABAR(); });
    ABAR();
    st16_dev(srdW, WS_S1R + (pm1 * 16 + ((srow + r1) & 15)) * 1024 + l16, *(LAS u32x4*)(lds + L_P + l16));
    st16_dev(srdW, WS_S1C + (pn1 * 16 + ((scol + r1) & 15)) * 1024 + l16, *(LAS u32x4*)(lds + L_P + 1024 + l16));
    st16_dev(srdW, WS_S3 + (pm1 * 16 + pn1) * 1024 + l16, (u32x4){~0u, ~0u, ~0u, ~0u});
    unsigned tch[4] = {0u, 0u, 0u, 0u};
    u32x4 wt[28];
    const i32x4 srdWin = make_srd(win, DM * FF * 2);
    auto win_half = [&](int h, int b0, int b1) AI {
      int pos = lane / 14, gi = lane - pos * 14;
#pragma unroll
      for (int i = 0; i < 14; ++i) {
        if (i == b0 || i == b1) ABAR();
        ld16_nt(wt[h * 14 + i], srdWin, swap23(pos) * (FF * 2) + (pn1 * 112 + gi * 8) * 2, pm1 * (64u * FF * 2) + h * (FH * 2));
        gi += 8; pos += 4; if (gi >= 14) { gi -= 14; pos += 1; }
      }
    };
    ABARS(2);
    wait_vm<0>();
    st8_dev(srdW, F_S1 + myflag, tag2);
    unsigned tcw[2];
    {
      const i32x4 srdO = make_srd(wout, FH * DM * 2);
#pragma unroll
      for (int i = 0; i < 2; ++i) {
        const int k = (idx & 3) * 128 + i * 64 + lane;
        touch(tcw[i], srdO, k < 448 ? (XROT * (xcd & 3) * 448 + k) * (DM * 2) + pn2 * 128 : 0xfffffff0u);
      }
    }
    touch(tch[3], make_srd(bsp, SEQ * 2), pm1 * 256 + (lane & 1) * 128);
    touch(tch[0], srdW, lane < 16 ? row_flags(F_O, pm2, lane) : lane < 32 ? row_flags(F_H, pm1, lane) : lane < 48 ? col_flags(F_Z2, pn1, lane) : row_flags(F_Y, pm2, lane));
    touch(tch[1], srdW, lane < 16 ? row_flags(F_S1, pm1, lane) : lane < 32 ? col_flags(F_S1, pn1, lane) : col_flags(F_W3, pn1, lane));
    ABARS(2);
    {
      const int pmh = pm1 > 0 ? pm1 - 1 : 0;
      const unsigned fo = lane < 16 ? row_flags(F_S1, pm1, lane) : lane < 32 ? row_flags(F_S1, pmh, lane) : col_flags(F_S1, pn1, lane);
      u32x2 f0;
      ld8_async(f0, srdW, fo);
      ABAR();
      wait_vm<0>();
      pin(f0); pin(tch[0]); pin(tch[1]); pin(tch[2]); pin(tch[3]); pin(tcw[0]); pin(tcw[1]);
      if (!__all(__builtin_bit_cast(u64, f0) == tag)) await(ws, fo, tag);
      const unsigned ro = WS_S1R + pm1 * 16384 + l16, co = lane < 48 ? WS_S1C + pn1 * 16384 + l16 : WS_S1R + pmh * 16384 + (56 + (lane & 7)) * 16;
      u32x4 gr[16], gc[16];
      f32x4 tr, tc;
      gather<0, 12>(gr, srdW, ro);
      ABAR();
      gather<12, 16>(gr, srdW, ro);
      gather<0, 8>(gc, srdW, co);
      ABAR();
      gather<8, 16>(gc, srdW, co);
      wait_vm<16>();
      accum<0, 16>(tr, gr);
      ABAR();
      put_rows(lds, L_MU, L_RS, 8 + lane, tr, 1.f / DM);
      wait_vm<0>();
      accum<0, 8>(tc, gc);
      accum<8, 16>(tc, gc);
      ABAR();
      if (lane < 48) *(LAS f32x4*)(lds + L_CS + l16) = tc;
      put_rows(lds, L_MU, L_RS, lane & 7, tc, 1.f / DM, lane >= 48 && lane < 56);
      wait_lgkm0();
    }
    {
      u32x4 xt[16];
      const i32x4 srdX = make_srd(x, SEQ * DM * 2);
      win_half(0, 6, 14);
      win_half(1, 4, 14);
      ABAR();
#pragma unroll
      for (int i = 0; i < 16; ++i) {
        const int r = i * 8 + (lane >> 3);
        ld16_plain(xt[i], srdX, (lane >> 3) * (DM * 2) + (((lane & 7) ^ ((r >> 1) & 7)) * 16), (pm2 * 128 + i * 8) * (DM * 2) + pn2 * 128);
      }
      wait_vm<16>();
#pragma unroll
      for (int i = 0; i < 28; ++i) { pin(wt[i]); *(LAS u32x4*)(lds + L_WT + i * 1024 + l16) = wt[i]; }
      wait_vm<0>();
#pragma unroll
      for (int i = 0; i < 16; ++i) { pin(xt[i]); *(LAS u32x4*)(lds + L_H + i * 1024 + l16) = xt[i]; }
    }
    ABAR();
    {
      const unsigned cb = L_WT + b224_base(lane);
      bf16x8 fr[2][2][2];
      float cs4[4];
      auto rd2 = [&](int q) AI {
#pragma unroll
        for (int u = 0; u < 2; ++u) {
          const int n = q * 2 + u;
          LAS char* p = lds + cb + (n / 7) * 14336 + (n % 7) * 32;
          fr[q & 1][u][0] = cat8(lds_tr(p), lds_tr(p + 8 * 224));
          fr[q & 1][u][1] = cat8(lds_tr(p + 32 * 224), lds_tr(p + 40 * 224));
        }
      };
      rd2(0);
      wait_lgkm0();
      ABAR();
#pragma unroll
      for (int q = 0; q < 7; ++q) {
        ABAR();
#pragma unroll
        for (int u = 0; u < 2; ++u) {
          const int n = q * 2 + u;
#pragma unroll
          for (int kk = 0; kk < 2; ++kk)
            st16_dev(srdW, WS_W3 + (pn1 * 16 + pm1) * 28672 + (n / 7) * 14336 + (kk * 7 + n % 7) * 1024 + l16, __builtin_bit_cast(u32x4, fr[q & 1][u][kk]));
          cs4[n & 3] = mfma(ones, fr[q & 1][u][1], mfma(ones, fr[q & 1][u][0], zero4))[0];
        }
        if (q < 6) rd2(q + 1);
        if ((q & 1) || q == 6)
          st4_dev(srdW, WS_S2C + (pn1 * 16 + pm1) * 1024 + (q >> 1) * 256 + lane * 4,
                  __float_as_uint(bsel(lane < 32 ? ~0u : 0u, bsel(lane < 16 ? ~0u : 0u, cs4[0], cs4[1]), bsel(lane < 48 ? ~0u : 0u, cs4[2], cs4[3]))));
      }
    }
    ABARS(4);
    wait_vm<0>();
    st8_dev(srdW, F_W3 + myflag, tag2);
    ABARS(5);
    {
      u32x2 f0;
      const unsigned fo = col_flags(F_W3, pn1, lane);
      ld8_async(f0, srdW, fo);
      ABAR();
      wait_vm<0>();
      pin(f0);
      if (!__all(__builtin_bit_cast(u64, f0) == tag)) await(ws, fo, tag);
    }
    u32x4 hpark[16];
    {
      u32x4 gc[16];
      f32x4 tc;
      ABAR();
      gather<0, 16>(gc, srdW, WS_S2C + pn1 * 16384 + l16);
#pragma unroll
      for (int i = 0; i < 16; ++i) hpark[i] = *(LAS u32x4*)(lds + L_H + i * 1024 + l16);
      wait_vm<0>();
      accum<0, 16>(tc, gc);
      if (lane < 56) *(LAS f32x4*)(lds + L_CS + l16) = tc;
    }
    ABARS(6);
    {
      u32x4 gr[16];
      f32x4 tr;
      const unsigned ro = WS_S2R + pm1 * 16384 + l16;
      ABAR();  gather<0, 4>(gr, srdW, ro);
      ABAR();  gather<4, 8>(gr, srdW, ro);
      ABAR();  gather<8, 12>(gr, srdW, ro);
      ABAR();  gather<12, 16>(gr, srdW, ro); wait_vm<8>(); accum<0, 8>(tr, gr);
      ABARS(2);
      wait_vm<0>(); accum<8, 16>(tr, gr);
      ABAR();
      put_rows(lds, L_MU, L_RS, lane, tr, 1.f / DM);
      wait_lgkm0();
    }
    ABARS(5);
    {
      const i32x4 srdSp = make_srd(wsp, SEQ * SEQ * 2);
#pragma unroll
      for (int i = 0; i < 2; ++i) touch(tch[i], srdSp, (pm1 * 128 + i * 64 + lane) * (SEQ * 2) + (XROT * (xcd >> 1) * 8 + 4 + (idx >> 3)) * 128);
    }
    dma16<0>(make_srd(bsp, SEQ * 2), lane < 16 ? pm1 * 256 + l16 : 0xfffffff0u, 0u, L_C2);
    ABARS(2);
    {
      u32x4 gv[16];
      f32x4 t;
      unsigned mx = 0;
      const unsigned so = WS_S3 + pm1 * 16384 + l16;
#pragma unroll
      for (int j = 0; j < 16; ++j) asm volatile("buffer_load_dwordx4 %0, %1, %2, %3 offen sc1" : "=v"(gv[j]) : "v"(so), "s"(srdW), "s"(j * 1024) : "memory");
      ABAR();
      wait_vm<8>();
      pin(tch[0]); pin(tch[1]);
      accum<0, 8>(t, gv);
#pragma unroll
      for (int j = 0; j < 8; ++j) mx = max(max(mx, gv[j][1]), gv[j][3]);
      pin(mx);
      ABAR();
      wait_vm<0>();
      accum<8, 16>(t, gv);
#pragma unroll
      for (int j = 8; j < 16; ++j) mx = max(max(mx, gv[j][1]), gv[j][3]);
      if (!__all(mx != ~0u)) {
#pragma unroll 1
        for (unsigned spin = 0; spin < (1u << 17); ++spin) {
          u32x4 gw[16];
          gather<0, 16>(gw, srdW, so);
          wait_vm<0>();
          accum<0, 16>(t, gw);
          mx = 0;
#pragma unroll
          for (int j = 0; j < 16; ++j) mx = max(max(mx, gw[j][1]), gw[j][3]);
          if (__all(mx != ~0u)) break;
        }
      }
      ABAR();
      put_rows(lds, L_M3, L_R3, lane, t, 1.f / FH);
      wait_lgkm0();
    }
    ABARS(6);
    st8_dev(srdW, F_Z2 + myflag, tag2);
    ABARS(2);
    {
      u32x2 f0;
      const unsigned fo = col_flags(F_Z2, pn1, lane);
      ld8_async(f0, srdW, fo);
      ABAR();
      wait_vm<0>();
      pin(f0);
      const bool up = __all(__builtin_bit_cast(u64, f0) == tag);
      *(LAS unsigned*)(lds + L_TAG + 8) = up;
      wait_lgkm0();
      ABAR();
      if (!up) await(ws, fo, tag);
      ABAR();
    }
#pragma unroll
    for (int i = 0; i < 16; ++i) *(LAS u32x4*)(lds + L_H + i * 1024 + l16) = hpark[i];
    wait_lgkm0();
    ABARS(64);
    return;
  }

  if (wid >= 4) {
    const int dw = wid - 4;
    int slot;
    const unsigned l16 = lane * 16;
    u64 tag;
    unsigned seen = 0;
    auto gate = [&](unsigned fo, unsigned need) AI { if (dw == 0 && (seen & need) != need) seen = await_mask(ws, fo, tag, need); };
    auto first_steps = [&](auto PC, auto& ix, auto& iw, unsigned fo, auto mask) AI {
      using P = decltype(PC);
      seen = 0;
      gate(fo, mask(0) | mask(1) | mask(2) | mask(3));
      HBAR();
      hstart<P>(ix);
      slot = 0;
      gate(fo, mask(4)); hstep<P, 0>(ix, iw, slot);
      gate(fo, mask(5)); hstep<P, 1>(ix, iw, slot);
      gate(fo, mask(6)); hstep<P, 2>(ix, iw, slot);
      gate(fo, mask(7)); hstep<P, 3>(ix, iw, slot);
      gate(fo, 0xffffu);
    };
    {
      using namespace g1;
      using P = Pipe<NK, NS, 14, 0>;
      const i32x4 srdA = make_srd(x, SEQ * DM * 2), srdB = make_srd(wqkv, DM * 3 * DM * 2);
      unsigned va[6], vb[8];
#pragma unroll
      for (int i = 0; i < 8; ++i) {
        const int p = dw * 8 + i, pos = (p & 7) * 8 + (lane >> 3), nb = ((lane & 7) >> 1) ^ ((pos >> 1) & 3);
        vb[i] = swap23(pos) * (3 * DM * 2) + (dw * DM + pn1 * 64 + nb * 16 + (lane & 1) * 8) * 2 - (i & 3) * 1024;
      }
#pragma unroll
      for (int i = 0; i < 6; ++i) {
        const int r = (dw * 6 + i) * 8 + (lane >> 3);
        int grow = r < 128 ? pm1 * 128 + r : pm1 * 128 - 16 + (r - 128);
        grow = grow < 0 ? 8 : grow;
        va[i] = grow * (DM * 2) + (((lane & 7) ^ ((r >> 1) & 7)) * 16) - (i & 3) * 1024;
      }
      auto ia = [&](int t, int s) {
        const unsigned kt = (t + r1) & 15;
        dmaS<4>(srdA, va, kt * 128u, s * ST + dw * 6144u);
        dmaS<2>(srdA, va + 4, kt * 128u, s * ST + dw * 6144u + 4096u);
      };
      auto ib = [&](int t, int s) {
        const unsigned kt = (t + r1) & 15;
        dmaS<4>(srdB, vb, kt * (64u * 3 * DM * 2), s * ST + AB + dw * 8192u);
        dmaS<4>(srdB, vb + 4, kt * (64u * 3 * DM * 2), s * ST + AB + dw * 8192u + 4096u);
      };
      ib(0, 0); ia(0, 0); ib(1, 1);
      wait_vm<8>();
      HBAR();
      ia(1, 1); ia(2, 2); ib(2, 2);
      tag = uniform64(*(LAS u64*)(lds + L_TAG));
      TS(14);
      slot = 0;
      g1_first_steps(dw, [&](int t) {
        wait_vm<P::PW>();
        HBAR();
        ia(t + NS, slot); ib(t + NS, slot);
        slot = slot + 1 == NS ? 0 : slot + 1;
      });
      TS(15);
      hsteps<P, 4, NK>(ia, ib, slot);
    }
    TS(1);
    auto lin16 = [&](unsigned src, unsigned dst) AI {
      const unsigned g0 = dw * 4096u, g1 = 12288u + dw * 1024u;
      dmaL<4>(srdW, l16, src + g0, dst + g0);
      if (dw == 2) dmaL<2>(srdW, l16, src + g1, dst + g1);
      else dmaL<1>(srdW, l16, src + g1, dst + g1);
    };
    unsigned vbs[3];
#pragma unroll
    for (int i = 0; i < 3; ++i) {
      const int pos = (i < 2 ? dw * 2 + i : (6 + dw) & 7) * 8 + (lane >> 3), nb = ((lane & 7) >> 1) ^ ((pos >> 1) & 3);
      vbs[i] = swap23(pos) * (DM * 2) + (pn2 * 64 + nb * 16 + (lane & 1) * 8) * 2 - (i == 1 ? 1024 : 0);
    }
    auto w8 = [&](i32x4 srd, unsigned kt, int s) AI {
      dmaS<2>(srd, vbs, kt * (64u * DM * 2), s * gs::ST + gs::AB + dw * 2048u);
      if (dw != 2) dmaS<1>(srd, vbs + 2, kt * (64u * DM * 2), s * gs::ST + gs::AB + (6 + dw) * 1024u);
    };
    const unsigned vflex = dw == 2 ? l16 : vbs[2];
    auto gs_steady = [&](auto T1, i32x4 srdB, unsigned abase, int r0, int nk) AI {
      i32x4 srdF;
#pragma unroll
      for (int e = 0; e < 4; ++e) srdF[e] = dw == 2 ? srdW[e] : srdB[e];
      steady<4, decltype(T1)::value, 16>(
          [&](int u) AI {
            const unsigned kt = rot(u, r0, nk), a = abase + kt * 16384u, b = kt * (64u * DM * 2), l = (u & 3) * gs::ST;
            return Nx{a + dw * 4096u, a + 12288u + dw * 1024u, b, dw == 2 ? a + 15360u : b,
                      l + dw * 4096u, l + 12288u + dw * 1024u, l + gs::AB + dw * 2048u, l + (dw == 2 ? 15360u : gs::AB + (6 + dw) * 1024u)};
          },
          [&](unsigned so, unsigned l) AI { dmaL<4>(srdW, l16, so, l); }, [&](unsigned so, unsigned l) AI { dmaS<2>(srdB, vbs, so, l); },
          [&](unsigned so, unsigned l) AI { dmaL<1>(srdW, l16, so, l); }, [&](unsigned so, unsigned l) AI { dmaL<1>(srdF, vflex, so, l); });
    };
    {
      using namespace gs;
      constexpr int NK = 16;
      using P = Pipe<NK, NS, 5, 3>;
      const i32x4 srdB = make_srd(wo, DM * DM * 2);
      const int r0 = XROT * (xcd & 3) * 4;
      auto ix = [&](int t, int s) { lin16(WS_O + (pm2 * 16 + ((t + r0) & 15)) * 16384u, s * ST); };
      auto iw = [&](int t, int s) { w8(srdB, (t + r0) & 15, s); };
      HBAR();
      HBAR();
      iw(0, 0); iw(1, 1); iw(2, 2); iw(3, 3);
      TS(2);
      first_steps(P{}, ix, iw, row_flags(F_O, pm2, lane), [&](int u) { return 1u << ((u + r0) & 15); });
      TS(3);
      gs_steady(IC<NK - NS>{}, srdB, WS_O + pm2 * 16 * 16384u, r0, NK);
      hsteps<P, NK - NS, NK>(ix, iw, slot);
    }
    TS(4);
    {
      using namespace gf;
      using P = Pipe<NK, NS, 5, 5>;
      const i32x4 srdSp = make_srd(wsp, SEQ * SEQ * 2);
      auto lin14 = [&](unsigned src, int s) AI {
        const unsigned dst = B0 + s * SEG;
        dmaL<4>(srdW, l16, src + dw * 4096u, dst + dw * 4096u);
        if (dw != 2) dmaL<1>(srdW, l16, src + (12 + dw) * 1024u, dst + (12 + dw) * 1024u);
      };
      auto ia3 = [&](int t, int s) { lin16(WS_H + (pm1 * 16 + (t & 15)) * 16384u, s * AB); };
      auto ib3 = [&](int t, int s) { lin14(WS_W3 + (pn1 * 16 + (t & 15)) * 28672u + (t < 16 ? 14336u : 0u), s); };
      unsigned va4[6];
#pragma unroll
      for (int i = 0; i < 6; ++i) { const int r = (i < 4 ? dw * 4 + i : 8 + dw + i) * 8 + (lane >> 3); va4[i] = (pm1 * 128 + r) * (SEQ * 2) + (((lane & 7) ^ ((r >> 1) & 7)) * 16) - (i & 3) * 1024; }
      const int r4 = XROT * (xcd >> 1) * 8;
      auto ia4 = [&](int t, int s) {
        const unsigned kt = (t + r4) & 31;
        dmaS<4>(srdSp, va4, kt * 128u, s * AB + dw * 4096u);
        if (dw == 2) dmaS<2>(srdSp, va4 + 4, kt * 128u, s * AB + 14 * 1024u);
        else dmaS<1>(srdSp, va4 + 4, kt * 128u, s * AB + (12 + dw) * 1024u);
      };
      auto ib4 = [&](int t, int s) { lin14(WS_Z2 + (pn1 * 32 + ((t + r4) & 31)) * (unsigned)SEG, s); };
      HBAR();
      ib3(0, 0); ib3(1, 1); ib3(2, 2); ib3(3, 3);
      TS(5);
      first_steps(P{}, ia3, ib3, row_flags(F_H, pm1, lane), [&](int u) { return 1u << u; });
      TS(6);
      auto L4 = [&](unsigned so, unsigned l) AI { dmaL<4>(srdW, l16, so, l); };
      auto L1 = [&](unsigned so, unsigned l) AI { dmaL<1>(srdW, l16, so, l); };
      steady<4, 28, 20>(
          [&](int u) AI {
            const unsigned a = WS_H + (pm1 * 16 + (u & 15)) * 16384u, b = WS_W3 + (pn1 * 16 + (u & 15)) * 28672u + (u < 16 ? 14336u : 0u);
            const unsigned la = (u & 3) * AB, lb = B0 + (u & 3) * SEG;
            return Nx{a + dw * 4096u, a + 12288u + dw * 1024u, b + dw * 4096u, dw == 2 ? a + 15360u : b + 12288u + dw * 1024u,
                      la + dw * 4096u, la + 12288u + dw * 1024u, lb + dw * 4096u, dw == 2 ? la + 15360u : lb + 12288u + dw * 1024u};
          },
          L4, L4, L1, L1);
      TS(9);
      wait_vm<20>(); HBAR(); ia4(32, 0);
      wait_vm<15>(); HBAR(); ia4(33, 1);
      wait_vm<10>(); HBAR();
      const bool early = __builtin_amdgcn_readfirstlane(*(LAS unsigned*)(lds + L_TAG + 8)) != 0;
      if (early) { ib4(32, 0); ib4(33, 1); }
      ia4(34, 2);
      HBAR();
      if (!early) { ib4(32, 0); ib4(33, 1); }
      ib4(34, 2); ia4(35, 3); ib4(35, 3);
      wait_vm<18>(); HBAR();
      wait_vm<14>(); HBAR(); ib4(36, 0); ia4(36, 0);
      wait_vm<20>(); HBAR(); ib4(37, 1); ia4(37, 1);
      i32x4 srdF;
#pragma unroll
      for (int e = 0; e < 4; ++e) srdF[e] = dw == 2 ? srdSp[e] : srdW[e];
      const unsigned vf4 = dw == 2 ? va4[5] + 1024u : l16;
      steady<34, 60, 20>(
          [&](int u) AI {
            const unsigned kt = (u + r4) & 31, a = kt * 128u, b = WS_Z2 + (pn1 * 32 + kt) * (unsigned)SEG;
            const unsigned la = (u & 3) * AB, lb = B0 + (u & 3) * SEG;
            return Nx{a, a, b + dw * 4096u, dw == 2 ? a : b + 12288u + dw * 1024u,
                      la + dw * 4096u, la + 12288u + dw * 1024u, lb + dw * 4096u, dw == 2 ? la + 15360u : lb + 12288u + dw * 1024u};
          },
          [&](unsigned so, unsigned l) AI { dmaS<4>(srdSp, va4, so, l); }, L4,
          [&](unsigned so, unsigned l) AI { dmaS<1>(srdSp, va4 + 4, so, l); }, [&](unsigned so, unsigned l) AI { dmaL<1>(srdF, vf4, so, l); });
      hsteps<P, 60, NK>(ia4, ib4, slot);
    }
    TS(10);
    {
      using namespace gs;
      constexpr int NK = 28;
      using P = Pipe<NK, NS, 5, 3>;
      const i32x4 srdB = make_srd(wout, FH * DM * 2);
      const int r0 = XROT * (xcd & 3) * 7;
      auto ix = [&](int t, int s) { lin16(WS_Y + (pm2 * 28 + rot(t, r0, NK)) * 16384u, s * ST); };
      auto iw = [&](int t, int s) { w8(srdB, rot(t, r0, NK), s); };
      iw(0, 0); iw(1, 1);
      HBAR();
      iw(2, 2); iw(3, 3);
      TS(11);
      first_steps(P{}, ix, iw, row_flags(F_Y, pm2, lane), [&](int u) { const int k = rot(u, r0, NK); return (1u << (k * 8 / 14)) | (1u << ((k * 8 + 7) / 14)); });
      TS(12);
      gs_steady(IC<NK - NS>{}, srdB, WS_Y + pm2 * 28 * 16384u, r0, NK);
      hsteps<P, NK - NS, NK>(ix, iw, slot);
    }
    TS(13);
    return;
  }

  const u64 tag = uniform64(__hip_atomic_load((gu64*)(ws + WS_EPOCH), __ATOMIC_RELAXED, __HIP_MEMORY_SCOPE_AGENT)) + 1;
  if (tid == 0) *(LAS u64*)(lds + L_TAG) = tag;
  wait_lgkm0();
#define LANES const int tid = fresh_tid(), lane = tid & 63, i16 = lane & 15, g = lane >> 4; (void)i16; (void)g
  auto publish = [&](unsigned flag) {
    wait_vm<0>();
    bar();
    if (threadIdx.x < 16) set_flag(ws, flag + threadIdx.x * 8, tag);
    bar();
  };
  {
    using namespace g1;
    LANES;
    const int hd = wid & 1, rh = wid >> 1;
    unsigned ab[2], hb[2], bb[2];
#pragma unroll
    for (int kk = 0; kk < 2; ++kk) { ab[kk] = a_base(lane, kk) + rh * 64 * 128; hb[kk] = a_base(lane, kk) + 128 * 128; bb[kk] = AB + b128_base(lane, hd * 2 + kk); }
    f32x4 aq[4][2], ak[4][2], av[4][2], ah[2];
#pragma unroll
    for (int m = 0; m < 4; ++m)
#pragma unroll
      for (int j = 0; j < 2; ++j) aq[m][j] = ak[m][j] = av[m][j] = zero4;
    ah[0] = ah[1] = zero4;
    auto load = [&](Fr& f, unsigned so, unsigned, int kk) {
#pragma unroll
      for (int m = 0; m < 4; ++m) f.a[m] = lds_a(lds + so + ab[kk] + m * 2048);
      f.a[4] = lds_a(lds + so + hb[kk]);
#pragma unroll
      for (int s = 0; s < 3; ++s)
#pragma unroll
        for (int j = 0; j < 2; ++j)
#pragma unroll
          for (int hh = 0; hh < 2; ++hh) f.b[s * 2 + j][hh] = lds_tr(lds + so + bb[j] + s * 8192 + (kk * 32 + hh * 8) * 128);
    };
    auto mma2 = [&](Fr& f, auto RH) {
      bf16x8 b[6];
#pragma unroll
      for (int i = 0; i < 6; ++i) b[i] = cat8(f.b[i][0], f.b[i][1]);
#pragma unroll
      for (int m = 0; m < 4; ++m)
#pragma unroll
        for (int j = 0; j < 2; ++j) {
          aq[m][j] = mfma(b[j], f.a[m], aq[m][j]);
          ak[m][j] = mfma(b[2 + j], f.a[m], ak[m][j]);
          av[m][j] = mfma(f.a[m], b[4 + j], av[m][j]);
        }
#pragma unroll
      for (int j = 0; j < 2; ++j) {
        if constexpr (decltype(RH)::value == 0) ah[j] = mfma(b[2 + j], f.a[4], ah[j]);
        else ah[j] = mfma(f.a[4], b[4 + j], ah[j]);
      }
    };
    const int ra = i16 & 1;
    const bool curA = i16 - 4 * g - ra >= 0, curB = i16 - 4 * g - ra - 2 >= 0;
    LAS char* t16 = lds + L_MU + (16 + rh * 64 + i16) * 4;
    LAS char* t4g = lds + L_MU + (16 + rh * 64 + 4 * g) * 4;
    LAS char* tka = lds + L_RS + (rh * 64 + 4 * g + ra + (curA ? 16 : 0)) * 4;
    LAS char* tkb = lds + L_RS + (rh * 64 + 4 * g + ra + 2 + (curB ? 16 : 0)) * 4;
    LAS char* xch = lds + L_X + hd * 3072 + lane * 16;
    unsigned oaddr = WS_O + (pm1 * 16 + pn1) * 16384 + (hd * 4 + (g >> 1)) * 2048 + (rh * 64 + (g & 1) * 16 + i16) * 16;
    unsigned mA = curA ? ~0u : 0u, mB = curB ? ~0u : 0u;
    pin(t16); pin(t4g); pin(tka); pin(tkb); pin(xch); pin(oaddr); pin(mA); pin(mB);
    if (rh == 0) compute_loop<NK, NS, ST, ST, 17, 26, Fr>(load, [&](Fr& f) { mma2(f, IC<0>{}); });
    else compute_loop<NK, NS, ST, ST, 17, 26, Fr>(load, [&](Fr& f) { mma2(f, IC<1>{}); });
    TS(1);
    f32x4 csq[2], csk[2]; float csv[2];
    const int co = L_CS - L_MU + (hd * 32 - 16 - rh * 64) * 4;
#pragma unroll
    for (int j = 0; j < 2; ++j) {
      csq[j] = *(LAS f32x4*)(t4g + co + j * 64);
      csk[j] = *(LAS f32x4*)(t4g + co + 256 + j * 64);
      csv[j] = *(LAS float*)(t16 + co + 512 + j * 64);
    }
    bf16x8 Q[4], K[5]; u32x2 V[5][2];
    float rq[4], rka[4], rkb[4];
#pragma unroll
    for (int m = 3; m >= 0; --m) {
      const float mu = *(LAS float*)(t16 + m * 64);
      const f32x4 mu4 = *(LAS f32x4*)(t4g + m * 64);
      rq[m] = *(LAS float*)(t16 + (L_RS - L_MU) + m * 64);
      rka[m] = *(LAS float*)(tka + m * 64);
      rkb[m] = *(LAS float*)(tkb + m * 64);
      K[m + 1] = cat8(pk4(ak[m][0] - mu * csk[0]), pk4(ak[m][1] - mu * csk[1]));
      V[m + 1][0] = pk4(av[m][0] - mu4 * csv[0]);
      V[m + 1][1] = pk4(av[m][1] - mu4 * csv[1]);
      Q[m] = cat8(pk4(aq[m][0] - mu * csq[0]), pk4(aq[m][1] - mu * csq[1]));
    }
    if (rh == 0) {
      const float mu = *(LAS float*)(t16 - 64);
      K[0] = cat8(pk4(ah[0] - mu * csk[0]), pk4(ah[1] - mu * csk[1]));
      *(LAS bf16x8*)(xch) = K[4];
      *(LAS bf16x8*)(xch + 1024) = cat8(V[4][0], V[4][1]);
    } else {
      const f32x4 mu4 = *(LAS f32x4*)(t4g - 320);
      *(LAS bf16x8*)(xch + 2048) = cat8(pk4(ah[0] - mu4 * csv[0]), pk4(ah[1] - mu4 * csv[1]));
      K[0] = K[4];
    }
    wait_lgkm0(); bar();
    {
      u32x4 v = *(LAS u32x4*)(xch + (rh == 0 ? 2048 : 1024));
      V[0][0] = (u32x2){v[0], v[1]}; V[0][1] = (u32x2){v[2], v[3]};
      if (rh == 1) K[0] = *(LAS bf16x8*)(xch);
    }
    constexpr float SC = 0.17677669529663687f * 1.4426950408889634f;
    f32x4 sp[4], sc[4];
#pragma unroll
    for (int m = 3; m >= 0; --m) { sc[m] = mfma(K[m + 1], Q[m], zero4); sp[m] = mfma(K[m], Q[m], zero4); }
    const bool nohalo = pm1 == 0 && rh == 0;
    const unsigned selA = ra ? 0x01000c0cu : 0x0c0c0100u, selB = ra ? 0x03020c0cu : 0x0c0c0302u;
    const unsigned mR = ra ? ~0u : 0u;
    float la[4], lb[4], mx[4];
#pragma unroll
    for (int m = 0; m < 4; ++m) {
      const float a = bsel(mA, bsel(mR, sc[m][1], sc[m][0]), bsel(mR, sp[m][1], sp[m][0]));
      const float b = bsel(mB, bsel(mR, sc[m][3], sc[m][2]), bsel(mR, sp[m][3], sp[m][2]));
      const float f = SC * rq[m];
      la[m] = a * (f * rka[m]); lb[m] = b * (f * rkb[m]);
    }
    la[0] = bsel(nohalo ? mA : ~0u, la[0], -INFINITY); lb[0] = bsel(nohalo ? mB : ~0u, lb[0], -INFINITY);
#pragma unroll
    for (int m = 0; m < 4; ++m) mx[m] = max4(vmax(la[m], lb[m]));
    f32x4 o[4][2];
#pragma unroll
    for (int m = 0; m < 4; ++m) { la[m] = __builtin_amdgcn_exp2f(la[m] - mx[m]); lb[m] = __builtin_amdgcn_exp2f(lb[m] - mx[m]); }
#pragma unroll
    for (int m = 0; m < 4; ++m) mx[m] = __builtin_amdgcn_rcpf(sum4(la[m] + lb[m]));
#pragma unroll
    for (int m = 0; m < 4; ++m) {
      const unsigned w = pk2(la[m] * (mx[m] * rka[m]), lb[m] * (mx[m] * rkb[m]));
      const unsigned wa = __builtin_amdgcn_perm(0u, w, selA), wb = __builtin_amdgcn_perm(0u, w, selB);
      const bf16x8 P = __builtin_bit_cast(bf16x8, (u32x4){wa & ~mA, wb & ~mB, wa & mA, wb & mB});
#pragma unroll
      for (int db = 0; db < 2; ++db) o[m][db] = mfma(cat8(V[m][db], V[m + 1][db]), P, zero4);
    }
    TS(2);
#pragma unroll
    for (int p = 0; p < 2; ++p)
#pragma unroll
      for (int db = 0; db < 2; ++db)
        st16_dev(srdW, oaddr + db * 4096 + p * 512, chunk16(pk4(o[2 * p][db]), pk4(o[2 * p + 1][db])));
    TS(3);
    publish(F_O + (pm1 * 16 + pn1) * 128);
  }
  auto small_gemm = [&](auto NKC, u32x2 (&res)[2][4]) AI {
    using namespace gs;
    LANES;
    unsigned ab[2], bb[4];
#pragma unroll
    for (int kk = 0; kk < 2; ++kk) ab[kk] = (kk * 4 + g) * 2048 + (wid * 32 + i16) * 16;
#pragma unroll
    for (int j = 0; j < 4; ++j) bb[j] = AB + b128_base(lane, j);
    f32x4 acc[2][4];
#pragma unroll
    for (int m = 0; m < 2; ++m)
#pragma unroll
      for (int j = 0; j < 4; ++j) acc[m][j] = unpk4(*(LAS u32x2*)(lds + L_H + img_off(wid * 32 + m * 16 + i16, j * 2 + (g >> 1)) + (g & 1) * 8));
    auto load = [&](Fr& f, unsigned so, unsigned) {
#pragma unroll
      for (int kk = 0; kk < 2; ++kk) {
#pragma unroll
        for (int m = 0; m < 2; ++m) f.a[kk][m] = lds_a(lds + so + ab[kk] + m * 256);
#pragma unroll
        for (int j = 0; j < 4; ++j)
#pragma unroll
          for (int hh = 0; hh < 2; ++hh) f.b[kk][j][hh] = lds_tr(lds + so + bb[j] + (kk * 32 + hh * 8) * 128);
      }
    };
    auto mma = [&](Fr& f) {
#pragma unroll
      for (int kk = 0; kk < 2; ++kk)
#pragma unroll
        for (int m = 0; m < 2; ++m)
#pragma unroll
          for (int j = 0; j < 4; ++j) acc[m][j] = mfma(cat8(f.b[kk][j][0], f.b[kk][j][1]), f.a[kk][m], acc[m][j]);
    };
    Fr f0;
    unsigned sa = 0, sb = 0;
    bar();
    load(f0, 0u, 0u);
    deep_segment<decltype(NKC)::value, NS, ST, ST, 20, 16>(f0, sa, sb, load, mma);
#pragma unroll
    for (int m = 0; m < 2; ++m)
#pragma unroll
      for (int j = 0; j < 4; ++j) res[m][j] = pk4(acc[m][j]);
  };
  {
    u32x2 h[2][4];
    small_gemm(IC<16>{}, h);
    TS(5);
    LANES;
#pragma unroll
    for (int j = 0; j < 4; ++j)
      st16_dev(srdW, WS_H + (pm2 * 16 + pn2) * 16384 + (j * 2 + (g >> 1)) * 2048 + (wid * 32 + (g & 1) * 16 + i16) * 16, chunk16(h[0][j], h[1][j]));
    const unsigned o = diag_lane ? WS_S2R + (pm2 * 16 + pn2) * 1024 + (wid * 32 + i16) * 8 : 0xffff0000u;
#pragma unroll
    for (int m = 0; m < 2; ++m) st8_dev(srdW, o + m * 128, __builtin_bit_cast(u32x2, rowstat(cat8(h[m][0], h[m][1]), cat8(h[m][2], h[m][3]))));
#pragma unroll
    for (int m = 0; m < 2; ++m)
#pragma unroll
      for (int j = 0; j < 4; ++j) *(LAS u32x2*)(lds + L_H + img_off(wid * 32 + m * 16 + i16, j * 2 + (g >> 1)) + (g & 1) * 8) = h[m][j];
    wait_lgkm0();
    TS(6);
    publish(F_H + (pm2 * 16 + pn2) * 128);
  }
  {
    using namespace gf;
    LANES;
    unsigned ab[2];
#pragma unroll
    for (int kk = 0; kk < 2; ++kk) ab[kk] = (kk * 4 + g) * 2048 + (wid * 32 + i16) * 16;
    const unsigned bb = B0 + lane * 16;
    f32x4 acc[2][7];
    LAS char *pa0 = lds + ab[0], *pa1 = lds + ab[1], *pb = lds + bb;
    unsigned sa = 0, sb = 0;
    auto rd = [&](Fr& f, int r, int ms) AI {
      const int kk = r / 9, q = r % 9;
      if (q < 2) f.a[kk][q] = lds_a((kk ? pa1 : pa0) + q * ms);
      else f.b[kk][q - 2] = lds_a(pb + (kk * 7 + q - 2) * 1024);
    };
    auto advance = [&]() AI {
      sa += AB; sb += SEG; if (sa == NS * AB) { sa = 0; sb = 0; }
      pa0 = lds + (sa + ab[0]); pa1 = lds + (sa + ab[1]); pb = lds + (sb + bb);
    };
    auto tile = [&](Fr& c, Fr& n, int ms, auto hook) AI {
      cbar();
#pragma unroll
      for (int i = 0; i < 28; ++i) {
        const int kk = i / 14, m = (i % 14) / 7, j = i % 7;
        asm volatile("v_mfma_f32_16x16x32_bf16 %0, %1, %2, %0" : "+v"(acc[m][j]) : "v"(c.b[kk][j]), "v"(c.a[kk][m]));
        if (i < 18) { rd(n, i, ms); __builtin_amdgcn_sched_barrier(0); }
        hook(i);
        if (i == 24) { sa += AB; sb += SEG; if (sa == NS * AB) { sa = 0; sb = 0; } spin(sa); spin(sb); }
        if (i == 25) { pa0 = lds + (sa + ab[0]); pin(pa0); }
        if (i == 26) { pa1 = lds + (sa + ab[1]); pin(pa1); }
        if (i == 27) { pb = lds + (sb + bb); pin(pb); }
        __builtin_amdgcn_sched_barrier(0);
      }
    };
    auto nohook = [](int) AI {};
    Fr f0;
    auto settle = [&]() AI {
      asm volatile("s_nop 7\n\ts_nop 1" ::: "memory");
      __builtin_amdgcn_sched_barrier(0);
    };
    auto segment = [&](int n, int ms) AI {
      Fr f1;
#pragma unroll 1
      for (int t = 0; t < n; t += 2) { tile(f0, f1, ms, nohook); tile(f1, f0, ms, nohook); }
    };
    u32x2 zz[2][7];
    float mu[2], c1[2], c2[2], ir[2];
    auto rowconst = [&]() AI {
#pragma unroll
      for (int m = 0; m < 2; ++m) {
        mu[m] = -*(LAS float*)(lds + L_MU + (wid * 32 + m * 16 + i16) * 4);
        const float rs = *(LAS float*)(lds + L_RS + (wid * 32 + m * 16 + i16) * 4);
        c1[m] = -2.302208198f * rs; c2[m] = -0.1029432397f * rs * rs * rs; ir[m] = __builtin_amdgcn_rcpf(rs);
      }
    };
    auto act = [&]() AI {
      constexpr bool centre = true;
#pragma unroll
      for (int j0 = 0; j0 < 7; j0 += 2) {
        f32x4 e[4], cs[2];
        const int n = j0 < 6 ? 4 : 2;
        if (centre) {
          cs[0] = *(LAS f32x4*)(lds + L_CS + (112 + j0 * 16 + 4 * g) * 4);
          cs[1] = *(LAS f32x4*)(lds + L_CS + (112 + (j0 < 6 ? j0 + 1 : j0) * 16 + 4 * g) * 4);
#pragma unroll
          for (int u = 0; u < 4; ++u) if (u < n) acc[u & 1][j0 + (u >> 1)] += mu[u & 1] * cs[u >> 1];
        }
#pragma unroll
        for (int u = 0; u < 4; ++u) if (u < n) e[u] = acc[u & 1][j0 + (u >> 1)] * acc[u & 1][j0 + (u >> 1)];
#pragma unroll
        for (int u = 0; u < 4; ++u) if (u < n) e[u] = e[u] * c2[u & 1] + c1[u & 1];
#pragma unroll
        for (int u = 0; u < 4; ++u) if (u < n) e[u] = e[u] * acc[u & 1][j0 + (u >> 1)];
#pragma unroll
        for (int u = 0; u < 4; ++u) if (u < n)
#pragma unroll
          for (int i = 0; i < 4; ++i) e[u][i] = __builtin_amdgcn_exp2f(e[u][i]);
#pragma unroll
        for (int u = 0; u < 4; ++u) if (u < n) e[u] = e[u] * ir[u & 1] + ir[u & 1];
#pragma unroll
        for (int u = 0; u < 4; ++u) if (u < n)
#pragma unroll
          for (int i = 0; i < 4; ++i) e[u][i] = __builtin_amdgcn_rcpf(e[u][i]);
#pragma unroll
        for (int u = 0; u < 4; ++u) if (u < n) { zz[u & 1][j0 + (u >> 1)] = pk4(acc[u & 1][j0 + (u >> 1)] * e[u]); pin(zz[u & 1][j0 + (u >> 1)]); }
        __builtin_amdgcn_sched_barrier(0);
      }
    };
#pragma unroll
    for (int m = 0; m < 2; ++m)
#pragma unroll
      for (int j = 0; j < 7; ++j) acc[m][j] = zero4;
    bar();
#pragma unroll
    for (int r = 0; r < 18; ++r) rd(f0, r, 256);
    advance();
    segment(16, 256);
    settle();
    TS(7);
    rowconst();
    act();
    {
      f32x2 st[2];
#pragma unroll
      for (int m = 0; m < 2; ++m)
        st[m] = rowstat(cat8(zz[m][0], zz[m][1]), cat8(zz[m][2], zz[m][3])) + rowstat(cat8(zz[m][4], zz[m][5]), cat8(zz[m][6], (u32x2){0u, 0u}));
      const unsigned o = diag_lane ? WS_S3 + (pm1 * 16 + pn1) * 1024 + (wid * 32 + i16) * 8 : 0xffff0000u;
#pragma unroll
      for (int m = 0; m < 2; ++m) st8_dev(srdW, o + m * 128, (u32x2){__float_as_uint(st[m][0]), min(__float_as_uint(st[m][1]), ~1u)});
    }
#pragma unroll
    for (int j = 0; j < 7; ++j) {
      const f32x4 cs = *(LAS f32x4*)(lds + L_CS + (j * 16 + 4 * g) * 4);
      acc[0][j] = mu[0] * cs; acc[1][j] = mu[1] * cs;
    }
    asm volatile("s_nop 1" ::: "memory");
    TS(8);
    Fr f1;
    segment(6, 256);
    TS(9);
#pragma unroll
    for (int m = 0; m < 2; ++m) {
      const float r3 = *(LAS float*)(lds + L_R3 + (wid * 32 + m * 16 + i16) * 4), m3 = -r3 * *(LAS float*)(lds + L_M3 + (wid * 32 + m * 16 + i16) * 4);
#pragma unroll
      for (int j = 0; j < 7; ++j)
        *(LAS u32x2*)(lds + L_ZS + (wid * 32 + m * 16 + swap23(i16)) * 232 + 8 * g + j * 32) = pk4(unpk4(zz[m][j]) * r3 + m3);
    }
    wait_lgkm0();
    {
      LAS char* p = lds + L_ZS + (wid * 32 + b_pos_lane(lane)) * 232 + (lane & 3) * 8;
      u32x2 lo[7], hi[7];
#pragma unroll
      for (int j = 0; j < 7; ++j) { lo[j] = lds_tr(p + j * 32); hi[j] = lds_tr(p + j * 32 + 8 * 232); }
#pragma unroll
      for (int j = 0; j < 7; ++j)
        st16_dev(srdW, WS_Z2 + (pn1 * 32 + pm1 * 2) * SEG + (wid * 7 + j) * 1024 + lane * 16, (u32x4){lo[j][0], lo[j][1], hi[j][0], hi[j][1]});
    }
    TS(10);
#pragma unroll 1
    for (int t = 22; t < 32; t += 2) {
      if (t == 26) wait_vm<0>();
      tile(f0, f1, 256, nohook); tile(f1, f0, 256, nohook);
    }
    settle();
    TS(11);
    rowconst();
    u32x32 zv;
#pragma unroll
    for (int m = 0; m < 2; ++m) {
      const float b = (float)*(LAS __bf16*)(lds + L_C2 + (wid * 32 + m * 16 + i16) * 2);
#pragma unroll
      for (int j = 0; j < 7; ++j) { const u32x2 p = pk4(acc[m][j]); zv[4 * j + m] = p[0]; zv[4 * j + 2 + m] = p[1]; acc[m][j] = zero4 + b; }
    }
    TS(12);
    bar();
    ab[0] = a_base(lane, 0) + wid * 32 * 128; ab[1] = a_base(lane, 1) + wid * 32 * 128;
    pa0 = lds + ab[0]; pa1 = lds + ab[1]; pb = lds + bb; sa = 0; sb = 0;
#pragma unroll
    for (int r = 0; r < 18; ++r) rd(f0, r, 2048);
    advance();
    {
      unsigned w = 0;
      float u0, u1, e0, e1;
      auto gel = [&](int m, int i) AI {
        if (i == 0) asm volatile("v_lshlrev_b32 %0, 16, %1" : "=v"(u0) : "v"(w));
        if (i == 1) asm volatile("v_and_b32 %0, 0xffff0000, %1" : "=v"(u1) : "v"(w));
        if (i == 2) asm volatile("v_mul_f32 %0, %1, %1" : "=v"(e0) : "v"(u0));
        if (i == 3) asm volatile("v_mul_f32 %0, %1, %1" : "=v"(e1) : "v"(u1));
        if (i == 4) asm volatile("v_fma_f32 %0, %0, %1, %2" : "+v"(e0) : "v"(c2[m]), "v"(c1[m]));
        if (i == 5) asm volatile("v_fma_f32 %0, %0, %1, %2" : "+v"(e1) : "v"(c2[m]), "v"(c1[m]));
        if (i == 6) asm volatile("v_mul_f32 %0, %0, %1" : "+v"(e0) : "v"(u0));
        if (i == 7) asm volatile("v_mul_f32 %0, %0, %1" : "+v"(e1) : "v"(u1));
        if (i == 8) asm volatile("v_exp_f32 %0, %0" : "+v"(e0));
        if (i == 9) asm volatile("v_exp_f32 %0, %0" : "+v"(e1));
        if (i == 10) asm volatile("v_fma_f32 %0, %0, %1, %1" : "+v"(e0) : "v"(ir[m]));
        if (i == 11) asm volatile("v_fma_f32 %0, %0, %1, %1" : "+v"(e1) : "v"(ir[m]));
        if (i == 12) asm volatile("v_rcp_f32 %0, %0" : "+v"(e0));
        if (i == 13) asm volatile("v_rcp_f32 %0, %0" : "+v"(e1));
        if (i == 14) asm volatile("v_mul_f32 %0, %0, %1" : "+v"(e0) : "v"(u0));
        if (i == 15) asm volatile("v_mul_f32 %0, %0, %1" : "+v"(e1) : "v"(u1));
        if (i == 16) asm volatile("v_cvt_pk_bf16_f32 %0, %1, %2" : "=v"(w) : "v"(e0), "v"(e1));

      };
      w = zv[0];
#pragma unroll 1
      for (int it = 0; it < 28; it += 2) {
        tile(f0, f1, 2048, [&](int i) AI { gel(0, i); if (i == 18) { zv[it] = w; pin(zv); } if (i == 20) { w = zv[it + 1]; pin(w); } });
        tile(f1, f0, 2048, [&](int i) AI { gel(1, i); if (i == 18) { zv[it + 1] = w; pin(zv); } if (i == 20) { w = zv[it + 2]; pin(w); } });
      }
    }
    segment(4, 2048);
    settle();
    TS(13);
#pragma unroll
    for (int j = 0; j < 7; ++j)
      st16_dev(srdW, WS_Y + (pm1 * 16 + pn1) * 28672 + (j * 2 + (g >> 1)) * 2048 + (wid * 32 + (g & 1) * 16 + i16) * 16,
               chunk16(pk4(unpk4((u32x2){zv[4 * j], zv[4 * j + 2]}) * acc[0][j]), pk4(unpk4((u32x2){zv[4 * j + 1], zv[4 * j + 3]}) * acc[1][j])));
    TS(15);
    publish(F_Y + (pm1 * 16 + pn1) * 128);
  }
  {
    u32x2 r[2][4];
    small_gemm(IC<28>{}, r);
    TS(16);
    LANES;
#pragma unroll
    for (int m = 0; m < 2; ++m)
#pragma unroll
      for (int p = 0; p < 2; ++p)
        st16_dev(make_srd(out, SEQ * DM * 2), (pm2 * 128 + wid * 32 + m * 16 + i16) * (DM * 2) + pn2 * 128 + (p * 4 + (g & 1) * 2 + (g >> 1)) * 16, chunk16(r[m][2 * p], r[m][2 * p + 1]));
  }
  if (blockIdx.x == 0 && tid == 0) __hip_atomic_store((gu64*)(ws + WS_EPOCH), tag, __ATOMIC_RELAXED, __HIP_MEMORY_SCOPE_AGENT);
  TS(17);
}

extern "C" void kernel_launch(void* const* d_in, const int* in_sizes, int n_in, void* d_out, int out_size, void* d_ws, size_t ws_size,
                              hipStream_t stream) {
  (void)hipFuncSetAttribute((const void*)k_mega, hipFuncAttributeMaxDynamicSharedMemorySize, LDS_BYTES);
  k_mega<<<256, 512, LDS_BYTES, stream>>>((const __bf16*)d_in[0], (const __bf16*)d_in[1], (const __bf16*)d_in[2], (const __bf16*)d_in[4],
                                          (const __bf16*)d_in[5], (const __bf16*)d_in[6], (const __bf16*)d_in[7], (char*)d_out, (char*)d_ws + (size_t)WSBASE * 1048576);
}
```

```cpp
#include <hip/hip_runtime.h>
#include <hip/hip_bf16.h>
#include <stdint.h>
#include <type_traits>

#define DI __device__ __forceinline__
#define LAS __attribute__((address_space(3)))
#define AI __attribute__((always_inline))
typedef __bf16 bf16x8 __attribute__((ext_vector_type(8)));
typedef __bf16 bf16x2 __attribute__((ext_vector_type(2)));
typedef short s16x4 __attribute__((ext_vector_type(4)));
typedef float f32x4 __attribute__((ext_vector_type(4)));
typedef float f32x2 __attribute__((ext_vector_type(2)));
typedef int i32x4 __attribute__((ext_vector_type(4)));
typedef unsigned u32x4 __attribute__((ext_vector_type(4)));
typedef unsigned u32x2 __attribute__((ext_vector_type(2)));
typedef unsigned u32x32 __attribute__((ext_vector_type(32)));
typedef unsigned long long u64;
typedef __attribute__((address_space(1))) u64 gu64;
typedef __attribute__((address_space(1))) unsigned gu32;
template <int V> using IC = std::integral_constant<int, V>;

#define POLL_SLEEP 1
#define CODEPF 1
#define FLAGPF 1
#define WEARLY 0
#define W2POS 2
#define AUXLATE 0
#define WOTOUCH 0
#define WB0 0
#define WB1 0
#define XROT 1
#define WSBASE 0

#define CODE_BYTES 66560
constexpr int SEQ = 2048, DM = 1024, FF = 3584, FH = 1792;
constexpr float LN_EPS = 1e-5f;

constexpr unsigned WS_EPOCH = 0;
constexpr unsigned F_S1 = 32768, F_O = 2 * 32768, F_H = 3 * 32768, F_W3 = 4 * 32768, F_S3 = 5 * 32768, F_Z2 = 6 * 32768, F_Y = 7 * 32768;
constexpr unsigned WS_S1R = 1u << 18;
constexpr unsigned WS_S1C = WS_S1R + (1u << 18);
constexpr unsigned WS_S2R = WS_S1C + (1u << 18);
constexpr unsigned WS_S2C = WS_S2R + (1u << 18);
constexpr unsigned WS_S3 = WS_S2C + (1u << 18);
constexpr unsigned WS_O = 2u << 20;
constexpr unsigned WS_H = WS_O + (4u << 20);
constexpr unsigned WS_Z2 = WS_H + (4u << 20);
constexpr unsigned WS_Y = WS_Z2 + (8u << 20);
constexpr unsigned WS_W3 = WS_Y + (8u << 20);
constexpr unsigned WS_END = WS_W3 + (8u << 20);
constexpr size_t WS_TS = 64u << 20;
#define TS(i) do {} while (0)

constexpr unsigned L_WT = 98304;
constexpr unsigned L_X = 129024;
constexpr unsigned L_H = 135168;
constexpr unsigned L_MISC = 153600;
constexpr unsigned L_MU = L_MISC, L_RS = L_MU + 576, L_CS = L_RS + 576, L_P = L_CS + 896, L_M3 = L_P + 2048, L_R3 = L_M3 + 512, L_TAG = L_R3 + 512,
                   L_C2 = L_TAG + 16;
constexpr unsigned LDS_BYTES = 163840;
static_assert(L_C2 + 2 * 896 <= LDS_BYTES, "lds");

DI i32x4 make_srd(const void* p, unsigned bytes) {
  u64 a = (u64)p;
  i32x4 r;
  r[0] = __builtin_amdgcn_readfirstlane((int)(unsigned)a);
  r[1] = __builtin_amdgcn_readfirstlane((int)(unsigned)(a >> 32));
  r[2] = (int)bytes;
  r[3] = 0x00020000;
  return r;
}
template <int POL> DI void dma16(i32x4 srd, unsigned voff, unsigned soff, unsigned ldsaddr) {
  if constexpr (POL == 2)
    asm volatile("s_mov_b32 m0, %0\n\ts_nop 0\n\tbuffer_load_dwordx4 %1, %2, %3 offen nt lds" :: "s"(ldsaddr), "v"(voff), "s"(srd), "s"(soff) : "memory");
  else if constexpr (POL == 1)
    asm volatile("s_mov_b32 m0, %0\n\ts_nop 0\n\tbuffer_load_dwordx4 %1, %2, %3 offen sc1 lds" :: "s"(ldsaddr), "v"(voff), "s"(srd), "s"(soff) : "memory");
  else
    asm volatile("s_mov_b32 m0, %0\n\ts_nop 0\n\tbuffer_load_dwordx4 %1, %2, %3 offen lds" :: "s"(ldsaddr), "v"(voff), "s"(srd), "s"(soff) : "memory");
}
template <int N> DI void dmaL(i32x4 srd, unsigned v, unsigned so, unsigned l) {
  if constexpr (N == 1) asm volatile("s_mov_b32 m0, %0\n\ts_nop 0\n\tbuffer_load_dwordx4 %1, %2, %3 offen sc1 lds" :: "s"(l), "v"(v), "s"(srd), "s"(so) : "memory");
  if constexpr (N == 2) asm volatile("s_mov_b32 m0, %0\n\ts_nop 0\n\tbuffer_load_dwordx4 %1, %2, %3 offen sc1 lds\n\tbuffer_load_dwordx4 %1, %2, %3 offen offset:1024 sc1 lds" :: "s"(l), "v"(v), "s"(srd), "s"(so) : "memory");
  if constexpr (N == 4) asm volatile("s_mov_b32 m0, %0\n\ts_nop 0\n\tbuffer_load_dwordx4 %1, %2, %3 offen sc1 lds\n\tbuffer_load_dwordx4 %1, %2, %3 offen offset:1024 sc1 lds\n\t"
                                     "buffer_load_dwordx4 %1, %2, %3 offen offset:2048 sc1 lds\n\tbuffer_load_dwordx4 %1, %2, %3 offen offset:3072 sc1 lds" :: "s"(l), "v"(v), "s"(srd), "s"(so) : "memory");
}
template <int N> DI void dmaS(i32x4 srd, const unsigned* v, unsigned so, unsigned l) {
  if constexpr (N == 1) asm volatile("s_mov_b32 m0, %0\n\ts_nop 0\n\tbuffer_load_dwordx4 %1, %2, %3 offen lds" :: "s"(l), "v"(v[0]), "s"(srd), "s"(so) : "memory");
  if constexpr (N == 2) asm volatile("s_mov_b32 m0, %0\n\ts_nop 0\n\tbuffer_load_dwordx4 %1, %2, %3 offen lds\n\tbuffer_load_dwordx4 %4, %2, %3 offen offset:1024 lds" :: "s"(l), "v"(v[0]), "s"(srd), "s"(so), "v"(v[1]) : "memory");
  if constexpr (N == 4) asm volatile("s_mov_b32 m0, %0\n\ts_nop 0\n\tbuffer_load_dwordx4 %1, %2, %3 offen lds\n\tbuffer_load_dwordx4 %4, %2, %3 offen offset:1024 lds\n\t"
                                     "buffer_load_dwordx4 %5, %2, %3 offen offset:2048 lds\n\tbuffer_load_dwordx4 %6, %2, %3 offen offset:3072 lds"
                                     :: "s"(l), "v"(v[0]), "s"(srd), "s"(so), "v"(v[1]), "v"(v[2]), "v"(v[3]) : "memory");
}
DI void ld16_async(u32x4& d, i32x4 srd, unsigned voff) { asm volatile("buffer_load_dwordx4 %0, %1, %2, 0 offen sc1" : "=v"(d) : "v"(voff), "s"(srd) : "memory"); }
DI void ld8_async(u32x2& d, i32x4 srd, unsigned voff) { asm volatile("buffer_load_dwordx2 %0, %1, %2, 0 offen sc1" : "=v"(d) : "v"(voff), "s"(srd) : "memory"); }
DI void touch(unsigned& d, i32x4 srd, unsigned voff) { asm volatile("buffer_load_dword %0, %1, %2, 0 offen" : "=v"(d) : "v"(voff), "s"(srd) : "memory"); }
template <int J0, int J1> DI void gather(u32x4 (&d)[16], i32x4 srd, unsigned off);
DI void ld16_nt(u32x4& d, i32x4 srd, unsigned voff, unsigned soff) { asm volatile("buffer_load_dwordx4 %0, %1, %2, %3 offen nt" : "=v"(d) : "v"(voff), "s"(srd), "s"(soff) : "memory"); }
DI void ld16_plain(u32x4& d, i32x4 srd, unsigned voff, unsigned soff) { asm volatile("buffer_load_dwordx4 %0, %1, %2, %3 offen" : "=v"(d) : "v"(voff), "s"(srd), "s"(soff) : "memory"); }
template <class T> DI void pin(T& v) { asm volatile("" : "+v"(v)); }
DI void st16_dev(i32x4 srd, unsigned voff, u32x4 v) { asm volatile("buffer_store_dwordx4 %0, %1, %2, 0 offen sc1\n\ts_nop 1" :: "v"(v), "v"(voff), "s"(srd) : "memory"); }
DI void st8_dev(i32x4 srd, unsigned voff, u32x2 v) { asm volatile("buffer_store_dwordx2 %0, %1, %2, 0 offen sc1" :: "v"(v), "v"(voff), "s"(srd) : "memory"); }
DI void st4_dev(i32x4 srd, unsigned voff, unsigned v) { asm volatile("buffer_store_dword %0, %1, %2, 0 offen sc1" :: "v"(v), "v"(voff), "s"(srd) : "memory"); }
template <int N> DI void wait_vm() { asm volatile("s_waitcnt vmcnt(%0)" :: "i"(N) : "memory"); }
DI void wait_lgkm0() { __builtin_amdgcn_s_waitcnt(0xC07F); asm volatile("" ::: "memory"); }
DI void bar() { asm volatile("" ::: "memory"); __builtin_amdgcn_s_barrier(); asm volatile("" ::: "memory"); }
#define HBAR() bar()
#define ABAR() do { __builtin_amdgcn_sched_barrier(0); bar(); __builtin_amdgcn_sched_barrier(0); } while (0)
#define ABARS(N) do { _Pragma("unroll 1") for (int i_ = 0; i_ < (N); ++i_) ABAR(); } while (0)
DI unsigned pk2(float lo, float hi) { bf16x2 v = {(__bf16)lo, (__bf16)hi}; return __builtin_bit_cast(unsigned, v); }
DI u32x2 pk4(f32x4 v) { return (u32x2){pk2(v[0], v[1]), pk2(v[2], v[3])}; }
DI f32x2 unpk2(unsigned u) { return (f32x2){__uint_as_float(u << 16), __uint_as_float(u & 0xffff0000u)}; }
DI f32x4 unpk4(u32x2 u) { const f32x2 a = unpk2(u[0]), b = unpk2(u[1]); return (f32x4){a[0], a[1], b[0], b[1]}; }
DI bf16x8 cat8(u32x2 a, u32x2 b) { u32x4 v = {a[0], a[1], b[0], b[1]}; return __builtin_bit_cast(bf16x8, v); }
DI f32x4 mfma(bf16x8 a, bf16x8 b, f32x4 c) { return __builtin_amdgcn_mfma_f32_16x16x32_bf16(a, b, c, 0, 0, 0); }
DI bf16x8 lds_a(LAS char* p) { return *(LAS bf16x8*)p; }
DI u32x2 lds_tr(LAS char* p) { return __builtin_bit_cast(u32x2, __builtin_amdgcn_ds_read_tr16_b64_v4i16((LAS s16x4*)p)); }
#undef lds_tr
DI int swap23(int k) { return (k & ~12) | ((k & 4) << 1) | ((k & 8) >> 1); }
DI float sum4(float x) {
  auto r = __builtin_amdgcn_permlane16_swap(__float_as_uint(x), __float_as_uint(x), false, false);
  x = __uint_as_float(r[0]) + __uint_as_float(r[1]);
  auto s = __builtin_amdgcn_permlane32_swap(__float_as_uint(x), __float_as_uint(x), false, false);
  return __uint_as_float(s[0]) + __uint_as_float(s[1]);
}
DI float bsel(unsigned m, float a, float b) { return __uint_as_float((m & __float_as_uint(a)) | (~m & __float_as_uint(b))); }
DI float vmax(float a, float b) { float r; asm("v_max_f32 %0, %1, %2" : "=v"(r) : "v"(a), "v"(b)); return r; }
DI float max4(float x) {
  auto r = __builtin_amdgcn_permlane16_swap(__float_as_uint(x), __float_as_uint(x), false, false);
  x = vmax(__uint_as_float(r[0]), __uint_as_float(r[1]));
  auto s = __builtin_amdgcn_permlane32_swap(__float_as_uint(x), __float_as_uint(x), false, false);
  return vmax(__uint_as_float(s[0]), __uint_as_float(s[1]));
}
DI u32x4 chunk16(u32x2 a, u32x2 b) {
  auto r0 = __builtin_amdgcn_permlane16_swap(a[0], b[0], false, false);
  auto r1 = __builtin_amdgcn_permlane16_swap(a[1], b[1], false, false);
  return (u32x4){r0[0], r1[0], r0[1], r1[1]};
}
DI f32x2 mean_rstd(f32x2 t, float inv_n) {
  float mu = t[0] * inv_n, ex = t[1] * inv_n;
  pin(mu); pin(ex);
  float var = fmaxf(ex - mu * mu, 0.f);
  pin(var);
  return (f32x2){mu, __builtin_amdgcn_rsqf(var + LN_EPS)};
}
DI u64 uniform64(u64 v) {
  return ((u64)(unsigned)__builtin_amdgcn_readfirstlane((int)(unsigned)(v >> 32)) << 32) | (unsigned)__builtin_amdgcn_readfirstlane((int)(unsigned)v);
}
DI int fresh_tid() { int t = threadIdx.x; asm volatile("" : "+v"(t)); return t; }
DI void set_flag(char* ws, unsigned flag_off, u64 tag) { __hip_atomic_store((gu64*)(ws + flag_off), tag, __ATOMIC_RELAXED, __HIP_MEMORY_SCOPE_AGENT); }
DI void await(char* ws, unsigned flag_off, u64 tag) {
#pragma unroll 1
  for (unsigned spin = 0; spin < (1u << 17); ++spin) {
    const u64 f = __hip_atomic_load((gu64*)(ws + flag_off), __ATOMIC_RELAXED, __HIP_MEMORY_SCOPE_AGENT);
    if (__all(f == tag)) break;
    __builtin_amdgcn_s_sleep(POLL_SLEEP);
  }
  asm volatile("" ::: "memory");
}
DI unsigned row_flags(unsigned f, int pm, int lane) { return f + (pm * 16 + (lane & 15)) * 128; }
DI unsigned col_flags(unsigned f, int pn, int lane) { return f + ((lane & 15) * 16 + pn) * 128; }
template <int J0, int J1> DI void gather(u32x4 (&d)[16], i32x4 srd, unsigned off) {
#pragma unroll
  for (int j = J0; j < J1; ++j) ld16_async(d[j], srd, off + j * 1024);
}
template <int J0, int J1> DI void accum(f32x4& t, u32x4 (&d)[16]) {
#pragma unroll
  for (int j = J0; j < J1; ++j) {
    pin(d[j]);
    if (j == 0) t = __builtin_bit_cast(f32x4, d[j]);
    else asm volatile("v_add_f32 %0, %0, %4\n\tv_add_f32 %1, %1, %5\n\tv_add_f32 %2, %2, %6\n\tv_add_f32 %3, %3, %7"
                      : "+v"(t[0]), "+v"(t[1]), "+v"(t[2]), "+v"(t[3]) : "v"(d[j][0]), "v"(d[j][1]), "v"(d[j][2]), "v"(d[j][3]));
  }
}
DI void put_rows(LAS char* lds, unsigned l_mu, unsigned l_rs, int row2, f32x4 t, float inv_n, bool on = true) {
  const f32x2 a = mean_rstd((f32x2){t[0], t[1]}, inv_n), b = mean_rstd((f32x2){t[2], t[3]}, inv_n);
  if (on) { *(LAS f32x2*)(lds + l_mu + row2 * 8) = (f32x2){a[0], b[0]}; *(LAS f32x2*)(lds + l_rs + row2 * 8) = (f32x2){a[1], b[1]}; }
}
DI unsigned await_mask(char* ws, unsigned flag_off, u64 tag, unsigned need) {
  unsigned seen = 0;
#pragma unroll 1
  for (unsigned spin = 0; spin < (1u << 17); ++spin) {
    const u64 f = __hip_atomic_load((gu64*)(ws + flag_off), __ATOMIC_RELAXED, __HIP_MEMORY_SCOPE_AGENT);
    seen = (unsigned)__ballot(f == tag) & 0xffffu;
    if ((seen & need) == need) break;
    __builtin_amdgcn_s_sleep(POLL_SLEEP);
  }
  asm volatile("" ::: "memory");
  return seen;
}

DI int b_pos_lane(int lane) { const int g = lane >> 4, q = (lane & 15) >> 2; return (g >> 1) * 16 + (g & 1) * 4 + q; }
DI unsigned b128_base(int lane, int nb) { const int pos = b_pos_lane(lane); return pos * 128 + ((nb ^ ((pos >> 1) & 3)) * 32) + (lane & 3) * 8; }
DI unsigned b224_base(int lane) { return b_pos_lane(lane) * 224 + (lane & 3) * 8; }
DI unsigned a_base(int lane, int kk) { const int r = lane & 15, g = lane >> 4; return r * 128 + (((kk * 4 + g) ^ ((r >> 1) & 7)) * 16); }
DI unsigned img_off(int row, int chunk) { return row * 128 + ((chunk ^ ((row >> 1) & 7)) * 16); }

#define SGB(mask, n) __builtin_amdgcn_sched_group_barrier(mask, n, 0)
template <int NR, int NM> DI void interleave() {
  constexpr int RP = NR > NM - 2 ? 2 : 1;
  SGB(0x8, 1);
  SGB(0x2, 8);
#pragma unroll
  for (int i = 0; i < (NR + RP - 1) / RP; ++i) { SGB(0x100, RP); SGB(0x8, 1); }
  SGB(0x8, NM);
}
DI void cbar() {
  __builtin_amdgcn_sched_barrier(0);
  __builtin_amdgcn_s_waitcnt(0xC07F);
  bar();
  __builtin_amdgcn_sched_barrier(0);
}
template <int NK, int NS, int SA, int SB, int NR, int NM, class F, class L, class M>
DI void compute_loop(L load, M mma) {
  F f0, f1;
  bar();
  load(f0, 0u, 0u, 0);
  unsigned sa = 0, sb = 0;
  __builtin_amdgcn_sched_barrier(0);
#pragma unroll 1
  for (int t = 0; t < NK - 1; ++t) {
    load(f1, sa, sb, 1);
    mma(f0);
    interleave<NR, NM>();
    cbar();
    sa += SA; sb += SB; if (sa == NS * SA) { sa = 0; sb = 0; }
    load(f0, sa, sb, 0);
    mma(f1);
    interleave<NR, NM>();
    __builtin_amdgcn_sched_barrier(0);
  }
  load(f1, sa, sb, 1);
  mma(f0);
  interleave<NR, NM>();
  cbar();
  mma(f1);
}

template <int N, int NS, int SA, int SB, int NR, int NM, class F, class L, class M>
DI void deep_segment(F& f0, unsigned& sa, unsigned& sb, L load, M mma) {
  F f1;
#pragma unroll 1
  for (int t = 0; t < N; t += 2) {
    cbar();
    sa += SA; sb += SB; if (sa == NS * SA) { sa = 0; sb = 0; }
    load(f1, sa, sb);
    mma(f0);
    interleave<NR, NM>();
    cbar();
    sa += SA; sb += SB; if (sa == NS * SA) { sa = 0; sb = 0; }
    load(f0, sa, sb);
    mma(f1);
    interleave<NR, NM>();
  }
  __builtin_amdgcn_sched_barrier(0);
}

template <int NK_, int NS_, int XP_, int WP_> struct Pipe {
  static constexpr int NK = NK_, NS = NS_, XP = XP_, PW = XP_ + WP_;
  static constexpr int allowed(int t) {
    int n = 0;
    for (int s = t + 2; s <= t + NS - 1 && s < NK; ++s) n += s < NS ? XP : PW;
    return n;
  }
};
template <class P, int T, int EXTRA = 0, class IX, class IW> DI void hstep(IX& ix, IW& iw, int& slot) {
  wait_vm<P::allowed(T) + EXTRA>();
  HBAR();
  if constexpr (T + P::NS < P::NK) { iw(T + P::NS, slot); ix(T + P::NS, slot); }
  slot = slot + 1 == P::NS ? 0 : slot + 1;
}
template <class P, int T0, int T1, class IX, class IW> DI void hsteps(IX& ix, IW& iw, int& slot) {
  if constexpr (T0 < T1) {
    if constexpr (T0 >= P::NS - 2 && T1 <= P::NK - P::NS && T1 - T0 > 1) {
#pragma unroll 1
      for (int t = T0; t < T1; ++t) {
        wait_vm<(P::NS - 2) * P::PW>();
        HBAR();
        iw(t + P::NS, slot); ix(t + P::NS, slot);
        slot = slot + 1 == P::NS ? 0 : slot + 1;
      }
    } else if constexpr (T0 >= P::NS - 2 && T0 + 1 < P::NK - P::NS && T1 > P::NK - P::NS) {
      hsteps<P, T0, P::NK - P::NS>(ix, iw, slot);
      hsteps<P, P::NK - P::NS, T1>(ix, iw, slot);
    } else {
      hstep<P, T0>(ix, iw, slot);
      hsteps<P, T0 + 1, T1>(ix, iw, slot);
    }
  }
}
template <class P, class IX> DI void hstart(IX& ix) {
  ix(0, 0); ix(1, 1);
  wait_vm<P::XP>();
  HBAR();
#pragma unroll
  for (int s = 2; s < P::NS; ++s) ix(s, s);
}
struct Nx { unsigned a0, a1, b, f, la0, la1, lb, lf; };
template <class T> DI void spin(T& v) { asm volatile("" : "+s"(v)); }
template <int T0, int T1, int WAIT, int EARLY, class ST, class MK, class G0, class G1, class G2, class G3, class GT>
DI void steady(ST start, MK mk, G0 g0, G1 g1, G2 g2, G3 g3, GT gt) {
  static_assert(((T1 - T0) & 1) == 0, "even");
  auto keep = [](Nx& q) AI { spin(q.a0); spin(q.a1); spin(q.b); spin(q.f); spin(q.la0); spin(q.la1); spin(q.lb); spin(q.lf); };
  Nx c = mk(T0 + 4), n;
  keep(c);
  start();
  auto head = [&](auto W, Nx& p, Nx& q, int u) AI {
    wait_vm<decltype(W)::value>();
    bar();
    g0(p.a0, p.la0);
    q = mk(u);
    keep(q);
    gt(u);
  };
  auto rest = [&](Nx& p) AI { g1(p.b, p.lb); g2(p.a1, p.la1); g3(p.f, p.lf); };
  head(IC<EARLY>{}, c, n, T0 + 5); rest(c);
  head(IC<EARLY>{}, n, c, T0 + 6); rest(n);
  head(IC<WAIT>{}, c, n, T0 + 7);
#pragma unroll 1
  for (int u = T0 + 8; u < T1 + 4; u += 2) { rest(c); head(IC<WAIT>{}, n, c, u); rest(n); head(IC<WAIT>{}, c, n, u + 1); }
  rest(c); head(IC<WAIT>{}, n, c, T1 + 4); rest(n);
}
DI int rot(int t, int r0, int nk) { const int k = t + r0; return k >= nk ? k - nk : k; }

namespace g1 { constexpr int NK = 16, NS = 3, AB = 144 * 128, ST = AB + 3 * 8192; struct Fr { bf16x8 a[5]; u32x2 b[6][2]; }; }
namespace gs { constexpr int NS = 4, AB = 16384, ST = AB + 8192; struct Fr { bf16x8 a[2][2]; u32x2 b[2][4][2]; }; }
namespace gf { constexpr int NK = 64, NS = 4, AB = 16384, SEG = 14336, B0 = NS * AB; struct Fr { bf16x8 a[2][2]; bf16x8 b[2][7]; }; }

__global__ __launch_bounds__(512, 2) void k_mega(const __bf16* __restrict__ x, const __bf16* __restrict__ wqkv, const __bf16* __restrict__ wo,
                                                 const __bf16* __restrict__ win, const __bf16* __restrict__ wsp, const __bf16* __restrict__ bsp,
                                                 const __bf16* __restrict__ wout, char* __restrict__ out, char* __restrict__ ws) {
  extern __shared__ __attribute__((aligned(16))) char smem_[];
  LAS char* lds = (LAS char*)smem_;
  const int tid = threadIdx.x, lane = tid & 63, wid = __builtin_amdgcn_readfirstlane(tid >> 6);
  const int xcd = blockIdx.x & 7, idx = blockIdx.x >> 3;
  const int pm1 = (idx & 7) * 2 + (xcd & 1), pn1 = (xcd >> 1) * 4 + (idx >> 3);
  const int pm2 = (idx & 3) * 4 + (xcd & 3), pn2 = (idx >> 2) * 2 + (xcd >> 2);
  const i32x4 srdW = make_srd(ws, WS_END);
  TS(0);
  u64 pc0;
  asm volatile("s_getpc_b64 %0" : "=s"(pc0));
  const i32x4 srdC = make_srd((const void*)(pc0 & ~127ull), CODE_BYTES);
  const unsigned code_off = lane < 18 ? (idx * 18 + lane) * 128 : 0xfffffff0u;
  if (CODEPF && wid == 1) { unsigned v; touch(v, srdC, code_off); wait_vm<0>(); pin(v); }

  const bf16x8 ones = __builtin_bit_cast(bf16x8, (u32x4){0x3f803f80u, 0x3f803f80u, 0x3f803f80u, 0x3f803f80u});
  const f32x4 zero4 = {0.f, 0.f, 0.f, 0.f};
  const int r1 = (((xcd >> 1) + (xcd & 1) * 2 + (idx & 1)) & 3) * 4;
  const int srow = idx >> 3, scol = (idx & 7) >> 1;
  auto rowstat = [&](bf16x8 a0, bf16x8 a1) AI {
    const f32x4 sm = mfma(ones, a1, mfma(ones, a0, zero4)), sq = mfma(a1, a1, mfma(a0, a0, zero4));
    return (f32x2){sm[0], (lane & 2) ? ((lane & 1) ? sq[3] : sq[2]) : ((lane & 1) ? sq[1] : sq[0])};
  };
  const bool diag_lane = ((lane & 15) >> 2) == (lane >> 4);
  auto g1_first_steps = [&](int hw, auto between) AI {
    using namespace g1;
#pragma unroll 1
    for (int t = 0; t < 4; ++t) {
      bf16x8 ra[2][2]; u32x2 rb[3][4];
      const unsigned so = (t == 3 ? 0 : t) * ST;
      if (t == srow) {
#pragma unroll
        for (int m = 0; m < 2; ++m)
#pragma unroll
          for (int kk = 0; kk < 2; ++kk) ra[m][kk] = lds_a(lds + so + a_base(lane, kk) + (hw * 2 + m) * 2048);
      }
      if (t == scol) {
#pragma unroll
        for (int i = 0; i < 3; ++i) {
          const int n = hw * 3 + i;
          const unsigned cb = so + AB + (n >> 2) * 8192 + b128_base(lane, n & 3);
#pragma unroll
          for (int j = 0; j < 4; ++j) rb[i][j] = lds_tr(lds + cb + ((j >> 1) * 32 + (j & 1) * 8) * 128);
        }
      }
      wait_lgkm0();
      between(t);
      if (t == srow) {
        const f32x2 r0 = rowstat(ra[0][0], ra[0][1]), r1 = rowstat(ra[1][0], ra[1][1]);
        if (diag_lane) { *(LAS f32x2*)(lds + L_P + (hw * 32 + (lane & 15)) * 8) = r0; *(LAS f32x2*)(lds + L_P + (hw * 32 + 16 + (lane & 15)) * 8) = r1; }
      }
      if (t == scol) {
#pragma unroll
        for (int i = 0; i < 3; ++i)
          *(LAS float*)(lds + L_P + 1024 + ((hw * 3 + i) * 16 + (lane & 15)) * 4) =
              mfma(ones, cat8(rb[i][2], rb[i][3]), mfma(ones, cat8(rb[i][0], rb[i][1]), zero4))[0];
      }
    }
    wait_lgkm0();
  };

  if (wid == 7) {
    const unsigned l16 = lane * 16;
    unsigned vw[14];
    {
      int pos = lane / 14, gi = lane - pos * 14;
#pragma unroll
      for (int i = 0; i < 14; ++i) {
        vw[i] = swap23(pos) * (FF * 2) + (pn1 * 112 + gi * 8) * 2;
        pin(vw[i]);
        gi += 8; pos += 4; if (gi >= 14) { gi -= 14; pos += 1; }
      }
    }
    ABAR();
    const u64 tag = uniform64(*(LAS u64*)(lds + L_TAG));
    const u32x2 tag2 = __builtin_bit_cast(u32x2, tag);
    const unsigned myflag = lane < 16 ? (pm1 * 16 + pn1) * 128 + lane * 8 : 0xff000000u;
    g1_first_steps(3, [&](int) { ABAR(); });
    ABAR();
    st16_dev(srdW, WS_S1R + (pm1 * 16 + ((srow + r1) & 15)) * 1024 + l16, *(LAS u32x4*)(lds + L_P + l16));
    st16_dev(srdW, WS_S1C + (pn1 * 16 + ((scol + r1) & 15)) * 1024 + l16, *(LAS u32x4*)(lds + L_P + 1024 + l16));
    st16_dev(srdW, WS_S3 + (pm1 * 16 + pn1) * 1024 + l16, (u32x4){~0u, ~0u, ~0u, ~0u});
    unsigned tch[4] = {0u, 0u, 0u, 0u};
    u32x4 wt[28];
    const i32x4 srdWin = make_srd(win, DM * FF * 2);
    auto win_half = [&](int h, int b0, int b1) AI {
#pragma unroll
      for (int i = 0; i < 14; ++i) {
        if (i == b0 || i == b1) ABAR();
        ld16_nt(wt[h * 14 + i], srdWin, vw[i], pm1 * (64u * FF * 2) + h * (FH * 2));
      }
    };
    ABARS(2);
    wait_vm<0>();
    st8_dev(srdW, F_S1 + myflag, tag2);
    unsigned tcw[2];
    {
      const i32x4 srdO = make_srd(wout, FH * DM * 2);
#pragma unroll
      for (int i = 0; i < 2; ++i) {
        const int k = (idx & 3) * 128 + i * 64 + lane;
        touch(tcw[i], srdO, k < 448 ? (XROT * (xcd & 3) * 448 + k) * (DM * 2) + pn2 * 128 : 0xfffffff0u);
      }
    }
    touch(tch[3], make_srd(bsp, SEQ * 2), pm1 * 256 + (lane & 1) * 128);
    touch(tch[0], srdW, lane < 16 ? row_flags(F_O, pm2, lane) : lane < 32 ? row_flags(F_H, pm1, lane) : lane < 48 ? col_flags(F_Z2, pn1, lane) : row_flags(F_Y, pm2, lane));
    touch(tch[1], srdW, lane < 16 ? row_flags(F_S1, pm1, lane) : lane < 32 ? col_flags(F_S1, pn1, lane) : col_flags(F_W3, pn1, lane));
    ABARS(2);
    {
      const int pmh = pm1 > 0 ? pm1 - 1 : 0;
      const unsigned fo = lane < 16 ? row_flags(F_S1, pm1, lane) : lane < 32 ? row_flags(F_S1, pmh, lane) : col_flags(F_S1, pn1, lane);
      u32x2 f0;
      ld8_async(f0, srdW, fo);
      ABAR();
      wait_vm<0>();
      pin(f0); pin(tch[0]); pin(tch[1]); pin(tch[2]); pin(tch[3]); pin(tcw[0]); pin(tcw[1]);
      if (!__all(__builtin_bit_cast(u64, f0) == tag)) await(ws, fo, tag);
      const unsigned ro = WS_S1R + pm1 * 16384 + l16, co = lane < 48 ? WS_S1C + pn1 * 16384 + l16 : WS_S1R + pmh * 16384 + (56 + (lane & 7)) * 16;
      u32x4 gr[16], gc[16];
      f32x4 tr, tc;
      gather<0, 12>(gr, srdW, ro);
      ABAR();
      gather<12, 16>(gr, srdW, ro);
      gather<0, 8>(gc, srdW, co);
      ABAR();
      gather<8, 16>(gc, srdW, co);
      wait_vm<16>();
      accum<0, 16>(tr, gr);
      ABAR();
      put_rows(lds, L_MU, L_RS, 8 + lane, tr, 1.f / DM);
      wait_vm<0>();
      accum<0, 8>(tc, gc);
      accum<8, 16>(tc, gc);
      ABAR();
      if (lane < 48) *(LAS f32x4*)(lds + L_CS + l16) = tc;
      put_rows(lds, L_MU, L_RS, lane & 7, tc, 1.f / DM, lane >= 48 && lane < 56);
      wait_lgkm0();
    }
    {
      u32x4 xt[16];
      const i32x4 srdX = make_srd(x, SEQ * DM * 2);
      win_half(0, WB0, 14);
      win_half(1, WB1, 14);
      ABAR();
#pragma unroll
      for (int i = 0; i < 16; ++i) {
        const int r = i * 8 + (lane >> 3);
        ld16_plain(xt[i], srdX, (lane >> 3) * (DM * 2) + (((lane & 7) ^ ((r >> 1) & 7)) * 16), (pm2 * 128 + i * 8) * (DM * 2) + pn2 * 128);
      }
      wait_vm<16>();
#pragma unroll
      for (int i = 0; i < 28; ++i) { pin(wt[i]); *(LAS u32x4*)(lds + L_WT + i * 1024 + l16) = wt[i]; }
      wait_vm<0>();
#pragma unroll
      for (int i = 0; i < 16; ++i) { pin(xt[i]); *(LAS u32x4*)(lds + L_H + i * 1024 + l16) = xt[i]; }
    }
    ABAR();
    {
      const unsigned cb = L_WT + b224_base(lane);
      bf16x8 fr[2][2][2];
      float cs4[4];
      auto rd2 = [&](int q) AI {
#pragma unroll
        for (int u = 0; u < 2; ++u) {
          const int n = q * 2 + u;
          LAS char* p = lds + cb + (n / 7) * 14336 + (n % 7) * 32;
          fr[q & 1][u][0] = cat8(lds_tr(p), lds_tr(p + 8 * 224));
          fr[q & 1][u][1] = cat8(lds_tr(p + 32 * 224), lds_tr(p + 40 * 224));
        }
      };
      rd2(0);
      wait_lgkm0();
      ABAR();
#pragma unroll
      for (int q = 0; q < 7; ++q) {
        ABAR();
#pragma unroll
        for (int u = 0; u < 2; ++u) {
          const int n = q * 2 + u;
#pragma unroll
          for (int kk = 0; kk < 2; ++kk)
            st16_dev(srdW, WS_W3 + (pn1 * 16 + pm1) * 28672 + (n / 7) * 14336 + (kk * 7 + n % 7) * 1024 + l16, __builtin_bit_cast(u32x4, fr[q & 1][u][kk]));
          cs4[n & 3] = mfma(ones, fr[q & 1][u][1], mfma(ones, fr[q & 1][u][0], zero4))[0];
        }
        if (q < 6) rd2(q + 1);
        if ((q & 1) || q == 6)
          st4_dev(srdW, WS_S2C + (pn1 * 16 + pm1) * 1024 + (q >> 1) * 256 + lane * 4,
                  __float_as_uint(bsel(lane < 32 ? ~0u : 0u, bsel(lane < 16 ? ~0u : 0u, cs4[0], cs4[1]), bsel(lane < 48 ? ~0u : 0u, cs4[2], cs4[3]))));
      }
    }
    ABARS(4);
    wait_vm<0>();
    st8_dev(srdW, F_W3 + myflag, tag2);
    ABARS(5);
    {
      u32x2 f0;
      const unsigned fo = col_flags(F_W3, pn1, lane);
      ld8_async(f0, srdW, fo);
      ABAR();
      wait_vm<0>();
      pin(f0);
      if (!__all(__builtin_bit_cast(u64, f0) == tag)) await(ws, fo, tag);
    }
    {
      u32x4 gc[16];
      f32x4 tc;
      ABAR();
      gather<0, 16>(gc, srdW, WS_S2C + pn1 * 16384 + l16);
      wait_vm<0>();
      accum<0, 16>(tc, gc);
      if (lane < 56) *(LAS f32x4*)(lds + L_CS + l16) = tc;
    }
    ABARS(6);
    {
      u32x4 gr[16];
      f32x4 tr;
      const unsigned ro = WS_S2R + pm1 * 16384 + l16;
      ABAR();  gather<0, 4>(gr, srdW, ro);
      ABAR();  gather<4, 8>(gr, srdW, ro);
      ABAR();  gather<8, 12>(gr, srdW, ro);
      ABAR();  gather<12, 16>(gr, srdW, ro); wait_vm<8>(); accum<0, 8>(tr, gr);
      ABARS(2);
      wait_vm<0>(); accum<8, 16>(tr, gr);
      ABAR();
      put_rows(lds, L_MU, L_RS, lane, tr, 1.f / DM);
      wait_lgkm0();
    }
    ABARS(5);
    {
      const i32x4 srdSp = make_srd(wsp, SEQ * SEQ * 2);
#pragma unroll
      for (int i = 0; i < 2; ++i) touch(tch[i], srdSp, (pm1 * 128 + i * 64 + lane) * (SEQ * 2) + (XROT * (xcd >> 1) * 8 + 4 + (idx >> 3)) * 128);
    }
    dma16<0>(make_srd(bsp, SEQ * 2), lane < 16 ? pm1 * 256 + l16 : 0xfffffff0u, 0u, L_C2);
    ABARS(2);
    {
      u32x4 gv[16];
      f32x4 t;
      unsigned mx = 0;
      const unsigned so = WS_S3 + pm1 * 16384 + l16;
#pragma unroll
      for (int j = 0; j < 16; ++j) asm volatile("buffer_load_dwordx4 %0, %1, %2, %3 offen sc1" : "=v"(gv[j]) : "v"(so), "s"(srdW), "s"(j * 1024) : "memory");
      ABAR();
      wait_vm<8>();
      pin(tch[0]); pin(tch[1]);
      accum<0, 8>(t, gv);
#pragma unroll
      for (int j = 0; j < 8; ++j) mx = max(max(mx, gv[j][1]), gv[j][3]);
      pin(mx);
      ABAR();
      wait_vm<0>();
      accum<8, 16>(t, gv);
#pragma unroll
      for (int j = 8; j < 16; ++j) mx = max(max(mx, gv[j][1]), gv[j][3]);
      if (!__all(mx != ~0u)) {
#pragma unroll 1
        for (unsigned spin = 0; spin < (1u << 17); ++spin) {
          u32x4 gw[16];
          gather<0, 16>(gw, srdW, so);
          wait_vm<0>();
          accum<0, 16>(t, gw);
          mx = 0;
#pragma unroll
          for (int j = 0; j < 16; ++j) mx = max(max(mx, gw[j][1]), gw[j][3]);
          if (__all(mx != ~0u)) break;
        }
      }
      ABAR();
      put_rows(lds, L_M3, L_R3, lane, t, 1.f / FH);
      wait_lgkm0();
    }
    ABARS(6);
    st8_dev(srdW, F_Z2 + myflag, tag2);
    ABARS(2);
    {
      u32x2 f0;
      const unsigned fo = col_flags(F_Z2, pn1, lane);
      ld8_async(f0, srdW, fo);
      ABAR();
      wait_vm<0>();
      pin(f0);
      const bool up = __all(__builtin_bit_cast(u64, f0) == tag);
      *(LAS unsigned*)(lds + L_TAG + 8) = up;
      wait_lgkm0();
      ABAR();
      if (!up) await(ws, fo, tag);
      ABAR();
    }
    ABARS(64);
    return;
  }

  if (wid >= 4) {
    const int dw = wid - 4;
    int slot;
    const unsigned l16 = lane * 16;
    u64 tag;
    unsigned seen = 0;
    auto gate = [&](unsigned fo, unsigned need) AI { if ((seen & need) != need) seen = await_mask(ws, fo, tag, need); };
    auto phase_start = [&](auto& ix, unsigned fo, auto mask) AI {
      seen = dw == 0 ? 0u : 0xffffu;
      gate(fo, mask(0) | mask(1) | mask(2) | mask(3));
      HBAR();
      ix(0, 0); ix(1, 1);
      wait_vm<5>();
      HBAR();
      ix(2, 2); ix(3, 3);
      gate(fo, mask(4));
    };
    auto tail4 = [&](auto PW) AI {
      wait_vm<2 * decltype(PW)::value>(); HBAR();
      wait_vm<decltype(PW)::value>(); HBAR();
      wait_vm<0>(); HBAR();
      HBAR();
    };
    {
      using namespace g1;
      using P = Pipe<NK, NS, 14, 0>;
      const i32x4 srdA = make_srd(x, SEQ * DM * 2), srdB = make_srd(wqkv, DM * 3 * DM * 2);
      unsigned va[6], vb[8];
#pragma unroll
      for (int i = 0; i < 8; ++i) {
        const int p = dw * 8 + i, pos = (p & 7) * 8 + (lane >> 3), nb = ((lane & 7) >> 1) ^ ((pos >> 1) & 3);
        vb[i] = swap23(pos) * (3 * DM * 2) + (dw * DM + pn1 * 64 + nb * 16 + (lane & 1) * 8) * 2 - (i & 3) * 1024;
      }
#pragma unroll
      for (int i = 0; i < 6; ++i) {
        const int r = (dw * 6 + i) * 8 + (lane >> 3);
        int grow = r < 128 ? pm1 * 128 + r : pm1 * 128 - 16 + (r - 128);
        grow = grow < 0 ? 8 : grow;
        va[i] = grow * (DM * 2) + (((lane & 7) ^ ((r >> 1) & 7)) * 16) - (i & 3) * 1024;
      }
      auto ia = [&](int t, int s) {
        const unsigned kt = (t + r1) & 15;
        dmaS<4>(srdA, va, kt * 128u, s * ST + dw * 6144u);
        dmaS<2>(srdA, va + 4, kt * 128u, s * ST + dw * 6144u + 4096u);
      };
      auto ib = [&](int t, int s) {
        const unsigned kt = (t + r1) & 15;
        dmaS<4>(srdB, vb, kt * (64u * 3 * DM * 2), s * ST + AB + dw * 8192u);
        dmaS<4>(srdB, vb + 4, kt * (64u * 3 * DM * 2), s * ST + AB + dw * 8192u + 4096u);
      };
      ib(0, 0); ia(0, 0); ib(1, 1);
      wait_vm<8>();
      HBAR();
      ia(1, 1); ia(2, 2); ib(2, 2);
      tag = uniform64(*(LAS u64*)(lds + L_TAG));
      TS(14);
      slot = 0;
      g1_first_steps(dw, [&](int t) {
        wait_vm<P::PW>();
        HBAR();
        ia(t + NS, slot); ib(t + NS, slot);
        slot = slot + 1 == NS ? 0 : slot + 1;
      });
      TS(15);
      hsteps<P, 4, NK>(ia, ib, slot);
    }
    TS(1);
    auto lin16 = [&](unsigned src, unsigned dst) AI {
      const unsigned g0 = dw * 4096u, g1 = 12288u + dw * 1024u;
      dmaL<4>(srdW, l16, src + g0, dst + g0);
      if (dw == 2) dmaL<2>(srdW, l16, src + g1, dst + g1);
      else dmaL<1>(srdW, l16, src + g1, dst + g1);
    };
    unsigned vbs[3];
#pragma unroll
    for (int i = 0; i < 3; ++i) {
      const int pos = (i < 2 ? dw * 2 + i : (6 + dw) & 7) * 8 + (lane >> 3), nb = ((lane & 7) >> 1) ^ ((pos >> 1) & 3);
      vbs[i] = swap23(pos) * (DM * 2) + (pn2 * 64 + nb * 16 + (lane & 1) * 8) * 2 - (i == 1 ? 1024 : 0);
    }
    auto w8 = [&](i32x4 srd, unsigned kt, int s) AI {
      dmaS<2>(srd, vbs, kt * (64u * DM * 2), s * gs::ST + gs::AB + dw * 2048u);
      if (dw != 2) dmaS<1>(srd, vbs + 2, kt * (64u * DM * 2), s * gs::ST + gs::AB + (6 + dw) * 1024u);
    };
    const unsigned vflex = dw == 2 ? l16 : vbs[2];
    auto gs_phase = [&](auto T1, auto& ix, i32x4 srdB, unsigned abase, int r0, int nk, unsigned fo, auto mask) AI {
      i32x4 srdF;
#pragma unroll
      for (int e = 0; e < 4; ++e) srdF[e] = dw == 2 ? srdW[e] : srdB[e];
      steady<0, decltype(T1)::value, 16, 10>(
          [&]() AI { phase_start(ix, fo, mask); },
          [&](int u) AI {
            const unsigned kt = rot(u, r0, nk), a = abase + kt * 16384u, b = kt * (64u * DM * 2), l = (u & 3) * gs::ST;
            return Nx{a + dw * 4096u, a + 12288u + dw * 1024u, b, dw == 2 ? a + 15360u : b,
                      l + dw * 4096u, l + 12288u + dw * 1024u, l + gs::AB + dw * 2048u, l + (dw == 2 ? 15360u : gs::AB + (6 + dw) * 1024u)};
          },
          [&](unsigned so, unsigned l) AI { dmaL<4>(srdW, l16, so, l); }, [&](unsigned so, unsigned l) AI { dmaS<2>(srdB, vbs, so, l); },
          [&](unsigned so, unsigned l) AI { dmaL<1>(srdW, l16, so, l); }, [&](unsigned so, unsigned l) AI { dmaL<1>(srdF, vflex, so, l); },
          [&](int u) AI { if (__builtin_expect(seen != 0xffffu, 0) && u < nk) gate(fo, mask(u)); });
      tail4(IC<8>{});
    };
    {
      using namespace gs;
      constexpr int NK = 16;
      const i32x4 srdB = make_srd(wo, DM * DM * 2);
      const int r0 = XROT * (xcd & 3) * 4;
      auto ix = [&](int t, int s) { lin16(WS_O + (pm2 * 16 + ((t + r0) & 15)) * 16384u, s * ST); };
      auto iw = [&](int t, int s) { w8(srdB, (t + r0) & 15, s); };
      HBAR();
      HBAR();
      iw(0, 0); iw(1, 1); iw(2, 2); iw(3, 3);
      TS(2);
      auto mask = [&](int u) AI { return 1u << ((u + r0) & 15); };
      gs_phase(IC<NK - NS>{}, ix, srdB, WS_O + pm2 * 16 * 16384u, r0, NK, row_flags(F_O, pm2, lane), mask);
    }
    TS(4);
    {
      using namespace gf;
      const i32x4 srdSp = make_srd(wsp, SEQ * SEQ * 2);
      auto lin14 = [&](unsigned src, int s) AI {
        const unsigned dst = B0 + s * SEG;
        dmaL<4>(srdW, l16, src + dw * 4096u, dst + dw * 4096u);
        if (dw != 2) dmaL<1>(srdW, l16, src + (12 + dw) * 1024u, dst + (12 + dw) * 1024u);
      };
      auto ia3 = [&](int t, int s) { lin16(WS_H + (pm1 * 16 + (t & 15)) * 16384u, s * AB); };
      auto ib3 = [&](int t, int s) { lin14(WS_W3 + (pn1 * 16 + (t & 15)) * 28672u + (t < 16 ? 14336u : 0u), s); };
      unsigned va4[6];
#pragma unroll
      for (int i = 0; i < 6; ++i) { const int r = (i < 4 ? dw * 4 + i : 8 + dw + i) * 8 + (lane >> 3); va4[i] = (pm1 * 128 + r) * (SEQ * 2) + (((lane & 7) ^ ((r >> 1) & 7)) * 16) - (i & 3) * 1024; }
      const int r4 = XROT * (xcd >> 1) * 8;
      auto ia4 = [&](int t, int s) {
        const unsigned kt = (t + r4) & 31;
        dmaS<4>(srdSp, va4, kt * 128u, s * AB + dw * 4096u);
        if (dw == 2) dmaS<2>(srdSp, va4 + 4, kt * 128u, s * AB + 14 * 1024u);
        else dmaS<1>(srdSp, va4 + 4, kt * 128u, s * AB + (12 + dw) * 1024u);
      };
      auto ib4 = [&](int t, int s) { lin14(WS_Z2 + (pn1 * 32 + ((t + r4) & 31)) * (unsigned)SEG, s); };
      HBAR();
      ib3(0, 0); ib3(1, 1); ib3(2, 2); ib3(3, 3);
      TS(5);
      auto L4 = [&](unsigned so, unsigned l) AI { dmaL<4>(srdW, l16, so, l); };
      auto L1 = [&](unsigned so, unsigned l) AI { dmaL<1>(srdW, l16, so, l); };
      steady<0, 28, 20, 10>(
          [&]() AI { phase_start(ia3, row_flags(F_H, pm1, lane), [](int u) AI { return 1u << u; }); },
          [&](int u) AI {
            const unsigned a = WS_H + (pm1 * 16 + (u & 15)) * 16384u, b = WS_W3 + (pn1 * 16 + (u & 15)) * 28672u + (u < 16 ? 14336u : 0u);
            const unsigned la = (u & 3) * AB, lb = B0 + (u & 3) * SEG;
            return Nx{a + dw * 4096u, a + 12288u + dw * 1024u, b + dw * 4096u, dw == 2 ? a + 15360u : b + 12288u + dw * 1024u,
                      la + dw * 4096u, la + 12288u + dw * 1024u, lb + dw * 4096u, dw == 2 ? la + 15360u : lb + 12288u + dw * 1024u};
          },
          L4, L4, L1, L1,
          [&](int u) AI { if (__builtin_expect(seen != 0xffffu, 0)) gate(row_flags(F_H, pm1, lane), u < 8 ? 1u << u : 0xffffu); });
      TS(9);
      i32x4 srdF;
#pragma unroll
      for (int e = 0; e < 4; ++e) srdF[e] = dw == 2 ? srdSp[e] : srdW[e];
      const unsigned vf4 = dw == 2 ? va4[5] + 1024u : l16;
      steady<32, 60, 20, 14>(
          [&]() AI {
            wait_vm<20>(); HBAR(); ia4(32, 0);
            wait_vm<15>(); HBAR(); ia4(33, 1);
            wait_vm<10>(); HBAR();
            const bool early = __builtin_amdgcn_readfirstlane(*(LAS unsigned*)(lds + L_TAG + 8)) != 0;
            if (early) { ib4(32, 0); ib4(33, 1); }
            ia4(34, 2);
            HBAR();
            if (!early) { ib4(32, 0); ib4(33, 1); }
            ib4(34, 2); ia4(35, 3); ib4(35, 3);
            wait_vm<18>(); HBAR();
          },
          [&](int u) AI {
            const unsigned kt = (u + r4) & 31, a = kt * 128u, b = WS_Z2 + (pn1 * 32 + kt) * (unsigned)SEG;
            const unsigned la = (u & 3) * AB, lb = B0 + (u & 3) * SEG;
            return Nx{a, a, b + dw * 4096u, dw == 2 ? a : b + 12288u + dw * 1024u,
                      la + dw * 4096u, la + 12288u + dw * 1024u, lb + dw * 4096u, dw == 2 ? la + 15360u : lb + 12288u + dw * 1024u};
          },
          [&](unsigned so, unsigned l) AI { dmaS<4>(srdSp, va4, so, l); }, L4,
          [&](unsigned so, unsigned l) AI { dmaS<1>(srdSp, va4 + 4, so, l); }, [&](unsigned so, unsigned l) AI { dmaL<1>(srdF, vf4, so, l); }, [](int) AI {});
      tail4(IC<10>{});
    }
    TS(10);
    {
      using namespace gs;
      constexpr int NK = 28;
      const i32x4 srdB = make_srd(wout, FH * DM * 2);
      const int r0 = XROT * (xcd & 3) * 7;
      auto ix = [&](int t, int s) { lin16(WS_Y + (pm2 * 28 + rot(t, r0, NK)) * 16384u, s * ST); };
      auto iw = [&](int t, int s) { w8(srdB, rot(t, r0, NK), s); };
      iw(0, 0); iw(1, 1);
      HBAR();
      iw(2, 2); iw(3, 3);
      TS(11);
      auto mask = [&](int u) AI { const int k = rot(u, r0, NK); return (1u << (k * 8 / 14)) | (1u << ((k * 8 + 7) / 14)); };
      gs_phase(IC<NK - NS>{}, ix, srdB, WS_Y + pm2 * 28 * 16384u, r0, NK, row_flags(F_Y, pm2, lane), mask);
    }
    TS(13);
    return;
  }

  const u64 tag = uniform64(__hip_atomic_load((gu64*)(ws + WS_EPOCH), __ATOMIC_RELAXED, __HIP_MEMORY_SCOPE_AGENT)) + 1;
  if (tid == 0) *(LAS u64*)(lds + L_TAG) = tag;
  wait_lgkm0();
#define LANES const int tid = fresh_tid(), lane = tid & 63, i16 = lane & 15, g = lane >> 4; (void)i16; (void)g
  auto publish = [&](unsigned flag) {
    wait_vm<0>();
    bar();
    if (threadIdx.x < 16) set_flag(ws, flag + threadIdx.x * 8, tag);
    bar();
  };
  {
    using namespace g1;
    LANES;
    const int hd = wid & 1, rh = wid >> 1;
    unsigned ab[2], hb[2], bb[2];
#pragma unroll
    for (int kk = 0; kk < 2; ++kk) { ab[kk] = a_base(lane, kk) + rh * 64 * 128; hb[kk] = a_base(lane, kk) + 128 * 128; bb[kk] = AB + b128_base(lane, hd * 2 + kk); }
    f32x4 aq[4][2], ak[4][2], av[4][2], ah[2];
#pragma unroll
    for (int m = 0; m < 4; ++m)
#pragma unroll
      for (int j = 0; j < 2; ++j) aq[m][j] = ak[m][j] = av[m][j] = zero4;
    ah[0] = ah[1] = zero4;
    auto load = [&](Fr& f, unsigned so, unsigned, int kk) {
#pragma unroll
      for (int m = 0; m < 4; ++m) f.a[m] = lds_a(lds + so + ab[kk] + m * 2048);
      f.a[4] = lds_a(lds + so + hb[kk]);
#pragma unroll
      for (int s = 0; s < 3; ++s)
#pragma unroll
        for (int j = 0; j < 2; ++j)
#pragma unroll
          for (int hh = 0; hh < 2; ++hh) f.b[s * 2 + j][hh] = lds_tr(lds + so + bb[j] + s * 8192 + (kk * 32 + hh * 8) * 128);
    };
    auto mma2 = [&](Fr& f, auto RH) {
      bf16x8 b[6];
#pragma unroll
      for (int i = 0; i < 6; ++i) b[i] = cat8(f.b[i][0], f.b[i][1]);
#pragma unroll
      for (int m = 0; m < 4; ++m)
#pragma unroll
        for (int j = 0; j < 2; ++j) {
          aq[m][j] = mfma(b[j], f.a[m], aq[m][j]);
          ak[m][j] = mfma(b[2 + j], f.a[m], ak[m][j]);
          av[m][j] = mfma(f.a[m], b[4 + j], av[m][j]);
        }
#pragma unroll
      for (int j = 0; j < 2; ++j) {
        if constexpr (decltype(RH)::value == 0) ah[j] = mfma(b[2 + j], f.a[4], ah[j]);
        else ah[j] = mfma(f.a[4], b[4 + j], ah[j]);
      }
    };
    const int ra = i16 & 1;
    const bool curA = i16 - 4 * g - ra >= 0, curB = i16 - 4 * g - ra - 2 >= 0;
    LAS char* t16 = lds + L_MU + (16 + rh * 64 + i16) * 4;
    LAS char* t4g = lds + L_MU + (16 + rh * 64 + 4 * g) * 4;
    LAS char* tka = lds + L_RS + (rh * 64 + 4 * g + ra + (curA ? 16 : 0)) * 4;
    LAS char* tkb = lds + L_RS + (rh * 64 + 4 * g + ra + 2 + (curB ? 16 : 0)) * 4;
    LAS char* xch = lds + L_X + hd * 3072 + lane * 16;
    unsigned oaddr = WS_O + (pm1 * 16 + pn1) * 16384 + (hd * 4 + (g >> 1)) * 2048 + (rh * 64 + (g & 1) * 16 + i16) * 16;
    unsigned mA = curA ? ~0u : 0u, mB = curB ? ~0u : 0u;
    pin(t16); pin(t4g); pin(tka); pin(tkb); pin(xch); pin(oaddr); pin(mA); pin(mB);
    if (rh == 0) compute_loop<NK, NS, ST, ST, 17, 26, Fr>(load, [&](Fr& f) { mma2(f, IC<0>{}); });
    else compute_loop<NK, NS, ST, ST, 17, 26, Fr>(load, [&](Fr& f) { mma2(f, IC<1>{}); });
    TS(1);
    f32x4 csq[2], csk[2]; float csv[2];
    const int co = L_CS - L_MU + (hd * 32 - 16 - rh * 64) * 4;
#pragma unroll
    for (int j = 0; j < 2; ++j) {
      csq[j] = *(LAS f32x4*)(t4g + co + j * 64);
      csk[j] = *(LAS f32x4*)(t4g + co + 256 + j * 64);
      csv[j] = *(LAS float*)(t16 + co + 512 + j * 64);
    }
    bf16x8 Q[4], K[5]; u32x2 V[5][2];
    float rq[4], rka[4], rkb[4];
#pragma unroll
    for (int m = 3; m >= 0; --m) {
      const float mu = *(LAS float*)(t16 + m * 64);
      const f32x4 mu4 = *(LAS f32x4*)(t4g + m * 64);
      rq[m] = *(LAS float*)(t16 + (L_RS - L_MU) + m * 64);
      rka[m] = *(LAS float*)(tka + m * 64);
      rkb[m] = *(LAS float*)(tkb + m * 64);
      K[m + 1] = cat8(pk4(ak[m][0] - mu * csk[0]), pk4(ak[m][1] - mu * csk[1]));
      V[m + 1][0] = pk4(av[m][0] - mu4 * csv[0]);
      V[m + 1][1] = pk4(av[m][1] - mu4 * csv[1]);
      Q[m] = cat8(pk4(aq[m][0] - mu * csq[0]), pk4(aq[m][1] - mu * csq[1]));
    }
    if (rh == 0) {
      const float mu = *(LAS float*)(t16 - 64);
      K[0] = cat8(pk4(ah[0] - mu * csk[0]), pk4(ah[1] - mu * csk[1]));
      *(LAS bf16x8*)(xch) = K[4];
      *(LAS bf16x8*)(xch + 1024) = cat8(V[4][0], V[4][1]);
    } else {
      const f32x4 mu4 = *(LAS f32x4*)(t4g - 320);
      *(LAS bf16x8*)(xch + 2048) = cat8(pk4(ah[0] - mu4 * csv[0]), pk4(ah[1] - mu4 * csv[1]));
      K[0] = K[4];
    }
    wait_lgkm0(); bar();
    {
      u32x4 v = *(LAS u32x4*)(xch + (rh == 0 ? 2048 : 1024));
      V[0][0] = (u32x2){v[0], v[1]}; V[0][1] = (u32x2){v[2], v[3]};
      if (rh == 1) K[0] = *(LAS bf16x8*)(xch);
    }
    constexpr float SC = 0.17677669529663687f * 1.4426950408889634f;
    f32x4 sp[4], sc[4];
#pragma unroll
    for (int m = 3; m >= 0; --m) { sc[m] = mfma(K[m + 1], Q[m], zero4); sp[m] = mfma(K[m], Q[m], zero4); }
    const bool nohalo = pm1 == 0 && rh == 0;
    const unsigned selA = ra ? 0x01000c0cu : 0x0c0c0100u, selB = ra ? 0x03020c0cu : 0x0c0c0302u;
    const unsigned mR = ra ? ~0u : 0u;
    float la[4], lb[4], mx[4];
#pragma unroll
    for (int m = 0; m < 4; ++m) {
      const float a = bsel(mA, bsel(mR, sc[m][1], sc[m][0]), bsel(mR, sp[m][1], sp[m][0]));
      const float b = bsel(mB, bsel(mR, sc[m][3], sc[m][2]), bsel(mR, sp[m][3], sp[m][2]));
      const float f = SC * rq[m];
      la[m] = a * (f * rka[m]); lb[m] = b * (f * rkb[m]);
    }
    la[0] = bsel(nohalo ? mA : ~0u, la[0], -INFINITY); lb[0] = bsel(nohalo ? mB : ~0u, lb[0], -INFINITY);
#pragma unroll
    for (int m = 0; m < 4; ++m) mx[m] = max4(vmax(la[m], lb[m]));
    f32x4 o[4][2];
#pragma unroll
    for (int m = 0; m < 4; ++m) { la[m] = __builtin_amdgcn_exp2f(la[m] - mx[m]); lb[m] = __builtin_amdgcn_exp2f(lb[m] - mx[m]); }
#pragma unroll
    for (int m = 0; m < 4; ++m) mx[m] = __builtin_amdgcn_rcpf(sum4(la[m] + lb[m]));
#pragma unroll
    for (int m = 0; m < 4; ++m) {
      const unsigned w = pk2(la[m] * (mx[m] * rka[m]), lb[m] * (mx[m] * rkb[m]));
      const unsigned wa = __builtin_amdgcn_perm(0u, w, selA), wb = __builtin_amdgcn_perm(0u, w, selB);
      const bf16x8 P = __builtin_bit_cast(bf16x8, (u32x4){wa & ~mA, wb & ~mB, wa & mA, wb & mB});
#pragma unroll
      for (int db = 0; db < 2; ++db) o[m][db] = mfma(cat8(V[m][db], V[m + 1][db]), P, zero4);
    }
    TS(2);
#pragma unroll
    for (int p = 0; p < 2; ++p)
#pragma unroll
      for (int db = 0; db < 2; ++db)
        st16_dev(srdW, oaddr + db * 4096 + p * 512, chunk16(pk4(o[2 * p][db]), pk4(o[2 * p + 1][db])));
    TS(3);
    publish(F_O + (pm1 * 16 + pn1) * 128);
  }
  auto small_gemm = [&](auto NKC, u32x2 (&res)[2][4]) AI {
    using namespace gs;
    LANES;
    unsigned ab[2], bb[4];
#pragma unroll
    for (int kk = 0; kk < 2; ++kk) ab[kk] = (kk * 4 + g) * 2048 + (wid * 32 + i16) * 16;
#pragma unroll
    for (int j = 0; j < 4; ++j) bb[j] = AB + b128_base(lane, j);
    f32x4 acc[2][4];
#pragma unroll
    for (int m = 0; m < 2; ++m)
#pragma unroll
      for (int j = 0; j < 4; ++j) acc[m][j] = unpk4(*(LAS u32x2*)(lds + L_H + img_off(wid * 32 + m * 16 + i16, j * 2 + (g >> 1)) + (g & 1) * 8));
    auto load = [&](Fr& f, unsigned so, unsigned) {
#pragma unroll
      for (int kk = 0; kk < 2; ++kk) {
#pragma unroll
        for (int m = 0; m < 2; ++m) f.a[kk][m] = lds_a(lds + so + ab[kk] + m * 256);
#pragma unroll
        for (int j = 0; j < 4; ++j)
#pragma unroll
          for (int hh = 0; hh < 2; ++hh) f.b[kk][j][hh] = lds_tr(lds + so + bb[j] + (kk * 32 + hh * 8) * 128);
      }
    };
    auto mma = [&](Fr& f) {
#pragma unroll
      for (int kk = 0; kk < 2; ++kk)
#pragma unroll
        for (int m = 0; m < 2; ++m)
#pragma unroll
          for (int j = 0; j < 4; ++j) acc[m][j] = mfma(cat8(f.b[kk][j][0], f.b[kk][j][1]), f.a[kk][m], acc[m][j]);
    };
    Fr f0;
    unsigned sa = 0, sb = 0;
    bar();
    load(f0, 0u, 0u);
    deep_segment<decltype(NKC)::value, NS, ST, ST, 20, 16>(f0, sa, sb, load, mma);
#pragma unroll
    for (int m = 0; m < 2; ++m)
#pragma unroll
      for (int j = 0; j < 4; ++j) res[m][j] = pk4(acc[m][j]);
  };
  {
    u32x2 h[2][4];
    small_gemm(IC<16>{}, h);
    TS(5);
    LANES;
#pragma unroll
    for (int j = 0; j < 4; ++j)
      st16_dev(srdW, WS_H + (pm2 * 16 + pn2) * 16384 + (j * 2 + (g >> 1)) * 2048 + (wid * 32 + (g & 1) * 16 + i16) * 16, chunk16(h[0][j], h[1][j]));
    const unsigned o = diag_lane ? WS_S2R + (pm2 * 16 + pn2) * 1024 + (wid * 32 + i16) * 8 : 0xffff0000u;
#pragma unroll
    for (int m = 0; m < 2; ++m) st8_dev(srdW, o + m * 128, __builtin_bit_cast(u32x2, rowstat(cat8(h[m][0], h[m][1]), cat8(h[m][2], h[m][3]))));
#pragma unroll
    for (int m = 0; m < 2; ++m)
#pragma unroll
      for (int j = 0; j < 4; ++j) *(LAS u32x2*)(lds + L_H + img_off(wid * 32 + m * 16 + i16, j * 2 + (g >> 1)) + (g & 1) * 8) = h[m][j];
    wait_lgkm0();
    TS(6);
    publish(F_H + (pm2 * 16 + pn2) * 128);
  }
  {
    using namespace gf;
    LANES;
    unsigned ab[2];
#pragma unroll
    for (int kk = 0; kk < 2; ++kk) ab[kk] = (kk * 4 + g) * 2048 + (wid * 32 + i16) * 16;
    const unsigned bb = B0 + lane * 16;
    f32x4 acc[2][7];
    LAS char *pa0 = lds + ab[0], *pa1 = lds + ab[1], *pb = lds + bb;
    unsigned sa = 0, sb = 0;
    auto rd = [&](Fr& f, int r, int ms) AI {
      const int kk = r / 9, q = r % 9;
      if (q < 2) f.a[kk][q] = lds_a((kk ? pa1 : pa0) + q * ms);
      else f.b[kk][q - 2] = lds_a(pb + (kk * 7 + q - 2) * 1024);
    };
    auto advance = [&]() AI {
      sa += AB; sb += SEG; if (sa == NS * AB) { sa = 0; sb = 0; }
      pa0 = lds + (sa + ab[0]); pa1 = lds + (sa + ab[1]); pb = lds + (sb + bb);
    };
    auto tile = [&](Fr& c, Fr& n, int ms, auto hook) AI {
      cbar();
#pragma unroll
      for (int i = 0; i < 28; ++i) {
        const int kk = i / 14, m = (i % 14) / 7, j = i % 7;
        asm volatile("v_mfma_f32_16x16x32_bf16 %0, %1, %2, %0" : "+v"(acc[m][j]) : "v"(c.b[kk][j]), "v"(c.a[kk][m]));
        if (i < 18) { rd(n, i, ms); __builtin_amdgcn_sched_barrier(0); }
        hook(i);
        if (i == 24) { sa += AB; sb += SEG; if (sa == NS * AB) { sa = 0; sb = 0; } spin(sa); spin(sb); }
        if (i == 25) { pa0 = lds + (sa + ab[0]); pin(pa0); }
        if (i == 26) { pa1 = lds + (sa + ab[1]); pin(pa1); }
        if (i == 27) { pb = lds + (sb + bb); pin(pb); }
        __builtin_amdgcn_sched_barrier(0);
      }
    };
    auto nohook = [](int) AI {};
    Fr f0;
    auto settle = [&]() AI {
      asm volatile("s_nop 7\n\ts_nop 1" ::: "memory");
      __builtin_amdgcn_sched_barrier(0);
    };
    auto segment = [&](int n, int ms) AI {
      Fr f1;
#pragma unroll 1
      for (int t = 0; t < n; t += 2) { tile(f0, f1, ms, nohook); tile(f1, f0, ms, nohook); }
    };
    u32x2 zz[2][7];
    float mu[2], c1[2], c2[2], ir[2];
    auto rowconst = [&]() AI {
#pragma unroll
      for (int m = 0; m < 2; ++m) {
        mu[m] = -*(LAS float*)(lds + L_MU + (wid * 32 + m * 16 + i16) * 4);
        const float rs = *(LAS float*)(lds + L_RS + (wid * 32 + m * 16 + i16) * 4);
        c1[m] = -2.302208198f * rs; c2[m] = -0.1029432397f * rs * rs * rs; ir[m] = __builtin_amdgcn_rcpf(rs);
      }
    };
    auto act = [&]() AI {
      constexpr bool centre = true;
#pragma unroll
      for (int j0 = 0; j0 < 7; j0 += 2) {
        f32x4 e[4], cs[2];
        const int n = j0 < 6 ? 4 : 2;
        if (centre) {
          cs[0] = *(LAS f32x4*)(lds + L_CS + (112 + j0 * 16 + 4 * g) * 4);
          cs[1] = *(LAS f32x4*)(lds + L_CS + (112 + (j0 < 6 ? j0 + 1 : j0) * 16 + 4 * g) * 4);
#pragma unroll
          for (int u = 0; u < 4; ++u) if (u < n) acc[u & 1][j0 + (u >> 1)] += mu[u & 1] * cs[u >> 1];
        }
#pragma unroll
        for (int u = 0; u < 4; ++u) if (u < n) e[u] = acc[u & 1][j0 + (u >> 1)] * acc[u & 1][j0 + (u >> 1)];
#pragma unroll
        for (int u = 0; u < 4; ++u) if (u < n) e[u] = e[u] * c2[u & 1] + c1[u & 1];
#pragma unroll
        for (int u = 0; u < 4; ++u) if (u < n) e[u] = e[u] * acc[u & 1][j0 + (u >> 1)];
#pragma unroll
        for (int u = 0; u < 4; ++u) if (u < n)
#pragma unroll
          for (int i = 0; i < 4; ++i) e[u][i] = __builtin_amdgcn_exp2f(e[u][i]);
#pragma unroll
        for (int u = 0; u < 4; ++u) if (u < n) e[u] = e[u] * ir[u & 1] + ir[u & 1];
#pragma unroll
        for (int u = 0; u < 4; ++u) if (u < n)
#pragma unroll
          for (int i = 0; i < 4; ++i) e[u][i] = __builtin_amdgcn_rcpf(e[u][i]);
#pragma unroll
        for (int u = 0; u < 4; ++u) if (u < n) { zz[u & 1][j0 + (u >> 1)] = pk4(acc[u & 1][j0 + (u >> 1)] * e[u]); pin(zz[u & 1][j0 + (u >> 1)]); }
        __builtin_amdgcn_sched_barrier(0);
      }
    };
#pragma unroll
    for (int m = 0; m < 2; ++m)
#pragma unroll
      for (int j = 0; j < 7; ++j) acc[m][j] = zero4;
    bar();
#pragma unroll
    for (int r = 0; r < 18; ++r) rd(f0, r, 256);
    advance();
    segment(16, 256);
    settle();
    TS(7);
    rowconst();
    act();
    {
      f32x2 st[2];
#pragma unroll
      for (int m = 0; m < 2; ++m)
        st[m] = rowstat(cat8(zz[m][0], zz[m][1]), cat8(zz[m][2], zz[m][3])) + rowstat(cat8(zz[m][4], zz[m][5]), cat8(zz[m][6], (u32x2){0u, 0u}));
      const unsigned o = diag_lane ? WS_S3 + (pm1 * 16 + pn1) * 1024 + (wid * 32 + i16) * 8 : 0xffff0000u;
#pragma unroll
      for (int m = 0; m < 2; ++m) st8_dev(srdW, o + m * 128, (u32x2){__float_as_uint(st[m][0]), min(__float_as_uint(st[m][1]), ~1u)});
    }
#pragma unroll
    for (int j = 0; j < 7; ++j) {
      const f32x4 cs = *(LAS f32x4*)(lds + L_CS + (j * 16 + 4 * g) * 4);
      acc[0][j] = mu[0] * cs; acc[1][j] = mu[1] * cs;
    }
    asm volatile("s_nop 1" ::: "memory");
    TS(8);
    Fr f1;
    segment(6, 256);
    TS(9);
    {
      const unsigned one = diag_lane ? ((lane & 1) ? 0x3f800000u : 0x00003f80u) : 0u;
      const u32x2 e = {(lane & 2) ? 0u : one, (lane & 2) ? one : 0u}, z = {0u, 0u};
      const bf16x8 elo = cat8(e, z), ehi = cat8(z, e);
      f32x4 r3[2], m3[2];
#pragma unroll
      for (int m = 0; m < 2; ++m) {
        r3[m] = *(LAS f32x4*)(lds + L_R3 + (wid * 32 + m * 16 + 4 * g) * 4);
        m3[m] = *(LAS f32x4*)(lds + L_M3 + (wid * 32 + m * 16 + 4 * g) * 4);
      }
      f32x4 t[2][7];
#pragma unroll
      for (int j = 0; j < 7; ++j)
#pragma unroll
        for (int m = 0; m < 2; ++m) t[m][j] = mfma(cat8(zz[m][j & ~1], zz[m][j < 6 ? j | 1 : 6]), (j & 1) ? ehi : elo, zero4);
#pragma unroll
      for (int m = 0; m < 2; ++m) m3[m] = -r3[m] * m3[m];
      const unsigned zo = WS_Z2 + (pn1 * 32 + pm1 * 2) * SEG + wid * 7168 + (((g & 1) * 2 + (g >> 1)) * 16 + i16) * 16;
#pragma unroll
      for (int j = 0; j < 7; ++j) st16_dev(srdW, zo + j * 1024, chunk16(pk4(t[0][j] * r3[0] + m3[0]), pk4(t[1][j] * r3[1] + m3[1])));
    }
    TS(10);
#pragma unroll 1
    for (int t = 22; t < 32; t += 2) {
      if (t == 26) wait_vm<0>();
      tile(f0, f1, 256, nohook); tile(f1, f0, 256, nohook);
    }
    settle();
    TS(11);
    rowconst();
    u32x32 zv;
#pragma unroll
    for (int m = 0; m < 2; ++m) {
      const float b = (float)*(LAS __bf16*)(lds + L_C2 + (wid * 32 + m * 16 + i16) * 2);
#pragma unroll
      for (int j = 0; j < 7; ++j) { const u32x2 p = pk4(acc[m][j]); zv[4 * j + m] = p[0]; zv[4 * j + 2 + m] = p[1]; acc[m][j] = zero4 + b; }
    }
    TS(12);
    bar();
    ab[0] = a_base(lane, 0) + wid * 32 * 128; ab[1] = a_base(lane, 1) + wid * 32 * 128;
    pa0 = lds + ab[0]; pa1 = lds + ab[1]; pb = lds + bb; sa = 0; sb = 0;
#pragma unroll
    for (int r = 0; r < 18; ++r) rd(f0, r, 2048);
    advance();
    {
      unsigned w = 0;
      float u0, u1, e0, e1;
      auto gel = [&](int m, int i) AI {
        if (i == 0) asm volatile("v_lshlrev_b32 %0, 16, %1" : "=v"(u0) : "v"(w));
        if (i == 1) asm volatile("v_and_b32 %0, 0xffff0000, %1" : "=v"(u1) : "v"(w));
        if (i == 2) asm volatile("v_mul_f32 %0, %1, %1" : "=v"(e0) : "v"(u0));
        if (i == 3) asm volatile("v_mul_f32 %0, %1, %1" : "=v"(e1) : "v"(u1));
        if (i == 4) asm volatile("v_fma_f32 %0, %0, %1, %2" : "+v"(e0) : "v"(c2[m]), "v"(c1[m]));
        if (i == 5) asm volatile("v_fma_f32 %0, %0, %1, %2" : "+v"(e1) : "v"(c2[m]), "v"(c1[m]));
        if (i == 6) asm volatile("v_mul_f32 %0, %0, %1" : "+v"(e0) : "v"(u0));
        if (i == 7) asm volatile("v_mul_f32 %0, %0, %1" : "+v"(e1) : "v"(u1));
        if (i == 8) asm volatile("v_exp_f32 %0, %0" : "+v"(e0));
        if (i == 9) asm volatile("v_exp_f32 %0, %0" : "+v"(e1));
        if (i == 10) asm volatile("v_fma_f32 %0, %0, %1, %1" : "+v"(e0) : "v"(ir[m]));
        if (i == 11) asm volatile("v_fma_f32 %0, %0, %1, %1" : "+v"(e1) : "v"(ir[m]));
        if (i == 12) asm volatile("v_rcp_f32 %0, %0" : "+v"(e0));
        if (i == 13) asm volatile("v_rcp_f32 %0, %0" : "+v"(e1));
        if (i == 14) asm volatile("v_mul_f32 %0, %0, %1" : "+v"(e0) : "v"(u0));
        if (i == 15) asm volatile("v_mul_f32 %0, %0, %1" : "+v"(e1) : "v"(u1));
        if (i == 16) asm volatile("v_cvt_pk_bf16_f32 %0, %1, %2" : "=v"(w) : "v"(e0), "v"(e1));

      };
      w = zv[0];
#pragma unroll 1
      for (int it = 0; it < 28; it += 2) {
        tile(f0, f1, 2048, [&](int i) AI { gel(0, i); if (i == 18) { zv[it] = w; pin(zv); } if (i == 20) { w = zv[it + 1]; pin(w); } });
        tile(f1, f0, 2048, [&](int i) AI { gel(1, i); if (i == 18) { zv[it + 1] = w; pin(zv); } if (i == 20) { w = zv[it + 2]; pin(w); } });
      }
    }
    segment(4, 2048);
    settle();
    TS(13);
#pragma unroll
    for (int j = 0; j < 7; ++j)
      st16_dev(srdW, WS_Y + (pm1 * 16 + pn1) * 28672 + (j * 2 + (g >> 1)) * 2048 + (wid * 32 + (g & 1) * 16 + i16) * 16,
               chunk16(pk4(unpk4((u32x2){zv[4 * j], zv[4 * j + 2]}) * acc[0][j]), pk4(unpk4((u32x2){zv[4 * j + 1], zv[4 * j + 3]}) * acc[1][j])));
    TS(15);
    publish(F_Y + (pm1 * 16 + pn1) * 128);
  }
  {
    u32x2 r[2][4];
    small_gemm(IC<28>{}, r);
    TS(16);
    LANES;
#pragma unroll
    for (int m = 0; m < 2; ++m)
#pragma unroll
      for (int p = 0; p < 2; ++p)
        st16_dev(make_srd(out, SEQ * DM * 2), (pm2 * 128 + wid * 32 + m * 16 + i16) * (DM * 2) + pn2 * 128 + (p * 4 + (g & 1) * 2 + (g >> 1)) * 16, chunk16(r[m][2 * p], r[m][2 * p + 1]));
  }
  if (blockIdx.x == 0 && tid == 0) __hip_atomic_store((gu64*)(ws + WS_EPOCH), tag, __ATOMIC_RELAXED, __HIP_MEMORY_SCOPE_AGENT);
  TS(17);
}

extern "C" void kernel_launch(void* const* d_in, const int* in_sizes, int n_in, void* d_out, int out_size, void* d_ws, size_t ws_size,
                              hipStream_t stream) {
  (void)hipFuncSetAttribute((const void*)k_mega, hipFuncAttributeMaxDynamicSharedMemorySize, LDS_BYTES);
  k_mega<<<256, 512, LDS_BYTES, stream>>>((const __bf16*)d_in[0], (const __bf16*)d_in[1], (const __bf16*)d_in[2], (const __bf16*)d_in[4],
                                          (const __bf16*)d_in[5], (const __bf16*)d_in[6], (const __bf16*)d_in[7], (char*)d_out, (char*)d_ws + (size_t)WSBASE * 1048576);
}
```

```cpp
#include <hip/hip_runtime.h>
#include <hip/hip_bf16.h>
#include <stdint.h>
#include <type_traits>

#define DI __device__ __forceinline__
#define LAS __attribute__((address_space(3)))
#define AI __attribute__((always_inline))
typedef __bf16 bf16x8 __attribute__((ext_vector_type(8)));
typedef __bf16 bf16x2 __attribute__((ext_vector_type(2)));
typedef short s16x4 __attribute__((ext_vector_type(4)));
typedef float f32x4 __attribute__((ext_vector_type(4)));
typedef float f32x2 __attribute__((ext_vector_type(2)));
typedef int i32x4 __attribute__((ext_vector_type(4)));
typedef unsigned u32x4 __attribute__((ext_vector_type(4)));
typedef unsigned u32x2 __attribute__((ext_vector_type(2)));
typedef unsigned u32x32 __attribute__((ext_vector_type(32)));
typedef unsigned long long u64;
typedef __attribute__((address_space(1))) u64 gu64;
template <int V> using IC = std::integral_constant<int, V>;

constexpr unsigned CODE_BYTES = 48128;
constexpr int SEQ = 2048, DM = 1024, FF = 3584, FH = 1792;
constexpr float LN_EPS = 1e-5f;

constexpr unsigned WS_EPOCH = 0;
constexpr unsigned F_S1 = 32768, F_O = 2 * 32768, F_H = 3 * 32768, F_W3 = 4 * 32768, F_Z2 = 5 * 32768, F_Y = 6 * 32768;
constexpr unsigned WS_S1R = 1u << 18;
constexpr unsigned WS_S1C = WS_S1R + (1u << 18);
constexpr unsigned WS_S2R = WS_S1C + (1u << 18);
constexpr unsigned WS_S2C = WS_S2R + (1u << 18);
constexpr unsigned WS_S3 = WS_S2C + (1u << 18);
constexpr unsigned WS_O = 2u << 20;
constexpr unsigned WS_H = WS_O + (4u << 20);
constexpr unsigned WS_Z2 = WS_H + (4u << 20);
constexpr unsigned WS_Y = WS_Z2 + (8u << 20);
constexpr unsigned WS_W3 = WS_Y + (8u << 20);
constexpr unsigned WS_END = WS_W3 + (8u << 20);

constexpr unsigned L_WT = 98304;
constexpr unsigned L_X = 129024;
constexpr unsigned L_H = 135168;
constexpr unsigned L_MISC = 153600;
constexpr unsigned L_MU = L_MISC, L_RS = L_MU + 576, L_CS = L_RS + 576, L_P = L_CS + 896, L_M3 = L_P + 2048, L_R3 = L_M3 + 512, L_TAG = L_R3 + 512,
                   L_B = L_TAG + 16;
constexpr unsigned LDS_BYTES = 163840;

DI i32x4 make_srd(const void* p, unsigned bytes) {
  u64 a = (u64)p;
  i32x4 r;
  r[0] = __builtin_amdgcn_readfirstlane((int)(unsigned)a);
  r[1] = __builtin_amdgcn_readfirstlane((int)(unsigned)(a >> 32));
  r[2] = (int)bytes;
  r[3] = 0x00020000;
  return r;
}
template <int N> DI void dmaL(i32x4 srd, unsigned v, unsigned so, unsigned l) {
  if constexpr (N == 1) asm volatile("s_mov_b32 m0, %0\n\ts_nop 0\n\tbuffer_load_dwordx4 %1, %2, %3 offen sc1 lds" :: "s"(l), "v"(v), "s"(srd), "s"(so) : "memory");
  if constexpr (N == 2) asm volatile("s_mov_b32 m0, %0\n\ts_nop 0\n\tbuffer_load_dwordx4 %1, %2, %3 offen sc1 lds\n\tbuffer_load_dwordx4 %1, %2, %3 offen offset:1024 sc1 lds" :: "s"(l), "v"(v), "s"(srd), "s"(so) : "memory");
  if constexpr (N == 4) asm volatile("s_mov_b32 m0, %0\n\ts_nop 0\n\tbuffer_load_dwordx4 %1, %2, %3 offen sc1 lds\n\tbuffer_load_dwordx4 %1, %2, %3 offen offset:1024 sc1 lds\n\t"
                                     "buffer_load_dwordx4 %1, %2, %3 offen offset:2048 sc1 lds\n\tbuffer_load_dwordx4 %1, %2, %3 offen offset:3072 sc1 lds" :: "s"(l), "v"(v), "s"(srd), "s"(so) : "memory");
}
template <int N> DI void dmaS(i32x4 srd, const unsigned* v, unsigned so, unsigned l) {
  if constexpr (N == 1) asm volatile("s_mov_b32 m0, %0\n\ts_nop 0\n\tbuffer_load_dwordx4 %1, %2, %3 offen lds" :: "s"(l), "v"(v[0]), "s"(srd), "s"(so) : "memory");
  if constexpr (N == 2) asm volatile("s_mov_b32 m0, %0\n\ts_nop 0\n\tbuffer_load_dwordx4 %1, %2, %3 offen lds\n\tbuffer_load_dwordx4 %4, %2, %3 offen offset:1024 lds" :: "s"(l), "v"(v[0]), "s"(srd), "s"(so), "v"(v[1]) : "memory");
  if constexpr (N == 4) asm volatile("s_mov_b32 m0, %0\n\ts_nop 0\n\tbuffer_load_dwordx4 %1, %2, %3 offen lds\n\tbuffer_load_dwordx4 %4, %2, %3 offen offset:1024 lds\n\t"
                                     "buffer_load_dwordx4 %5, %2, %3 offen offset:2048 lds\n\tbuffer_load_dwordx4 %6, %2, %3 offen offset:3072 lds"
                                     :: "s"(l), "v"(v[0]), "s"(srd), "s"(so), "v"(v[1]), "v"(v[2]), "v"(v[3]) : "memory");
}
DI void ld16_async(u32x4& d, i32x4 srd, unsigned voff) { asm volatile("buffer_load_dwordx4 %0, %1, %2, 0 offen sc1" : "=v"(d) : "v"(voff), "s"(srd) : "memory"); }
DI void ld8_async(u32x2& d, i32x4 srd, unsigned voff) { asm volatile("buffer_load_dwordx2 %0, %1, %2, 0 offen sc1" : "=v"(d) : "v"(voff), "s"(srd) : "memory"); }
DI void touch(unsigned& d, i32x4 srd, unsigned voff) { asm volatile("buffer_load_dword %0, %1, %2, 0 offen" : "=v"(d) : "v"(voff), "s"(srd) : "memory"); }
DI void ld16_nt(u32x4& d, i32x4 srd, unsigned voff, unsigned soff) { asm volatile("buffer_load_dwordx4 %0, %1, %2, %3 offen nt" : "=v"(d) : "v"(voff), "s"(srd), "s"(soff) : "memory"); }
DI void ld16_plain(u32x4& d, i32x4 srd, unsigned voff, unsigned soff) { asm volatile("buffer_load_dwordx4 %0, %1, %2, %3 offen" : "=v"(d) : "v"(voff), "s"(srd), "s"(soff) : "memory"); }
template <class T> DI void pin(T& v) { asm volatile("" : "+v"(v)); }
DI void st16_dev(i32x4 srd, unsigned voff, u32x4 v) { asm volatile("buffer_store_dwordx4 %0, %1, %2, 0 offen sc1\n\ts_nop 1" :: "v"(v), "v"(voff), "s"(srd) : "memory"); }
DI void st8_dev(i32x4 srd, unsigned voff, u32x2 v) { asm volatile("buffer_store_dwordx2 %0, %1, %2, 0 offen sc1" :: "v"(v), "v"(voff), "s"(srd) : "memory"); }
DI void st4_dev(i32x4 srd, unsigned voff, unsigned v) { asm volatile("buffer_store_dword %0, %1, %2, 0 offen sc1" :: "v"(v), "v"(voff), "s"(srd) : "memory"); }
template <int N> DI void wait_vm() { asm volatile("s_waitcnt vmcnt(%0)" :: "i"(N) : "memory"); }
DI void wait_lgkm0() { __builtin_amdgcn_s_waitcnt(0xC07F); asm volatile("" ::: "memory"); }
DI void bar() { asm volatile("" ::: "memory"); __builtin_amdgcn_s_barrier(); asm volatile("" ::: "memory"); }
#define ABAR() do { __builtin_amdgcn_sched_barrier(0); bar(); __builtin_amdgcn_sched_barrier(0); } while (0)
#define ABARS(N) do { _Pragma("unroll 1") for (int i_ = 0; i_ < (N); ++i_) ABAR(); } while (0)
DI unsigned pk2(float lo, float hi) { bf16x2 v = {(__bf16)lo, (__bf16)hi}; return __builtin_bit_cast(unsigned, v); }
DI u32x2 pk4(f32x4 v) { return (u32x2){pk2(v[0], v[1]), pk2(v[2], v[3])}; }
DI f32x2 unpk2(unsigned u) { return (f32x2){__uint_as_float(u << 16), __uint_as_float(u & 0xffff0000u)}; }
DI f32x4 unpk4(u32x2 u) { const f32x2 a = unpk2(u[0]), b = unpk2(u[1]); return (f32x4){a[0], a[1], b[0], b[1]}; }
DI bf16x8 cat8(u32x2 a, u32x2 b) { u32x4 v = {a[0], a[1], b[0], b[1]}; return __builtin_bit_cast(bf16x8, v); }
DI f32x4 mfma(bf16x8 a, bf16x8 b, f32x4 c) { return __builtin_amdgcn_mfma_f32_16x16x32_bf16(a, b, c, 0, 0, 0); }
DI bf16x8 lds_a(LAS char* p) { return *(LAS bf16x8*)p; }
DI u32x2 lds_tr(LAS char* p) { return __builtin_bit_cast(u32x2, __builtin_amdgcn_ds_read_tr16_b64_v4i16((LAS s16x4*)p)); }
DI int swap23(int k) { return (k & ~12) | ((k & 4) << 1) | ((k & 8) >> 1); }
DI float sum4(float x) {
  auto r = __builtin_amdgcn_permlane16_swap(__float_as_uint(x), __float_as_uint(x), false, false);
  x = __uint_as_float(r[0]) + __uint_as_float(r[1]);
  auto s = __builtin_amdgcn_permlane32_swap(__float_as_uint(x), __float_as_uint(x), false, false);
  return __uint_as_float(s[0]) + __uint_as_float(s[1]);
}
DI float bsel(unsigned m, float a, float b) { return __uint_as_float((m & __float_as_uint(a)) | (~m & __float_as_uint(b))); }
DI float vmax(float a, float b) { float r; asm("v_max_f32 %0, %1, %2" : "=v"(r) : "v"(a), "v"(b)); return r; }
DI float max4(float x) {
  auto r = __builtin_amdgcn_permlane16_swap(__float_as_uint(x), __float_as_uint(x), false, false);
  x = vmax(__uint_as_float(r[0]), __uint_as_float(r[1]));
  auto s = __builtin_amdgcn_permlane32_swap(__float_as_uint(x), __float_as_uint(x), false, false);
  return vmax(__uint_as_float(s[0]), __uint_as_float(s[1]));
}
DI u32x4 chunk16(u32x2 a, u32x2 b) {
  auto r0 = __builtin_amdgcn_permlane16_swap(a[0], b[0], false, false);
  auto r1 = __builtin_amdgcn_permlane16_swap(a[1], b[1], false, false);
  return (u32x4){r0[0], r1[0], r0[1], r1[1]};
}
DI f32x2 mean_rstd(f32x2 t, float inv_n) {
  float mu = t[0] * inv_n, ex = t[1] * inv_n;
  pin(mu); pin(ex);
  float var = fmaxf(ex - mu * mu, 0.f);
  pin(var);
  return (f32x2){mu, __builtin_amdgcn_rsqf(var + LN_EPS)};
}
DI u64 uniform64(u64 v) {
  return ((u64)(unsigned)__builtin_amdgcn_readfirstlane((int)(unsigned)(v >> 32)) << 32) | (unsigned)__builtin_amdgcn_readfirstlane((int)(unsigned)v);
}
DI int fresh_tid() { int t = threadIdx.x; asm volatile("" : "+v"(t)); return t; }
DI void set_flag(char* ws, unsigned flag_off, u64 tag) { __hip_atomic_store((gu64*)(ws + flag_off), tag, __ATOMIC_RELAXED, __HIP_MEMORY_SCOPE_AGENT); }
DI void await(char* ws, unsigned flag_off, u64 tag) {
#pragma unroll 1
  for (unsigned spin = 0; spin < (1u << 17); ++spin) {
    const u64 f = __hip_atomic_load((gu64*)(ws + flag_off), __ATOMIC_RELAXED, __HIP_MEMORY_SCOPE_AGENT);
    if (__all(f == tag)) break;
    __builtin_amdgcn_s_sleep(1);
  }
  asm volatile("" ::: "memory");
}
DI unsigned row_flags(unsigned f, int pm, int lane) { return f + (pm * 16 + (lane & 15)) * 128; }
DI unsigned col_flags(unsigned f, int pn, int lane) { return f + ((lane & 15) * 16 + pn) * 128; }
template <int J0, int J1> DI void gather(u32x4 (&d)[16], i32x4 srd, unsigned off) {
#pragma unroll
  for (int j = J0; j < J1; ++j) ld16_async(d[j], srd, off + j * 1024);
}
template <int J0, int J1> DI void accum(f32x4& t, u32x4 (&d)[16]) {
#pragma unroll
  for (int j = J0; j < J1; ++j) {
    pin(d[j]);
    if (j == 0) t = __builtin_bit_cast(f32x4, d[j]);
    else asm volatile("v_add_f32 %0, %0, %4\n\tv_add_f32 %1, %1, %5\n\tv_add_f32 %2, %2, %6\n\tv_add_f32 %3, %3, %7"
                      : "+v"(t[0]), "+v"(t[1]), "+v"(t[2]), "+v"(t[3]) : "v"(d[j][0]), "v"(d[j][1]), "v"(d[j][2]), "v"(d[j][3]));
  }
}
DI void put_rows(LAS char* lds, unsigned l_mu, unsigned l_rs, int row2, f32x4 t, float inv_n, bool on = true) {
  const f32x2 a = mean_rstd((f32x2){t[0], t[1]}, inv_n), b = mean_rstd((f32x2){t[2], t[3]}, inv_n);
  if (on) { *(LAS f32x2*)(lds + l_mu + row2 * 8) = (f32x2){a[0], b[0]}; *(LAS f32x2*)(lds + l_rs + row2 * 8) = (f32x2){a[1], b[1]}; }
}
DI unsigned await_mask(char* ws, unsigned flag_off, u64 tag, unsigned need) {
  unsigned seen = 0;
#pragma unroll 1
  for (unsigned spin = 0; spin < (1u << 17); ++spin) {
    const u64 f = __hip_atomic_load((gu64*)(ws + flag_off), __ATOMIC_RELAXED, __HIP_MEMORY_SCOPE_AGENT);
    seen = (unsigned)__ballot(f == tag) & 0xffffu;
    if ((seen & need) == need) break;
    __builtin_amdgcn_s_sleep(1);
  }
  asm volatile("" ::: "memory");
  return seen;
}

DI int b_pos_lane(int lane) { const int g = lane >> 4, q = (lane & 15) >> 2; return (g >> 1) * 16 + (g & 1) * 4 + q; }
DI unsigned b128_base(int lane, int nb) { const int pos = b_pos_lane(lane); return pos * 128 + ((nb ^ ((pos >> 1) & 3)) * 32) + (lane & 3) * 8; }
DI unsigned b224_base(int lane) { return b_pos_lane(lane) * 224 + (lane & 3) * 8; }
DI unsigned a_base(int lane, int kk) { const int r = lane & 15, g = lane >> 4; return r * 128 + (((kk * 4 + g) ^ ((r >> 1) & 7)) * 16); }
DI unsigned img_off(int row, int chunk) { return row * 128 + ((chunk ^ ((row >> 1) & 7)) * 16); }

#define SGB(mask, n) __builtin_amdgcn_sched_group_barrier(mask, n, 0)
template <int NR, int NM> DI void interleave() {
  constexpr int RP = NR > NM - 2 ? 2 : 1;
  SGB(0x8, 1);
  SGB(0x2, 8);
#pragma unroll
  for (int i = 0; i < (NR + RP - 1) / RP; ++i) { SGB(0x100, RP); SGB(0x8, 1); }
  SGB(0x8, NM);
}
DI void cbar() {
  __builtin_amdgcn_sched_barrier(0);
  __builtin_amdgcn_s_waitcnt(0xC07F);
  bar();
  __builtin_amdgcn_sched_barrier(0);
}
template <int NK, int NS, int SA, int SB, int NR, int NM, class F, class L, class M>
DI void compute_loop(L load, M mma) {
  F f0, f1;
  bar();
  load(f0, 0u, 0u, 0);
  unsigned sa = 0, sb = 0;
  __builtin_amdgcn_sched_barrier(0);
#pragma unroll 1
  for (int t = 0; t < NK - 1; ++t) {
    load(f1, sa, sb, 1);
    mma(f0);
    interleave<NR, NM>();
    cbar();
    sa += SA; sb += SB; if (sa == NS * SA) { sa = 0; sb = 0; }
    load(f0, sa, sb, 0);
    mma(f1);
    interleave<NR, NM>();
    __builtin_amdgcn_sched_barrier(0);
  }
  load(f1, sa, sb, 1);
  mma(f0);
  interleave<NR, NM>();
  cbar();
  mma(f1);
}

template <int N, int NS, int SA, int SB, int NR, int NM, class F, class L, class M>
DI void deep_segment(F& f0, unsigned& sa, unsigned& sb, L load, M mma) {
  F f1;
#pragma unroll 1
  for (int t = 0; t < N; t += 2) {
    cbar();
    sa += SA; sb += SB; if (sa == NS * SA) { sa = 0; sb = 0; }
    load(f1, sa, sb);
    mma(f0);
    interleave<NR, NM>();
    cbar();
    sa += SA; sb += SB; if (sa == NS * SA) { sa = 0; sb = 0; }
    load(f0, sa, sb);
    mma(f1);
    interleave<NR, NM>();
  }
  __builtin_amdgcn_sched_barrier(0);
}

struct Nx { unsigned a0, a1, b, f, la0, la1, lb, lf; };
template <class T> DI void spin(T& v) { asm volatile("" : "+s"(v)); }
template <int T0, int T1, int WAIT, int EARLY, class ST, class MK, class G0, class G1, class G2, class G3, class GT>
DI void steady(ST start, MK mk, G0 g0, G1 g1, G2 g2, G3 g3, GT gt) {
  auto keep = [](Nx& q) AI { spin(q.a0); spin(q.a1); spin(q.b); spin(q.f); spin(q.la0); spin(q.la1); spin(q.lb); spin(q.lf); };
  Nx c = mk(T0 + 4), n;
  keep(c);
  start();
  auto head = [&](auto W, Nx& p, Nx& q, int u) AI {
    wait_vm<decltype(W)::value>();
    bar();
    g0(p.a0, p.la0);
    q = mk(u);
    keep(q);
    gt(u);
  };
  auto rest = [&](Nx& p) AI { g1(p.b, p.lb); g2(p.a1, p.la1); g3(p.f, p.lf); };
  head(IC<EARLY>{}, c, n, T0 + 5); rest(c);
  head(IC<EARLY>{}, n, c, T0 + 6); rest(n);
  head(IC<WAIT>{}, c, n, T0 + 7);
#pragma unroll 1
  for (int u = T0 + 8; u < T1 + 4; u += 2) { rest(c); head(IC<WAIT>{}, n, c, u); rest(n); head(IC<WAIT>{}, c, n, u + 1); }
  rest(c); head(IC<WAIT>{}, n, c, T1 + 4); rest(n);
}
DI int rot(int t, int r0, int nk) { const int k = t + r0; return k >= nk ? k - nk : k; }

namespace g1 { constexpr int NK = 16, NS = 3, AB = 144 * 128, ST = AB + 3 * 8192; struct Fr { bf16x8 a[5]; u32x2 b[6][2]; }; }
namespace gs { constexpr int NS = 4, AB = 16384, ST = AB + 8192; struct Fr { bf16x8 a[2][2]; u32x2 b[2][4][2]; }; }
namespace gf { constexpr int NS = 4, AB = 16384, SEG = 14336, B0 = NS * AB; struct Fr { bf16x8 a[2][2]; bf16x8 b[2][7]; }; }

__global__ __launch_bounds__(512, 2) void k_mega(const __bf16* __restrict__ x, const __bf16* __restrict__ wqkv, const __bf16* __restrict__ wo,
                                                 const __bf16* __restrict__ win, const __bf16* __restrict__ wsp, const __bf16* __restrict__ bsp,
                                                 const __bf16* __restrict__ wout, char* __restrict__ out, char* __restrict__ ws) {
  extern __shared__ __attribute__((aligned(16))) char smem_[];
  LAS char* lds = (LAS char*)smem_;
  const int tid = threadIdx.x, lane = tid & 63, wid = __builtin_amdgcn_readfirstlane(tid >> 6);
  const int xcd = blockIdx.x & 7, idx = blockIdx.x >> 3;
  const int pm1 = (idx & 7) * 2 + (xcd & 1), pn1 = (xcd >> 1) * 4 + (idx >> 3);
  const int pm2 = (idx & 3) * 4 + (xcd & 3), pn2 = (idx >> 2) * 2 + (xcd >> 2);
  const i32x4 srdW = make_srd(ws, WS_END);
  u64 pc0;
  asm volatile("s_getpc_b64 %0" : "=s"(pc0));
  const i32x4 srdC = make_srd((const void*)(pc0 & ~127ull), CODE_BYTES);
  const unsigned code_off = lane < 12 ? (idx * 12 + lane) * 128 : 0xfffffff0u;
  if (wid == 1) { unsigned v; touch(v, srdC, code_off); wait_vm<0>(); pin(v); }

  const bf16x8 ones = __builtin_bit_cast(bf16x8, (u32x4){0x3f803f80u, 0x3f803f80u, 0x3f803f80u, 0x3f803f80u});
  const f32x4 zero4 = {0.f, 0.f, 0.f, 0.f};
  const int r1 = (((xcd >> 1) + (xcd & 1) * 2 + (idx & 1)) & 3) * 4;
  const int srow = idx >> 3, scol = (idx & 7) >> 1;
  auto rowstat = [&](bf16x8 a0, bf16x8 a1) AI {
    const f32x4 sm = mfma(ones, a1, mfma(ones, a0, zero4)), sq = mfma(a1, a1, mfma(a0, a0, zero4));
    return (f32x2){sm[0], (lane & 2) ? ((lane & 1) ? sq[3] : sq[2]) : ((lane & 1) ? sq[1] : sq[0])};
  };
  const bool diag_lane = ((lane & 15) >> 2) == (lane >> 4);
  auto g1_first_steps = [&](int hw, auto between) AI {
    using namespace g1;
#pragma unroll 1
    for (int t = 0; t < 4; ++t) {
      bf16x8 ra[2][2]; u32x2 rb[3][4];
      const unsigned so = (t == 3 ? 0 : t) * ST;
      if (t == srow) {
#pragma unroll
        for (int m = 0; m < 2; ++m)
#pragma unroll
          for (int kk = 0; kk < 2; ++kk) ra[m][kk] = lds_a(lds + so + a_base(lane, kk) + (hw * 2 + m) * 2048);
      }
      if (t == scol) {
#pragma unroll
        for (int i = 0; i < 3; ++i) {
          const int n = hw * 3 + i;
          const unsigned cb = so + AB + (n >> 2) * 8192 + b128_base(lane, n & 3);
#pragma unroll
          for (int j = 0; j < 4; ++j) rb[i][j] = lds_tr(lds + cb + ((j >> 1) * 32 + (j & 1) * 8) * 128);
        }
      }
      wait_lgkm0();
      between(t);
      if (t == srow) {
        const f32x2 r0 = rowstat(ra[0][0], ra[0][1]), r1 = rowstat(ra[1][0], ra[1][1]);
        if (diag_lane) { *(LAS f32x2*)(lds + L_P + (hw * 32 + (lane & 15)) * 8) = r0; *(LAS f32x2*)(lds + L_P + (hw * 32 + 16 + (lane & 15)) * 8) = r1; }
      }
      if (t == scol) {
#pragma unroll
        for (int i = 0; i < 3; ++i)
          *(LAS float*)(lds + L_P + 1024 + ((hw * 3 + i) * 16 + (lane & 15)) * 4) =
              mfma(ones, cat8(rb[i][2], rb[i][3]), mfma(ones, cat8(rb[i][0], rb[i][1]), zero4))[0];
      }
    }
    wait_lgkm0();
  };

  if (wid == 7) {
    const unsigned l16 = lane * 16;
    unsigned vw[14];
    {
      int pos = lane / 14, gi = lane - pos * 14;
#pragma unroll
      for (int i = 0; i < 14; ++i) {
        vw[i] = swap23(pos) * (FF * 2) + (pn1 * 112 + gi * 8) * 2;
        pin(vw[i]);
        gi += 8; pos += 4; if (gi >= 14) { gi -= 14; pos += 1; }
      }
    }
    ABAR();
    const u64 tag = uniform64(*(LAS u64*)(lds + L_TAG));
    const u32x2 tag2 = __builtin_bit_cast(u32x2, tag);
    const unsigned myflag = lane < 16 ? (pm1 * 16 + pn1) * 128 + lane * 8 : 0xff000000u;
    g1_first_steps(3, [&](int) { ABAR(); });
    ABAR();
    st16_dev(srdW, WS_S1R + (pm1 * 16 + ((srow + r1) & 15)) * 1024 + l16, *(LAS u32x4*)(lds + L_P + l16));
    st16_dev(srdW, WS_S1C + (pn1 * 16 + ((scol + r1) & 15)) * 1024 + l16, *(LAS u32x4*)(lds + L_P + 1024 + l16));
    st16_dev(srdW, WS_S3 + (pm1 * 16 + pn1) * 1024 + l16, (u32x4){~0u, ~0u, ~0u, ~0u});
    u32x4 wt[28];
    const i32x4 srdWin = make_srd(win, DM * FF * 2);
    auto win_half = [&](int h) AI {
#pragma unroll
      for (int i = 0; i < 14; ++i) ld16_nt(wt[h * 14 + i], srdWin, vw[i], pm1 * (64u * FF * 2) + h * (FH * 2));
    };
    ABARS(2);
    wait_vm<0>();
    st8_dev(srdW, F_S1 + myflag, tag2);
    unsigned tch[3];
    {
      const i32x4 srdO = make_srd(wout, FH * DM * 2);
#pragma unroll
      for (int i = 0; i < 2; ++i) {
        const int k = (idx & 3) * 128 + i * 64 + lane;
        touch(tch[i], srdO, k < 448 ? ((xcd & 3) * 448 + k) * (DM * 2) + pn2 * 128 : 0xfffffff0u);
      }
    }
    touch(tch[2], make_srd(bsp, SEQ * 2), pm1 * 256 + (lane & 1) * 128);
    ABARS(2);
    {
      const int pmh = pm1 > 0 ? pm1 - 1 : 0;
      const unsigned fo = lane < 16 ? row_flags(F_S1, pm1, lane) : lane < 32 ? row_flags(F_S1, pmh, lane) : col_flags(F_S1, pn1, lane);
      u32x2 f0;
      ld8_async(f0, srdW, fo);
      ABAR();
      wait_vm<0>();
      pin(f0); pin(tch[0]); pin(tch[1]); pin(tch[2]);
      if (!__all(__builtin_bit_cast(u64, f0) == tag)) await(ws, fo, tag);
      const unsigned ro = WS_S1R + pm1 * 16384 + l16, co = lane < 48 ? WS_S1C + pn1 * 16384 + l16 : WS_S1R + pmh * 16384 + (56 + (lane & 7)) * 16;
      u32x4 gr[16], gc[16];
      f32x4 tr, tc;
      gather<0, 12>(gr, srdW, ro);
      ABAR();
      gather<12, 16>(gr, srdW, ro);
      gather<0, 8>(gc, srdW, co);
      ABAR();
      gather<8, 16>(gc, srdW, co);
      wait_vm<16>();
      accum<0, 16>(tr, gr);
      ABAR();
      put_rows(lds, L_MU, L_RS, 8 + lane, tr, 1.f / DM);
      wait_vm<0>();
      accum<0, 16>(tc, gc);
      ABAR();
      if (lane < 48) *(LAS f32x4*)(lds + L_CS + l16) = tc;
      put_rows(lds, L_MU, L_RS, lane & 7, tc, 1.f / DM, lane >= 48 && lane < 56);
      wait_lgkm0();
    }
    {
      u32x4 xt[16];
      const i32x4 srdX = make_srd(x, SEQ * DM * 2);
      ABAR();
      win_half(0);
      ABAR();
      win_half(1);
      ABAR();
#pragma unroll
      for (int i = 0; i < 16; ++i) {
        const int r = i * 8 + (lane >> 3);
        ld16_plain(xt[i], srdX, (lane >> 3) * (DM * 2) + (((lane & 7) ^ ((r >> 1) & 7)) * 16), (pm2 * 128 + i * 8) * (DM * 2) + pn2 * 128);
      }
      ABAR();
      wait_vm<16>();
#pragma unroll
      for (int i = 0; i < 28; ++i) { pin(wt[i]); *(LAS u32x4*)(lds + L_WT + i * 1024 + l16) = wt[i]; }
      wait_vm<0>();
#pragma unroll
      for (int i = 0; i < 16; ++i) { pin(xt[i]); *(LAS u32x4*)(lds + L_H + i * 1024 + l16) = xt[i]; }
    }
    {
      const unsigned cb = L_WT + b224_base(lane);
      bf16x8 fr[2][2][2];
      float cs4[4];
      auto rd2 = [&](int q) AI {
#pragma unroll
        for (int u = 0; u < 2; ++u) {
          const int n = q * 2 + u;
          LAS char* p = lds + cb + (n / 7) * 14336 + (n % 7) * 32;
          fr[q & 1][u][0] = cat8(lds_tr(p), lds_tr(p + 8 * 224));
          fr[q & 1][u][1] = cat8(lds_tr(p + 32 * 224), lds_tr(p + 40 * 224));
        }
      };
      rd2(0);
      wait_lgkm0();
      ABAR();
#pragma unroll
      for (int q = 0; q < 7; ++q) {
        ABAR();
#pragma unroll
        for (int u = 0; u < 2; ++u) {
          const int n = q * 2 + u;
#pragma unroll
          for (int kk = 0; kk < 2; ++kk)
            st16_dev(srdW, WS_W3 + (pn1 * 16 + pm1) * 28672 + (n / 7) * 14336 + (kk * 7 + n % 7) * 1024 + l16, __builtin_bit_cast(u32x4, fr[q & 1][u][kk]));
          cs4[n & 3] = mfma(ones, fr[q & 1][u][1], mfma(ones, fr[q & 1][u][0], zero4))[0];
        }
        if (q < 6) rd2(q + 1);
        if ((q & 1) || q == 6)
          st4_dev(srdW, WS_S2C + (pn1 * 16 + pm1) * 1024 + (q >> 1) * 256 + lane * 4,
                  __float_as_uint(bsel(lane < 32 ? ~0u : 0u, bsel(lane < 16 ? ~0u : 0u, cs4[0], cs4[1]), bsel(lane < 48 ? ~0u : 0u, cs4[2], cs4[3]))));
      }
    }
    ABARS(4);
    wait_vm<0>();
    st8_dev(srdW, F_W3 + myflag, tag2);
    ABARS(5);
    {
      u32x2 f0;
      const unsigned fo = col_flags(F_W3, pn1, lane);
      ld8_async(f0, srdW, fo);
      ABAR();
      wait_vm<0>();
      pin(f0);
      if (!__all(__builtin_bit_cast(u64, f0) == tag)) await(ws, fo, tag);
    }
    {
      u32x4 gc[16];
      f32x4 tc;
      ABAR();
      gather<0, 16>(gc, srdW, WS_S2C + pn1 * 16384 + l16);
      wait_vm<0>();
      accum<0, 16>(tc, gc);
      if (lane < 56) *(LAS f32x4*)(lds + L_CS + l16) = tc;
    }
    ABARS(6);
    {
      u32x4 gr[16];
      f32x4 tr;
      const unsigned ro = WS_S2R + pm1 * 16384 + l16;
      ABAR();  gather<0, 4>(gr, srdW, ro);
      ABAR();  gather<4, 8>(gr, srdW, ro);
      ABAR();  gather<8, 12>(gr, srdW, ro);
      ABAR();  gather<12, 16>(gr, srdW, ro); wait_vm<8>(); accum<0, 8>(tr, gr);
      ABARS(2);
      wait_vm<0>(); accum<8, 16>(tr, gr);
      ABAR();
      put_rows(lds, L_MU, L_RS, lane, tr, 1.f / DM);
      wait_lgkm0();
    }
    ABARS(5);
    {
      const i32x4 srdSp = make_srd(wsp, SEQ * SEQ * 2);
#pragma unroll
      for (int i = 0; i < 2; ++i) touch(tch[i], srdSp, (pm1 * 128 + i * 64 + lane) * (SEQ * 2) + ((xcd >> 1) * 8 + 4 + (idx >> 3)) * 128);
    }
    {
      const unsigned v = lane < 16 ? pm1 * 256 + l16 : 0xfffffff0u;
      dmaS<1>(make_srd(bsp, SEQ * 2), &v, 0u, L_B);
    }
    ABARS(2);
    {
      u32x4 gv[16];
      f32x4 t;
      unsigned mx = 0;
      const unsigned so = WS_S3 + pm1 * 16384 + l16;
#pragma unroll
      for (int j = 0; j < 16; ++j) asm volatile("buffer_load_dwordx4 %0, %1, %2, %3 offen sc1" : "=v"(gv[j]) : "v"(so), "s"(srdW), "s"(j * 1024) : "memory");
      ABAR();
      wait_vm<8>();
      pin(tch[0]); pin(tch[1]);
      accum<0, 8>(t, gv);
#pragma unroll
      for (int j = 0; j < 8; ++j) mx = max(max(mx, gv[j][1]), gv[j][3]);
      pin(mx);
      ABAR();
      wait_vm<0>();
      accum<8, 16>(t, gv);
#pragma unroll
      for (int j = 8; j < 16; ++j) mx = max(max(mx, gv[j][1]), gv[j][3]);
      if (!__all(mx != ~0u)) {
#pragma unroll 1
        for (unsigned spin = 0; spin < (1u << 17); ++spin) {
          u32x4 gw[16];
          gather<0, 16>(gw, srdW, so);
          wait_vm<0>();
          accum<0, 16>(t, gw);
          mx = 0;
#pragma unroll
          for (int j = 0; j < 16; ++j) mx = max(max(mx, gw[j][1]), gw[j][3]);
          if (__all(mx != ~0u)) break;
        }
      }
      ABAR();
      put_rows(lds, L_M3, L_R3, lane, t, 1.f / FH);
      wait_lgkm0();
    }
    ABARS(6);
    st8_dev(srdW, F_Z2 + myflag, tag2);
    ABARS(2);
    {
      u32x2 f0;
      const unsigned fo = col_flags(F_Z2, pn1, lane);
      ld8_async(f0, srdW, fo);
      ABAR();
      wait_vm<0>();
      pin(f0);
      const bool up = __all(__builtin_bit_cast(u64, f0) == tag);
      *(LAS unsigned*)(lds + L_TAG + 8) = up;
      wait_lgkm0();
      ABAR();
      if (!up) await(ws, fo, tag);
      ABAR();
    }
    ABARS(64);
    return;
  }

  if (wid >= 4) {
    const int dw = wid - 4;
    const unsigned l16 = lane * 16;
    u64 tag;
    unsigned seen = 0;
    auto gate = [&](unsigned fo, unsigned need) AI { if ((seen & need) != need) seen = await_mask(ws, fo, tag, need); };
    auto phase_start = [&](auto& ix, unsigned fo, auto mask) AI {
      seen = dw == 0 ? 0u : 0xffffu;
      gate(fo, mask(0) | mask(1) | mask(2) | mask(3));
      bar();
      ix(0, 0); ix(1, 1);
      wait_vm<5>();
      bar();
      ix(2, 2); ix(3, 3);
      gate(fo, mask(4));
    };
    auto tail4 = [&](auto PW) AI {
      wait_vm<2 * decltype(PW)::value>(); bar();
      wait_vm<decltype(PW)::value>(); bar();
      wait_vm<0>(); bar();
      bar();
    };
    {
      using namespace g1;
      const i32x4 srdA = make_srd(x, SEQ * DM * 2), srdB = make_srd(wqkv, DM * 3 * DM * 2);
      unsigned va[6], vb[8];
#pragma unroll
      for (int i = 0; i < 8; ++i) {
        const int p = dw * 8 + i, pos = (p & 7) * 8 + (lane >> 3), nb = ((lane & 7) >> 1) ^ ((pos >> 1) & 3);
        vb[i] = swap23(pos) * (3 * DM * 2) + (dw * DM + pn1 * 64 + nb * 16 + (lane & 1) * 8) * 2 - (i & 3) * 1024;
      }
      dmaS<4>(srdB, vb, r1 * (64u * 3 * DM * 2), AB + dw * 8192u);
      dmaS<4>(srdB, vb + 4, r1 * (64u * 3 * DM * 2), AB + dw * 8192u + 4096u);
      __builtin_amdgcn_sched_barrier(0);
#pragma unroll
      for (int i = 0; i < 6; ++i) {
        const int r = (dw * 6 + i) * 8 + (lane >> 3);
        int grow = r < 128 ? pm1 * 128 + r : pm1 * 128 - 16 + (r - 128);
        grow = grow < 0 ? 8 : grow;
        va[i] = grow * (DM * 2) + (((lane & 7) ^ ((r >> 1) & 7)) * 16) - (i & 3) * 1024;
      }
      auto ia = [&](int t, int s) {
        const unsigned kt = (t + r1) & 15;
        dmaS<4>(srdA, va, kt * 128u, s * ST + dw * 6144u);
        dmaS<2>(srdA, va + 4, kt * 128u, s * ST + dw * 6144u + 4096u);
      };
      auto ib = [&](int t, int s) {
        const unsigned kt = (t + r1) & 15;
        dmaS<4>(srdB, vb, kt * (64u * 3 * DM * 2), s * ST + AB + dw * 8192u);
        dmaS<4>(srdB, vb + 4, kt * (64u * 3 * DM * 2), s * ST + AB + dw * 8192u + 4096u);
      };
      ia(0, 0); ib(1, 1);
      wait_vm<8>();
      bar();
      ia(1, 1); ia(2, 2); ib(2, 2);
      tag = uniform64(*(LAS u64*)(lds + L_TAG));
      int slot = 0;
      auto step = [&](int t) AI {
        wait_vm<14>();
        bar();
        ib(t + NS, slot); ia(t + NS, slot);
        slot = slot + 1 == NS ? 0 : slot + 1;
      };
      g1_first_steps(dw, step);
#pragma unroll 1
      for (int t = 4; t < NK - NS; ++t) step(t);
      wait_vm<14>(); bar();
      wait_vm<0>(); bar();
      bar();
    }
    auto lin16 = [&](unsigned src, unsigned dst) AI {
      const unsigned g0 = dw * 4096u, g1 = 12288u + dw * 1024u;
      dmaL<4>(srdW, l16, src + g0, dst + g0);
      if (dw == 2) dmaL<2>(srdW, l16, src + g1, dst + g1);
      else dmaL<1>(srdW, l16, src + g1, dst + g1);
    };
    unsigned vbs[3];
#pragma unroll
    for (int i = 0; i < 3; ++i) {
      const int pos = (i < 2 ? dw * 2 + i : (6 + dw) & 7) * 8 + (lane >> 3), nb = ((lane & 7) >> 1) ^ ((pos >> 1) & 3);
      vbs[i] = swap23(pos) * (DM * 2) + (pn2 * 64 + nb * 16 + (lane & 1) * 8) * 2 - (i == 1 ? 1024 : 0);
    }
    auto w8 = [&](i32x4 srd, unsigned kt, int s) AI {
      dmaS<2>(srd, vbs, kt * (64u * DM * 2), s * gs::ST + gs::AB + dw * 2048u);
      if (dw != 2) dmaS<1>(srd, vbs + 2, kt * (64u * DM * 2), s * gs::ST + gs::AB + (6 + dw) * 1024u);
    };
    const unsigned vflex = dw == 2 ? l16 : vbs[2];
    auto gs_phase = [&](auto T1, auto& ix, i32x4 srdB, unsigned abase, int r0, int nk, unsigned fo, auto mask) AI {
      i32x4 srdF;
#pragma unroll
      for (int e = 0; e < 4; ++e) srdF[e] = dw == 2 ? srdW[e] : srdB[e];
      steady<0, decltype(T1)::value, 16, 10>(
          [&]() AI { phase_start(ix, fo, mask); },
          [&](int u) AI {
            const unsigned kt = rot(u, r0, nk), a = abase + kt * 16384u, b = kt * (64u * DM * 2), l = (u & 3) * gs::ST;
            return Nx{a + dw * 4096u, a + 12288u + dw * 1024u, b, dw == 2 ? a + 15360u : b,
                      l + dw * 4096u, l + 12288u + dw * 1024u, l + gs::AB + dw * 2048u, l + (dw == 2 ? 15360u : gs::AB + (6 + dw) * 1024u)};
          },
          [&](unsigned so, unsigned l) AI { dmaL<4>(srdW, l16, so, l); }, [&](unsigned so, unsigned l) AI { dmaS<2>(srdB, vbs, so, l); },
          [&](unsigned so, unsigned l) AI { dmaL<1>(srdW, l16, so, l); }, [&](unsigned so, unsigned l) AI { dmaL<1>(srdF, vflex, so, l); },
          [&](int u) AI { if (__builtin_expect(seen != 0xffffu, 0) && u < nk) gate(fo, mask(u)); });
      tail4(IC<8>{});
    };
    {
      using namespace gs;
      constexpr int NK = 16;
      const i32x4 srdB = make_srd(wo, DM * DM * 2);
      const int r0 = (xcd & 3) * 4;
      auto ix = [&](int t, int s) { lin16(WS_O + (pm2 * 16 + ((t + r0) & 15)) * 16384u, s * ST); };
      auto iw = [&](int t, int s) { w8(srdB, (t + r0) & 15, s); };
      bar();
      bar();
      iw(0, 0); iw(1, 1); iw(2, 2); iw(3, 3);
      auto mask = [&](int u) AI { return 1u << ((u + r0) & 15); };
      gs_phase(IC<NK - NS>{}, ix, srdB, WS_O + pm2 * 16 * 16384u, r0, NK, row_flags(F_O, pm2, lane), mask);
    }
    {
      using namespace gf;
      const i32x4 srdSp = make_srd(wsp, SEQ * SEQ * 2);
      auto lin14 = [&](unsigned src, int s) AI {
        const unsigned dst = B0 + s * SEG;
        dmaL<4>(srdW, l16, src + dw * 4096u, dst + dw * 4096u);
        if (dw != 2) dmaL<1>(srdW, l16, src + (12 + dw) * 1024u, dst + (12 + dw) * 1024u);
      };
      auto ia3 = [&](int t, int s) { lin16(WS_H + (pm1 * 16 + (t & 15)) * 16384u, s * AB); };
      auto ib3 = [&](int t, int s) { lin14(WS_W3 + (pn1 * 16 + (t & 15)) * 28672u + (t < 16 ? 14336u : 0u), s); };
      unsigned va4[6];
#pragma unroll
      for (int i = 0; i < 6; ++i) { const int r = (i < 4 ? dw * 4 + i : 8 + dw + i) * 8 + (lane >> 3); va4[i] = (pm1 * 128 + r) * (SEQ * 2) + (((lane & 7) ^ ((r >> 1) & 7)) * 16) - (i & 3) * 1024; }
      const int r4 = (xcd >> 1) * 8;
      auto ia4 = [&](int t, int s) {
        const unsigned kt = (t + r4) & 31;
        dmaS<4>(srdSp, va4, kt * 128u, s * AB + dw * 4096u);
        if (dw == 2) dmaS<2>(srdSp, va4 + 4, kt * 128u, s * AB + 14 * 1024u);
        else dmaS<1>(srdSp, va4 + 4, kt * 128u, s * AB + (12 + dw) * 1024u);
      };
      auto ib4 = [&](int t, int s) { lin14(WS_Z2 + (pn1 * 32 + ((t + r4) & 31)) * (unsigned)SEG, s); };
      bar();
      ib3(0, 0); ib3(1, 1); ib3(2, 2); ib3(3, 3);
      auto L4 = [&](unsigned so, unsigned l) AI { dmaL<4>(srdW, l16, so, l); };
      auto L1 = [&](unsigned so, unsigned l) AI { dmaL<1>(srdW, l16, so, l); };
      steady<0, 28, 20, 10>(
          [&]() AI { phase_start(ia3, row_flags(F_H, pm1, lane), [](int u) AI { return 1u << u; }); },
          [&](int u) AI {
            const unsigned a = WS_H + (pm1 * 16 + (u & 15)) * 16384u, b = WS_W3 + (pn1 * 16 + (u & 15)) * 28672u + (u < 16 ? 14336u : 0u);
            const unsigned la = (u & 3) * AB, lb = B0 + (u & 3) * SEG;
            return Nx{a + dw * 4096u, a + 12288u + dw * 1024u, b + dw * 4096u, dw == 2 ? a + 15360u : b + 12288u + dw * 1024u,
                      la + dw * 4096u, la + 12288u + dw * 1024u, lb + dw * 4096u, dw == 2 ? la + 15360u : lb + 12288u + dw * 1024u};
          },
          L4, L4, L1, L1,
          [&](int u) AI { if (__builtin_expect(seen != 0xffffu, 0)) gate(row_flags(F_H, pm1, lane), u < 8 ? 1u << u : 0xffffu); });
      i32x4 srdF;
#pragma unroll
      for (int e = 0; e < 4; ++e) srdF[e] = dw == 2 ? srdSp[e] : srdW[e];
      const unsigned vf4 = dw == 2 ? va4[5] + 1024u : l16;
      steady<32, 60, 20, 14>(
          [&]() AI {
            wait_vm<20>(); bar(); ia4(32, 0);
            wait_vm<15>(); bar(); ia4(33, 1);
            wait_vm<10>(); bar();
            const bool early = __builtin_amdgcn_readfirstlane(*(LAS unsigned*)(lds + L_TAG + 8)) != 0;
            if (early) { ib4(32, 0); ib4(33, 1); }
            ia4(34, 2);
            bar();
            if (!early) { ib4(32, 0); ib4(33, 1); }
            ib4(34, 2); ia4(35, 3); ib4(35, 3);
            wait_vm<18>(); bar();
          },
          [&](int u) AI {
            const unsigned kt = (u + r4) & 31, a = kt * 128u, b = WS_Z2 + (pn1 * 32 + kt) * (unsigned)SEG;
            const unsigned la = (u & 3) * AB, lb = B0 + (u & 3) * SEG;
            return Nx{a, a, b + dw * 4096u, dw == 2 ? a : b + 12288u + dw * 1024u,
                      la + dw * 4096u, la + 12288u + dw * 1024u, lb + dw * 4096u, dw == 2 ? la + 15360u : lb + 12288u + dw * 1024u};
          },
          [&](unsigned so, unsigned l) AI { dmaS<4>(srdSp, va4, so, l); }, L4,
          [&](unsigned so, unsigned l) AI { dmaS<1>(srdSp, va4 + 4, so, l); }, [&](unsigned so, unsigned l) AI { dmaL<1>(srdF, vf4, so, l); }, [](int) AI {});
      tail4(IC<10>{});
    }
    {
      using namespace gs;
      constexpr int NK = 28;
      const i32x4 srdB = make_srd(wout, FH * DM * 2);
      const int r0 = (xcd & 3) * 7;
      auto ix = [&](int t, int s) { lin16(WS_Y + (pm2 * 28 + rot(t, r0, NK)) * 16384u, s * ST); };
      auto iw = [&](int t, int s) { w8(srdB, rot(t, r0, NK), s); };
      iw(0, 0); iw(1, 1);
      bar();
      iw(2, 2); iw(3, 3);
      auto mask = [&](int u) AI { const int k = rot(u, r0, NK); return (1u << (k * 8 / 14)) | (1u << ((k * 8 + 7) / 14)); };
      gs_phase(IC<NK - NS>{}, ix, srdB, WS_Y + pm2 * 28 * 16384u, r0, NK, row_flags(F_Y, pm2, lane), mask);
    }
    return;
  }

  const u64 tag = uniform64(__hip_atomic_load((gu64*)(ws + WS_EPOCH), __ATOMIC_RELAXED, __HIP_MEMORY_SCOPE_AGENT)) + 1;
  if (tid == 0) *(LAS u64*)(lds + L_TAG) = tag;
  wait_lgkm0();
#define LANES const int tid = fresh_tid(), lane = tid & 63, i16 = lane & 15, g = lane >> 4; (void)i16; (void)g
  auto publish = [&](unsigned flag) {
    wait_vm<0>();
    bar();
    if (threadIdx.x < 16) set_flag(ws, flag + threadIdx.x * 8, tag);
    bar();
  };
  {
    using namespace g1;
    LANES;
    const int hd = wid & 1, rh = wid >> 1;
    unsigned ab[2], hb[2], bb[2];
#pragma unroll
    for (int kk = 0; kk < 2; ++kk) { ab[kk] = a_base(lane, kk) + rh * 64 * 128; hb[kk] = a_base(lane, kk) + 128 * 128; bb[kk] = AB + b128_base(lane, hd * 2 + kk); }
    f32x4 aq[4][2], ak[4][2], av[4][2], ah[2];
#pragma unroll
    for (int m = 0; m < 4; ++m)
#pragma unroll
      for (int j = 0; j < 2; ++j) aq[m][j] = ak[m][j] = av[m][j] = zero4;
    ah[0] = ah[1] = zero4;
    auto load = [&](Fr& f, unsigned so, unsigned, int kk) {
#pragma unroll
      for (int m = 0; m < 4; ++m) f.a[m] = lds_a(lds + so + ab[kk] + m * 2048);
      f.a[4] = lds_a(lds + so + hb[kk]);
#pragma unroll
      for (int s = 0; s < 3; ++s)
#pragma unroll
        for (int j = 0; j < 2; ++j)
#pragma unroll
          for (int hh = 0; hh < 2; ++hh) f.b[s * 2 + j][hh] = lds_tr(lds + so + bb[j] + s * 8192 + (kk * 32 + hh * 8) * 128);
    };
    auto mma2 = [&](Fr& f, auto RH) {
      bf16x8 b[6];
#pragma unroll
      for (int i = 0; i < 6; ++i) b[i] = cat8(f.b[i][0], f.b[i][1]);
#pragma unroll
      for (int m = 0; m < 4; ++m)
#pragma unroll
        for (int j = 0; j < 2; ++j) {
          aq[m][j] = mfma(b[j], f.a[m], aq[m][j]);
          ak[m][j] = mfma(b[2 + j], f.a[m], ak[m][j]);
          av[m][j] = mfma(f.a[m], b[4 + j], av[m][j]);
        }
#pragma unroll
      for (int j = 0; j < 2; ++j) {
        if constexpr (decltype(RH)::value == 0) ah[j] = mfma(b[2 + j], f.a[4], ah[j]);
        else ah[j] = mfma(f.a[4], b[4 + j], ah[j]);
      }
    };
    const int ra = i16 & 1;
    const bool curA = i16 - 4 * g - ra >= 0, curB = i16 - 4 * g - ra - 2 >= 0;
    LAS char* t16 = lds + L_MU + (16 + rh * 64 + i16) * 4;
    LAS char* t4g = lds + L_MU + (16 + rh * 64 + 4 * g) * 4;
    LAS char* tka = lds + L_RS + (rh * 64 + 4 * g + ra + (curA ? 16 : 0)) * 4;
    LAS char* tkb = lds + L_RS + (rh * 64 + 4 * g + ra + 2 + (curB ? 16 : 0)) * 4;
    LAS char* xch = lds + L_X + hd * 3072 + lane * 16;
    unsigned oaddr = WS_O + (pm1 * 16 + pn1) * 16384 + (hd * 4 + (g >> 1)) * 2048 + (rh * 64 + (g & 1) * 16 + i16) * 16;
    unsigned mA = curA ? ~0u : 0u, mB = curB ? ~0u : 0u;
    pin(t16); pin(t4g); pin(tka); pin(tkb); pin(xch); pin(oaddr); pin(mA); pin(mB);
    if (rh == 0) compute_loop<NK, NS, ST, ST, 17, 26, Fr>(load, [&](Fr& f) { mma2(f, IC<0>{}); });
    else compute_loop<NK, NS, ST, ST, 17, 26, Fr>(load, [&](Fr& f) { mma2(f, IC<1>{}); });
    f32x4 csq[2], csk[2]; float csv[2];
    const int co = L_CS - L_MU + (hd * 32 - 16 - rh * 64) * 4;
#pragma unroll
    for (int j = 0; j < 2; ++j) {
      csq[j] = *(LAS f32x4*)(t4g + co + j * 64);
      csk[j] = *(LAS f32x4*)(t4g + co + 256 + j * 64);
      csv[j] = *(LAS float*)(t16 + co + 512 + j * 64);
    }
    bf16x8 Q[4], K[5]; u32x2 V[5][2];
    float rq[4], rka[4], rkb[4];
#pragma unroll
    for (int m = 3; m >= 0; --m) {
      const float mu = *(LAS float*)(t16 + m * 64);
      const f32x4 mu4 = *(LAS f32x4*)(t4g + m * 64);
      rq[m] = *(LAS float*)(t16 + (L_RS - L_MU) + m * 64);
      rka[m] = *(LAS float*)(tka + m * 64);
      rkb[m] = *(LAS float*)(tkb + m * 64);
      K[m + 1] = cat8(pk4(ak[m][0] - mu * csk[0]), pk4(ak[m][1] - mu * csk[1]));
      V[m + 1][0] = pk4(av[m][0] - mu4 * csv[0]);
      V[m + 1][1] = pk4(av[m][1] - mu4 * csv[1]);
      Q[m] = cat8(pk4(aq[m][0] - mu * csq[0]), pk4(aq[m][1] - mu * csq[1]));
    }
    if (rh == 0) {
      const float mu = *(LAS float*)(t16 - 64);
      K[0] = cat8(pk4(ah[0] - mu * csk[0]), pk4(ah[1] - mu * csk[1]));
      *(LAS bf16x8*)(xch) = K[4];
      *(LAS bf16x8*)(xch + 1024) = cat8(V[4][0], V[4][1]);
    } else {
      const f32x4 mu4 = *(LAS f32x4*)(t4g - 320);
      *(LAS bf16x8*)(xch + 2048) = cat8(pk4(ah[0] - mu4 * csv[0]), pk4(ah[1] - mu4 * csv[1]));
      K[0] = K[4];
    }
    wait_lgkm0(); bar();
    {
      u32x4 v = *(LAS u32x4*)(xch + (rh == 0 ? 2048 : 1024));
      V[0][0] = (u32x2){v[0], v[1]}; V[0][1] = (u32x2){v[2], v[3]};
      if (rh == 1) K[0] = *(LAS bf16x8*)(xch);
    }
    constexpr float SC = 0.17677669529663687f * 1.4426950408889634f;
    f32x4 sp[4], sc[4];
#pragma unroll
    for (int m = 3; m >= 0; --m) { sc[m] = mfma(K[m + 1], Q[m], zero4); sp[m] = mfma(K[m], Q[m], zero4); }
    const bool nohalo = pm1 == 0 && rh == 0;
    const unsigned selA = ra ? 0x01000c0cu : 0x0c0c0100u, selB = ra ? 0x03020c0cu : 0x0c0c0302u;
    const unsigned mR = ra ? ~0u : 0u;
    float la[4], lb[4], mx[4];
#pragma unroll
    for (int m = 0; m < 4; ++m) {
      const float a = bsel(mA, bsel(mR, sc[m][1], sc[m][0]), bsel(mR, sp[m][1], sp[m][0]));
      const float b = bsel(mB, bsel(mR, sc[m][3], sc[m][2]), bsel(mR, sp[m][3], sp[m][2]));
      const float f = SC * rq[m];
      la[m] = a * (f * rka[m]); lb[m] = b * (f * rkb[m]);
    }
    la[0] = bsel(nohalo ? mA : ~0u, la[0], -INFINITY); lb[0] = bsel(nohalo ? mB : ~0u, lb[0], -INFINITY);
#pragma unroll
    for (int m = 0; m < 4; ++m) mx[m] = max4(vmax(la[m], lb[m]));
    f32x4 o[4][2];
#pragma unroll
    for (int m = 0; m < 4; ++m) { la[m] = __builtin_amdgcn_exp2f(la[m] - mx[m]); lb[m] = __builtin_amdgcn_exp2f(lb[m] - mx[m]); }
#pragma unroll
    for (int m = 0; m < 4; ++m) mx[m] = __builtin_amdgcn_rcpf(sum4(la[m] + lb[m]));
#pragma unroll
    for (int m = 0; m < 4; ++m) {
      const unsigned w = pk2(la[m] * (mx[m] * rka[m]), lb[m] * (mx[m] * rkb[m]));
      const unsigned wa = __builtin_amdgcn_perm(0u, w, selA), wb = __builtin_amdgcn_perm(0u, w, selB);
      const bf16x8 P = __builtin_bit_cast(bf16x8, (u32x4){wa & ~mA, wb & ~mB, wa & mA, wb & mB});
#pragma unroll
      for (int db = 0; db < 2; ++db) o[m][db] = mfma(cat8(V[m][db], V[m + 1][db]), P, zero4);
    }
#pragma unroll
    for (int p = 0; p < 2; ++p)
#pragma unroll
      for (int db = 0; db < 2; ++db)
        st16_dev(srdW, oaddr + db * 4096 + p * 512, chunk16(pk4(o[2 * p][db]), pk4(o[2 * p + 1][db])));
    publish(F_O + (pm1 * 16 + pn1) * 128);
  }
  auto small_gemm = [&](auto NKC, u32x2 (&res)[2][4]) AI {
    using namespace gs;
    LANES;
    unsigned ab[2], bb[4];
#pragma unroll
    for (int kk = 0; kk < 2; ++kk) ab[kk] = (kk * 4 + g) * 2048 + (wid * 32 + i16) * 16;
#pragma unroll
    for (int j = 0; j < 4; ++j) bb[j] = AB + b128_base(lane, j);
    f32x4 acc[2][4];
#pragma unroll
    for (int m = 0; m < 2; ++m)
#pragma unroll
      for (int j = 0; j < 4; ++j) acc[m][j] = unpk4(*(LAS u32x2*)(lds + L_H + img_off(wid * 32 + m * 16 + i16, j * 2 + (g >> 1)) + (g & 1) * 8));
    auto load = [&](Fr& f, unsigned so, unsigned) {
#pragma unroll
      for (int kk = 0; kk < 2; ++kk) {
#pragma unroll
        for (int m = 0; m < 2; ++m) f.a[kk][m] = lds_a(lds + so + ab[kk] + m * 256);
#pragma unroll
        for (int j = 0; j < 4; ++j)
#pragma unroll
          for (int hh = 0; hh < 2; ++hh) f.b[kk][j][hh] = lds_tr(lds + so + bb[j] + (kk * 32 + hh * 8) * 128);
      }
    };
    auto mma = [&](Fr& f) {
#pragma unroll
      for (int kk = 0; kk < 2; ++kk)
#pragma unroll
        for (int m = 0; m < 2; ++m)
#pragma unroll
          for (int j = 0; j < 4; ++j) acc[m][j] = mfma(cat8(f.b[kk][j][0], f.b[kk][j][1]), f.a[kk][m], acc[m][j]);
    };
    Fr f0;
    unsigned sa = 0, sb = 0;
    bar();
    load(f0, 0u, 0u);
    deep_segment<decltype(NKC)::value, NS, ST, ST, 20, 16>(f0, sa, sb, load, mma);
#pragma unroll
    for (int m = 0; m < 2; ++m)
#pragma unroll
      for (int j = 0; j < 4; ++j) res[m][j] = pk4(acc[m][j]);
  };
  {
    u32x2 h[2][4];
    small_gemm(IC<16>{}, h);
    LANES;
#pragma unroll
    for (int j = 0; j < 4; ++j)
      st16_dev(srdW, WS_H + (pm2 * 16 + pn2) * 16384 + (j * 2 + (g >> 1)) * 2048 + (wid * 32 + (g & 1) * 16 + i16) * 16, chunk16(h[0][j], h[1][j]));
    const unsigned o = diag_lane ? WS_S2R + (pm2 * 16 + pn2) * 1024 + (wid * 32 + i16) * 8 : 0xffff0000u;
#pragma unroll
    for (int m = 0; m < 2; ++m) st8_dev(srdW, o + m * 128, __builtin_bit_cast(u32x2, rowstat(cat8(h[m][0], h[m][1]), cat8(h[m][2], h[m][3]))));
#pragma unroll
    for (int m = 0; m < 2; ++m)
#pragma unroll
      for (int j = 0; j < 4; ++j) *(LAS u32x2*)(lds + L_H + img_off(wid * 32 + m * 16 + i16, j * 2 + (g >> 1)) + (g & 1) * 8) = h[m][j];
    wait_lgkm0();
    publish(F_H + (pm2 * 16 + pn2) * 128);
  }
  {
    using namespace gf;
    LANES;
    unsigned ab[2];
#pragma unroll
    for (int kk = 0; kk < 2; ++kk) ab[kk] = (kk * 4 + g) * 2048 + (wid * 32 + i16) * 16;
    const unsigned bb = B0 + lane * 16;
    f32x4 acc[2][7];
    LAS char *pa0 = lds + ab[0], *pa1 = lds + ab[1], *pb = lds + bb;
    unsigned sa = 0, sb = 0;
    auto rd = [&](Fr& f, int r, int ms) AI {
      const int kk = r / 9, q = r % 9;
      if (q < 2) f.a[kk][q] = lds_a((kk ? pa1 : pa0) + q * ms);
      else f.b[kk][q - 2] = lds_a(pb + (kk * 7 + q - 2) * 1024);
    };
    auto advance = [&]() AI {
      sa += AB; sb += SEG; if (sa == NS * AB) { sa = 0; sb = 0; }
      pa0 = lds + (sa + ab[0]); pa1 = lds + (sa + ab[1]); pb = lds + (sb + bb);
    };
    auto tile = [&](Fr& c, Fr& n, int ms, auto hook) AI {
      cbar();
#pragma unroll
      for (int i = 0; i < 28; ++i) {
        const int kk = i / 14, m = (i % 14) / 7, j = i % 7;
        asm volatile("v_mfma_f32_16x16x32_bf16 %0, %1, %2, %0" : "+v"(acc[m][j]) : "v"(c.b[kk][j]), "v"(c.a[kk][m]));
        if (i < 18) { rd(n, i, ms); __builtin_amdgcn_sched_barrier(0); }
        hook(i);
        if (i == 24) { sa += AB; sb += SEG; if (sa == NS * AB) { sa = 0; sb = 0; } spin(sa); spin(sb); }
        if (i == 25) { pa0 = lds + (sa + ab[0]); pin(pa0); }
        if (i == 26) { pa1 = lds + (sa + ab[1]); pin(pa1); }
        if (i == 27) { pb = lds + (sb + bb); pin(pb); }
        __builtin_amdgcn_sched_barrier(0);
      }
    };
    auto nohook = [](int) AI {};
    Fr f0;
    auto settle = [&]() AI {
      asm volatile("s_nop 7\n\ts_nop 1" ::: "memory");
      __builtin_amdgcn_sched_barrier(0);
    };
    auto segment = [&](int n, int ms) AI {
      Fr f1;
#pragma unroll 1
      for (int t = 0; t < n; t += 2) { tile(f0, f1, ms, nohook); tile(f1, f0, ms, nohook); }
    };
    u32x2 zz[2][7];
    float mu[2], c1[2], c2[2], ir[2];
    auto rowconst = [&]() AI {
#pragma unroll
      for (int m = 0; m < 2; ++m) {
        mu[m] = -*(LAS float*)(lds + L_MU + (wid * 32 + m * 16 + i16) * 4);
        const float rs = *(LAS float*)(lds + L_RS + (wid * 32 + m * 16 + i16) * 4);
        c1[m] = -2.302208198f * rs; c2[m] = -0.1029432397f * rs * rs * rs; ir[m] = __builtin_amdgcn_rcpf(rs);
      }
    };
    auto act = [&]() AI {
#pragma unroll
      for (int j0 = 0; j0 < 7; j0 += 2) {
        f32x4 e[4], cs[2];
        const int n = j0 < 6 ? 4 : 2;
        cs[0] = *(LAS f32x4*)(lds + L_CS + (112 + j0 * 16 + 4 * g) * 4);
        cs[1] = *(LAS f32x4*)(lds + L_CS + (112 + (j0 < 6 ? j0 + 1 : j0) * 16 + 4 * g) * 4);
#pragma unroll
        for (int u = 0; u < 4; ++u) if (u < n) acc[u & 1][j0 + (u >> 1)] += mu[u & 1] * cs[u >> 1];
#pragma unroll
        for (int u = 0; u < 4; ++u) if (u < n) e[u] = acc[u & 1][j0 + (u >> 1)] * acc[u & 1][j0 + (u >> 1)];
#pragma unroll
        for (int u = 0; u < 4; ++u) if (u < n) e[u] = e[u] * c2[u & 1] + c1[u & 1];
#pragma unroll
        for (int u = 0; u < 4; ++u) if (u < n) e[u] = e[u] * acc[u & 1][j0 + (u >> 1)];
#pragma unroll
        for (int u = 0; u < 4; ++u) if (u < n)
#pragma unroll
          for (int i = 0; i < 4; ++i) e[u][i] = __builtin_amdgcn_exp2f(e[u][i]);
#pragma unroll
        for (int u = 0; u < 4; ++u) if (u < n) e[u] = e[u] * ir[u & 1] + ir[u & 1];
#pragma unroll
        for (int u = 0; u < 4; ++u) if (u < n)
#pragma unroll
          for (int i = 0; i < 4; ++i) e[u][i] = __builtin_amdgcn_rcpf(e[u][i]);
#pragma unroll
        for (int u = 0; u < 4; ++u) if (u < n) { zz[u & 1][j0 + (u >> 1)] = pk4(acc[u & 1][j0 + (u >> 1)] * e[u]); pin(zz[u & 1][j0 + (u >> 1)]); }
        __builtin_amdgcn_sched_barrier(0);
      }
    };
#pragma unroll
    for (int m = 0; m < 2; ++m)
#pragma unroll
      for (int j = 0; j < 7; ++j) acc[m][j] = zero4;
    bar();
#pragma unroll
    for (int r = 0; r < 18; ++r) rd(f0, r, 256);
    advance();
    segment(16, 256);
    settle();
    rowconst();
    act();
    {
      f32x2 st[2];
#pragma unroll
      for (int m = 0; m < 2; ++m)
        st[m] = rowstat(cat8(zz[m][0], zz[m][1]), cat8(zz[m][2], zz[m][3])) + rowstat(cat8(zz[m][4], zz[m][5]), cat8(zz[m][6], (u32x2){0u, 0u}));
      const unsigned o = diag_lane ? WS_S3 + (pm1 * 16 + pn1) * 1024 + (wid * 32 + i16) * 8 : 0xffff0000u;
#pragma unroll
      for (int m = 0; m < 2; ++m) st8_dev(srdW, o + m * 128, (u32x2){__float_as_uint(st[m][0]), min(__float_as_uint(st[m][1]), ~1u)});
    }
#pragma unroll
    for (int j = 0; j < 7; ++j) {
      const f32x4 cs = *(LAS f32x4*)(lds + L_CS + (j * 16 + 4 * g) * 4);
      acc[0][j] = mu[0] * cs; acc[1][j] = mu[1] * cs;
    }
    asm volatile("s_nop 1" ::: "memory");
    Fr f1;
    segment(6, 256);
    settle();
    {
      const unsigned one = diag_lane ? ((lane & 1) ? 0x3f800000u : 0x00003f80u) : 0u;
      const u32x2 e = {(lane & 2) ? 0u : one, (lane & 2) ? one : 0u}, z = {0u, 0u};
      const bf16x8 elo = cat8(e, z), ehi = cat8(z, e);
      f32x4 r3[2], m3[2];
#pragma unroll
      for (int m = 0; m < 2; ++m) {
        r3[m] = *(LAS f32x4*)(lds + L_R3 + (wid * 32 + m * 16 + 4 * g) * 4);
        m3[m] = *(LAS f32x4*)(lds + L_M3 + (wid * 32 + m * 16 + 4 * g) * 4);
      }
      f32x4 t[2][7];
#pragma unroll
      for (int j = 0; j < 7; ++j)
#pragma unroll
        for (int m = 0; m < 2; ++m) t[m][j] = mfma(cat8(zz[m][j & ~1], zz[m][j < 6 ? j | 1 : 6]), (j & 1) ? ehi : elo, zero4);
#pragma unroll
      for (int m = 0; m < 2; ++m) m3[m] = -r3[m] * m3[m];
      const unsigned zo = WS_Z2 + (pn1 * 32 + pm1 * 2) * SEG + wid * 7168 + (((g & 1) * 2 + (g >> 1)) * 16 + i16) * 16;
#pragma unroll
      for (int j = 0; j < 7; ++j) st16_dev(srdW, zo + j * 1024, chunk16(pk4(t[0][j] * r3[0] + m3[0]), pk4(t[1][j] * r3[1] + m3[1])));
    }
#pragma unroll 1
    for (int t = 22; t < 32; t += 2) {
      if (t == 26) wait_vm<0>();
      tile(f0, f1, 256, nohook); tile(f1, f0, 256, nohook);
    }
    settle();
    rowconst();
    u32x32 zv;
#pragma unroll
    for (int m = 0; m < 2; ++m) {
      const float b = (float)*(LAS __bf16*)(lds + L_B + (wid * 32 + m * 16 + i16) * 2);
#pragma unroll
      for (int j = 0; j < 7; ++j) { const u32x2 p = pk4(acc[m][j]); zv[4 * j + m] = p[0]; zv[4 * j + 2 + m] = p[1]; acc[m][j] = zero4 + b; }
    }
    bar();
    ab[0] = a_base(lane, 0) + wid * 32 * 128; ab[1] = a_base(lane, 1) + wid * 32 * 128;
    pa0 = lds + ab[0]; pa1 = lds + ab[1]; pb = lds + bb; sa = 0; sb = 0;
#pragma unroll
    for (int r = 0; r < 18; ++r) rd(f0, r, 2048);
    advance();
    {
      unsigned w = 0;
      float u0, u1, e0, e1;
      auto gel = [&](int m, int i) AI {
        if (i == 0) asm volatile("v_lshlrev_b32 %0, 16, %1" : "=v"(u0) : "v"(w));
        if (i == 1) asm volatile("v_and_b32 %0, 0xffff0000, %1" : "=v"(u1) : "v"(w));
        if (i == 2) asm volatile("v_mul_f32 %0, %1, %1" : "=v"(e0) : "v"(u0));
        if (i == 3) asm volatile("v_mul_f32 %0, %1, %1" : "=v"(e1) : "v"(u1));
        if (i == 4) asm volatile("v_fma_f32 %0, %0, %1, %2" : "+v"(e0) : "v"(c2[m]), "v"(c1[m]));
        if (i == 5) asm volatile("v_fma_f32 %0, %0, %1, %2" : "+v"(e1) : "v"(c2[m]), "v"(c1[m]));
        if (i == 6) asm volatile("v_mul_f32 %0, %0, %1" : "+v"(e0) : "v"(u0));
        if (i == 7) asm volatile("v_mul_f32 %0, %0, %1" : "+v"(e1) : "v"(u1));
        if (i == 8) asm volatile("v_exp_f32 %0, %0" : "+v"(e0));
        if (i == 9) asm volatile("v_exp_f32 %0, %0" : "+v"(e1));
        if (i == 10) asm volatile("v_fma_f32 %0, %0, %1, %1" : "+v"(e0) : "v"(ir[m]));
        if (i == 11) asm volatile("v_fma_f32 %0, %0, %1, %1" : "+v"(e1) : "v"(ir[m]));
        if (i == 12) asm volatile("v_rcp_f32 %0, %0" : "+v"(e0));
        if (i == 13) asm volatile("v_rcp_f32 %0, %0" : "+v"(e1));
        if (i == 14) asm volatile("v_mul_f32 %0, %0, %1" : "+v"(e0) : "v"(u0));
        if (i == 15) asm volatile("v_mul_f32 %0, %0, %1" : "+v"(e1) : "v"(u1));
        if (i == 16) asm volatile("v_cvt_pk_bf16_f32 %0, %1, %2" : "=v"(w) : "v"(e0), "v"(e1));
      };
      w = zv[0];
#pragma unroll 1
      for (int it = 0; it < 28; it += 2) {
        tile(f0, f1, 2048, [&](int i) AI { gel(0, i); if (i == 18) { zv[it] = w; pin(zv); } if (i == 20) { w = zv[it + 1]; pin(w); } });
        tile(f1, f0, 2048, [&](int i) AI { gel(1, i); if (i == 18) { zv[it + 1] = w; pin(zv); } if (i == 20) { w = zv[it + 2]; pin(w); } });
      }
    }
    segment(4, 2048);
    settle();
#pragma unroll
    for (int j = 0; j < 7; ++j)
      st16_dev(srdW, WS_Y + (pm1 * 16 + pn1) * 28672 + (j * 2 + (g >> 1)) * 2048 + (wid * 32 + (g & 1) * 16 + i16) * 16,
               chunk16(pk4(unpk4((u32x2){zv[4 * j], zv[4 * j + 2]}) * acc[0][j]), pk4(unpk4((u32x2){zv[4 * j + 1], zv[4 * j + 3]}) * acc[1][j])));
    publish(F_Y + (pm1 * 16 + pn1) * 128);
  }
  {
    u32x2 r[2][4];
    small_gemm(IC<28>{}, r);
    LANES;
    const i32x4 srdOut = make_srd(out, SEQ * DM * 2);
    const unsigned up = i16 < 8 ? 0u : ~0u;
    const unsigned oa = (pm2 * 128 + wid * 32 + (i16 & 7)) * (DM * 2) + pn2 * 128 + ((i16 >> 3) * 4 + (g & 1) * 2 + (g >> 1)) * 16;
#pragma unroll
    for (int m = 0; m < 2; ++m) {
      const u32x4 lo = chunk16(r[m][0], r[m][1]), hi = chunk16(r[m][2], r[m][3]);
      u32x4 ra, rb;
#pragma unroll
      for (int e = 0; e < 4; ++e) {
        const unsigned t = (unsigned)__builtin_amdgcn_update_dpp(0, (int)((up & lo[e]) | (~up & hi[e])), 0x128, 0xf, 0xf, true);
        ra[e] = (up & t) | (~up & lo[e]);
        rb[e] = (up & hi[e]) | (~up & t);
      }
      st16_dev(srdOut, oa + m * 16 * (DM * 2), ra);
      st16_dev(srdOut, oa + (m * 16 + 8) * (DM * 2), rb);
    }
  }
  if (blockIdx.x == 0 && tid == 0) __hip_atomic_store((gu64*)(ws + WS_EPOCH), tag, __ATOMIC_RELAXED, __HIP_MEMORY_SCOPE_AGENT);
}

extern "C" void kernel_launch(void* const* d_in, const int* in_sizes, int n_in, void* d_out, int out_size, void* d_ws, size_t ws_size,
                              hipStream_t stream) {
  (void)hipFuncSetAttribute((const void*)k_mega, hipFuncAttributeMaxDynamicSharedMemorySize, LDS_BYTES);
  k_mega<<<256, 512, LDS_BYTES, stream>>>((const __bf16*)d_in[0], (const __bf16*)d_in[1], (const __bf16*)d_in[2], (const __bf16*)d_in[4],
                                          (const __bf16*)d_in[5], (const __bf16*)d_in[6], (const __bf16*)d_in[7], (char*)d_out, (char*)d_ws);
}
```
